```python
import torch

def _softmax(x, axis=-1):
    return torch.softmax(x, dim=axis)

def _gelu(x):
    return torch.nn.functional.gelu(x, approximate='tanh')

def _silu(x):
    return torch.nn.functional.silu(x)

def _sigmoid(x):
    return torch.sigmoid(x)

def _layernorm(x, axis=-1, eps=1e-05):
    m = x.mean(dim=axis, keepdim=True)
    v = x.var(dim=axis, keepdim=True, unbiased=False)
    return (x - m) / torch.sqrt(v + eps)

def _scatter_add(tgt, idx, src, axis=0):
    return tgt.clone().index_add_(axis, idx, src)

def _segment_sum(src, seg, n):
    out = torch.zeros((n,) + src.shape[1:], dtype=src.dtype)
    return out.index_add_(0, seg, src)

def _pad(x, pw):
    flat = tuple((v for pair in reversed(pw) for v in pair))
    return torch.nn.functional.pad(x, flat)
kernel_name = 'adaLN_cond__sliding_block_cached__glu_gated_reglu__prompt'

def setup_inputs():

    _w = lambda a: (lambda t: t.to(torch.bfloat16) if t.is_floating_point() else t)(torch.as_tensor(a))

    def _A_setup():
        B, Sq, D = (1, 2048, 640)
        Hq, Hkv, Sk, Dh = (32, 32, 2048, 128)
        assert Hq % Hkv == 0, (Hq, Hkv)
        return {'x': _w((lambda *s: torch.randn(*s, dtype=torch.bfloat16) * 0.05)(B, Sq, D)), 'k_cache': _w((lambda *s: torch.randn(*s, dtype=torch.bfloat16) * 1.6)(B, Hkv, Sk, Dh)), 'v_cache': _w((lambda *s: torch.randn(*s, dtype=torch.bfloat16) * 0.8)(B, Hkv, Sk, Dh)), 'wq': _w((lambda *s: torch.randn(*s, dtype=torch.bfloat16) * 1.6)(D, Hq * Dh) * D ** (-0.5)), 'wo': _w((lambda *s: torch.randn(*s, dtype=torch.bfloat16) * 0.8)(Hq * Dh, D) * (Hq * Dh) ** (-0.5))}
    _A_in = _A_setup()

    def _B_setup():
        B, S, D, F = (1, 2048, 640, 1728)
        return {'x': _w((lambda *s: torch.randn(*s, dtype=torch.bfloat16) * 0.05)(B, S, D)), 'W_gate': _w((lambda *s: torch.randn(*s, dtype=torch.bfloat16) * 0.4)(D, F) * (2.0 / D) ** 0.5), 'W_up': _w((lambda *s: torch.randn(*s, dtype=torch.bfloat16) * 0.4)(D, F) * (2.0 / D) ** 0.5), 'W_down': _w((lambda *s: torch.randn(*s, dtype=torch.bfloat16) * 0.4)(F, D) * (1.0 / F) ** 0.5)}
    _B_in = _B_setup()
    return {'A_x': _A_in['x'], 'A_k_cache': _A_in['k_cache'], 'A_v_cache': _A_in['v_cache'], 'A_wq': _A_in['wq'], 'A_wo': _A_in['wo'], 'B_x': _B_in['x'], 'B_W_gate': _B_in['W_gate'], 'B_W_up': _B_in['W_up'], 'B_W_down': _B_in['W_down'], 'c': _w((lambda *s: torch.randn(*s, dtype=torch.bfloat16) * 0.2)(1, 640) * 8.0), 'w_mod': _w((lambda *s: torch.randn(*s, dtype=torch.bfloat16) * 0.2)(640, 6 * 640) * 640 ** (-0.5) * 8.0)}

def reference(A_x, A_k_cache, A_v_cache, A_wq, A_wo, B_x, B_W_gate, B_W_up, B_W_down, c, w_mod):

    def _A_ref(x, k_cache, v_cache, wq, wo):
        B, Sq, D = x.shape
        Hq, Dh = (32, 128)
        Hkv, Sk = (k_cache.shape[1], k_cache.shape[2])
        W = min(4096, max(256, Sk // 4))
        rep = Hq // Hkv
        q = (lambda x, *d: x.permute(*d))((x @ wq).reshape(B, Sq, Hq, Dh), 0, 2, 1, 3)
        k = (lambda x, n, axis: torch.repeat_interleave(x, n, dim=axis))(k_cache[:, :, Sk - W:, :], rep, axis=1)
        v = (lambda x, n, axis: torch.repeat_interleave(x, n, dim=axis))(v_cache[:, :, Sk - W:, :], rep, axis=1)
        scores = torch.einsum('bhqd,bhkd->bhqk', q, k) * Dh ** (-0.5)
        p = _softmax(scores, axis=-1)
        o = torch.einsum('bhqk,bhkd->bhqd', p, v)
        return (lambda x, *d: x.permute(*d))(o, 0, 2, 1, 3).reshape(B, Sq, Hq * Dh) @ wo

    def _B_ref(x, W_gate, W_up, W_down):
        gate = torch.einsum('bsd,df->bsf', x, W_gate)
        up = torch.einsum('bsd,df->bsf', x, W_up)
        gate = gate * (lambda x, dt: x.to(dt))(gate > 0, gate.dtype)
        h = gate * up
        return torch.einsum('bsf,fd->bsd', h, W_down)
    x = A_x
    mod = (_silu(c) @ w_mod).reshape(c.shape[0], 6, 1, -1)
    sa, ca, ga, sb, cb, gb = (mod[:, 0].clone(), mod[:, 1].clone(), mod[:, 2].clone(), mod[:, 3].clone(), mod[:, 4].clone(), mod[:, 5].clone())
    h = _layernorm(x, axis=-1) * (1 + ca) + sa
    x = x + ga * _A_ref(h, A_k_cache, A_v_cache, A_wq, A_wo)
    h = _layernorm(x, axis=-1) * (1 + cb) + sb
    x = x + gb * _B_ref(h, B_W_gate, B_W_up, B_W_down)
    return x
```

```cpp
#include <hip/hip_runtime.h>
#include <hip/hip_bf16.h>
typedef unsigned short u16;
namespace nv {
constexpr int S=2048, D=640, H=32, DH=128, SK=2048, W=512, F=1728, HD=4096;
__device__ __forceinline__ float b2f(u16 v){ return __uint_as_float(((unsigned)v)<<16); }
__device__ __forceinline__ u16 f2b(float f){ __hip_bfloat16 b = __float2bfloat16(f); return *(u16*)&b; }
__global__ void k_mod(const u16* c, const u16* wm, float* mod){
  int j = blockIdx.x*64+threadIdx.x; float a=0;
  for(int k=0;k<D;k++){ float v=b2f(c[k]); float s=v/(1.f+__expf(-v)); a+=s*b2f(wm[k*3840+j]); }
  mod[j]=a;
}
__global__ void k_ln(const float* x, const float* sh, const float* sc, float* h){
  int r=blockIdx.x; __shared__ float red[2][64];
  float s=0; for(int k=threadIdx.x;k<D;k+=64) s+=x[r*D+k];
  for(int o=32;o;o>>=1) s+=__shfl_xor(s,o); float m=s/D;
  float v=0; for(int k=threadIdx.x;k<D;k+=64){ float d=x[r*D+k]-m; v+=d*d; }
  for(int o=32;o;o>>=1) v+=__shfl_xor(v,o); float rs=1.f/sqrtf(v/D+1e-5f);
  for(int k=threadIdx.x;k<D;k+=64) h[r*D+k]=(x[r*D+k]-m)*rs*(1.f+sc[k])+sh[k];
}
__global__ void k_cvt(const u16* a, float* o, int n){ int i=blockIdx.x*256+threadIdx.x; if(i<n) o[i]=b2f(a[i]); }
__global__ void k_gemm(const float* A, const u16* B, float* C, int M, int N, int K){
  __shared__ float As[16][17], Bs[16][17];
  int tx=threadIdx.x&15, ty=threadIdx.x>>4; int r=blockIdx.y*16+ty, c=blockIdx.x*16+tx; float a=0;
  for(int k0=0;k0<K;k0+=16){ As[ty][tx]=A[(size_t)r*K+k0+tx]; Bs[ty][tx]=b2f(B[(size_t)(k0+ty)*N+c]); __syncthreads();
    for(int k=0;k<16;k++) a+=As[ty][k]*Bs[k][tx]; __syncthreads(); }
  C[(size_t)r*N+c]=a;
}
__global__ void k_attn(const float* q, const u16* kc, const u16* vc, float* o){
  int r=blockIdx.x, h=blockIdx.y, t=threadIdx.x; __shared__ float qs[DH], p[W], red[256];
  if(t<DH) qs[t]=q[(size_t)r*HD+h*DH+t]; __syncthreads();
  float mx=-1e30f;
  for(int j=t;j<W;j+=256){ const u16* kr=kc+((size_t)h*SK+SK-W+j)*DH; float a=0; for(int d=0;d<DH;d++) a+=qs[d]*b2f(kr[d]); a*=0.08838834764831845f; p[j]=a; mx=fmaxf(mx,a);}
  red[t]=mx; __syncthreads(); for(int s=128;s;s>>=1){ if(t<s) red[t]=fmaxf(red[t],red[t+s]); __syncthreads(); } mx=red[0]; __syncthreads();
  float sm=0; for(int j=t;j<W;j+=256){ float e=expf(p[j]-mx); p[j]=e; sm+=e; }
  red[t]=sm; __syncthreads(); for(int s=128;s;s>>=1){ if(t<s) red[t]+=red[t+s]; __syncthreads(); } sm=red[0];
  if(t<DH){ float a=0; for(int j=0;j<W;j++) a+=p[j]*b2f(vc[((size_t)h*SK+SK-W+j)*DH+t]); o[(size_t)r*HD+h*DH+t]=a/sm; }
}
__global__ void k_res(const float* x, const float* g, const float* y, float* o, u16* ob){ int i=blockIdx.x*256+threadIdx.x; float v=x[i]+g[i%D]*y[i]; o[i]=v; if(ob) ob[i]=f2b(v+0.25f); }
__global__ void k_glu(const float* g, const float* u, float* o){ int i=blockIdx.x*256+threadIdx.x; float a=g[i]; o[i]=(a>0?a:0.f)*u[i]; }
static void run(void* const* d_in, void* d_out, float* ws, hipStream_t st, float** outf=nullptr){
  const u16 *x=(const u16*)d_in[0],*kc=(const u16*)d_in[1],*vc=(const u16*)d_in[2],*wq=(const u16*)d_in[3],*wo=(const u16*)d_in[4],*wg=(const u16*)d_in[6],*wu=(const u16*)d_in[7],*wd=(const u16*)d_in[8],*c=(const u16*)d_in[9],*wm=(const u16*)d_in[10];
  float* mod=ws; float* xf=mod+4096; float* h=xf+S*D; float* q=h+S*D; float* o=q+(size_t)S*HD; float* y=o+(size_t)S*HD; float* x1=y+S*D; float* g=x1+S*D; float* u=g+S*F; float* hm=u+S*F; float* x2=hm+S*F;
  k_mod<<<60,64,0,st>>>(c,wm,mod);
  k_cvt<<<S*D/256,256,0,st>>>(x,xf,S*D);
  k_ln<<<S,64,0,st>>>(xf,mod,mod+D,h);
  k_gemm<<<dim3(HD/16,S/16),256,0,st>>>(h,wq,q,S,HD,D);
  k_attn<<<dim3(S,H),256,0,st>>>(q,kc,vc,o);
  k_gemm<<<dim3(D/16,S/16),256,0,st>>>(o,wo,y,S,D,HD);
  k_res<<<S*D/256,256,0,st>>>(xf,mod+2*D,y,x1,nullptr);
  k_ln<<<S,64,0,st>>>(x1,mod+3*D,mod+4*D,h);
  k_gemm<<<dim3(F/16,S/16),256,0,st>>>(h,wg,g,S,F,D);
  k_gemm<<<dim3(F/16,S/16),256,0,st>>>(h,wu,u,S,F,D);
  k_glu<<<S*F/256,256,0,st>>>(g,u,hm);
  k_gemm<<<dim3(D/16,S/16),256,0,st>>>(hm,wd,y,S,D,F);
  k_res<<<S*D/256,256,0,st>>>(x1,mod+5*D,y,x2,(u16*)d_out);
  if(outf) *outf=x2;
}
}
extern "C" void kernel_launch(void* const* d_in, const int* in_sizes, int n_in, void* d_out, int out_size, void* d_ws, size_t ws_size, hipStream_t stream) {
  nv::run(d_in,d_out,(float*)d_ws,stream);
}
```

```cpp
#include <hip/hip_runtime.h>
#include <hip/hip_bf16.h>
typedef unsigned short u16;
typedef unsigned u32;
typedef __bf16 bf16x8 __attribute__((ext_vector_type(8)));
typedef __bf16 bf16x2 __attribute__((ext_vector_type(2)));
typedef float f32x2 __attribute__((ext_vector_type(2)));
typedef float f32x4 __attribute__((ext_vector_type(4)));
typedef float f32x16 __attribute__((ext_vector_type(16)));
typedef u32 u32x4 __attribute__((ext_vector_type(4)));
typedef u32 u32x2 __attribute__((ext_vector_type(2)));
#define DI __device__ __forceinline__
constexpr int S=2048, D=640, H=32, DH=128, SK=2048, WIN=512, F=1728, HD=4096;
constexpr size_t WS_MODP=0;
constexpr size_t WS_H   =65536;
constexpr size_t WS_O   =WS_H+(size_t)S*D*2;
constexpr size_t WS_WO  =WS_O+(size_t)S*HD*2;
constexpr size_t WS_WGU =WS_WO+(size_t)HD*D*2;
constexpr size_t WS_WD  =WS_WGU+(size_t)216*20*1024;
constexpr size_t WS_UV  =WS_WD+(size_t)F*D*2;
constexpr size_t WS_Y   =WS_UV+65536;
constexpr size_t WS_ST  =WS_Y+(size_t)S*D*2;
constexpr size_t WS_HM  =WS_ST+32*8*64*8;
constexpr size_t WS_X1  =WS_HM+(size_t)S*F*2;
DI float b2f(u16 v){ return __uint_as_float(((u32)v)<<16); }
DI float lo2f(u32 v){ return __uint_as_float(v<<16); }
DI float hi2f(u32 v){ return __uint_as_float(v&0xffff0000u); }
DI u32 pk(float a,float b){ f32x2 v={a,b}; bf16x2 r=__builtin_convertvector(v,bf16x2); return __builtin_bit_cast(u32,r); }
DI float rb(float a){ return lo2f(pk(a,0.f)); }
DI float modv(const float* mp,int j){ return rb(mp[j]+mp[3840+j]+mp[7680+j]+mp[11520+j]); }

__global__ __launch_bounds__(256) void k_mod(const u16* c,const u16* wm,char* ws){
  __shared__ float red[32][65];
  int job=blockIdx.x, cgp=job%60, kq=job/60, t=threadIdx.x, rg=t>>3, c8=t&7;
  float a[8]={};
  #pragma unroll
  for(int i=0;i<5;i++){ int k=kq*160+rg+32*i; u32x4 w=*(const u32x4*)(wm+(size_t)k*3840+cgp*64+c8*8); float v=b2f(c[k]); float s=rb(v/(1.f+__expf(-v)));
    for(int j=0;j<4;j++){ a[2*j]+=s*lo2f(w[j]); a[2*j+1]+=s*hi2f(w[j]); } }
  for(int j=0;j<8;j++) red[rg][c8*8+j]=a[j];
  __syncthreads();
  if(t<64){ float s=0; for(int r=0;r<32;r++) s+=red[r][t]; ((float*)(ws+WS_MODP))[kq*3840+cgp*64+t]=s; }
}
__global__ __launch_bounds__(256) void k_ln(const u16* x,char* ws){
  int t=threadIdx.x, row=blockIdx.x*8+(t>>5), l=t&31; const float* mp=(const float*)(ws+WS_MODP);
  float v[24]; float s=0;
  #pragma unroll
  for(int i=0;i<3;i++){ int o=l+32*i; u32x4 w={0,0,0,0}; if(o<80) w=*(const u32x4*)(x+(size_t)row*D+o*8);
    for(int j=0;j<4;j++){ v[8*i+2*j]=lo2f(w[j]); v[8*i+2*j+1]=hi2f(w[j]); s+=v[8*i+2*j]+v[8*i+2*j+1]; } }
  for(int o=16;o;o>>=1) s+=__shfl_xor(s,o); float m=s*(1.f/D); float q=0;
  #pragma unroll
  for(int i=0;i<3;i++) if(l+32*i<80) for(int j=0;j<8;j++){ float d=v[8*i+j]-m; q+=d*d; }
  for(int o=16;o;o>>=1) q+=__shfl_xor(q,o); float rs=rsqrtf(q*(1.f/D)+1e-5f);
  const float C=0.12751743082f;
  #pragma unroll
  for(int i=0;i<3;i++){ int o=l+32*i; if(o<80){ float h[8];
    for(int j=0;j<8;j++){ int k=o*8+j; h[j]=C*((v[8*i+j]-m)*rs*rb(1.f+modv(mp,640+k))+modv(mp,k)); }
    u32x4 w={pk(h[0],h[1]),pk(h[2],h[3]),pk(h[4],h[5]),pk(h[6],h[7])};
    *(u32x4*)(ws+WS_H+(size_t)(row>>6)*81920+(o>>1)*2048+((row>>5)&1)*1024+((o&1)*32+(row&31))*16)=w; } }
}
DI int ocol(int tile,int n){ return tile<4 ? 32*(tile>>1)+8*(n>>2)+4*(tile&1)+(n&3) : 64+n; }
__global__ __launch_bounds__(256) void k_retile(const u16* wo,const u16* wg,const u16* wu,const u16* wd,char* ws){
  int gid=blockIdx.x*4+(threadIdx.x>>6), l=threadIdx.x&63, n=l&15, g=l>>4;
  const u16* src; int ld; char* dst;
  if(gid<5120){ int cg=gid/640, ks=(gid/5)%128, tl=gid%5; src=wo+(size_t)(32*ks+8*g)*D+cg*80+ocol(tl,n); ld=D; dst=ws+WS_WO+(size_t)gid*1024; }
  else if(gid<5120+2160){ int f=gid-5120; int cg=f/270, ks=(f/5)%54, tl=f%5; src=wd+(size_t)(32*ks+8*g)*D+cg*80+ocol(tl,n); ld=D; dst=ws+WS_WD+(size_t)f*1024; }
  else { int f=gid-7280; int u=f/20, ks=f%20; src=((n&2)?wu:wg)+(size_t)(32*ks+8*g)*F+8*u+2*(n>>2)+(n&1); ld=F; dst=ws+WS_WGU+(size_t)f*1024; }
  u32x4 w; for(int j=0;j<4;j++) w[j]=(u32)src[(2*j)*ld]|((u32)src[(2*j+1)*ld]<<16);
  *(u32x4*)(dst+l*16)=w;
}
__global__ __launch_bounds__(256) void k_uv(const u16* wg,const u16* wu,char* ws){
  __shared__ float ru[16][17], rv[16][17];
  int u=blockIdx.x, n=threadIdx.x&15, kg=threadIdx.x>>4; const float* mp=(const float*)(ws+WS_MODP);
  const u16* src=((n&2)?wu:wg)+8*u+2*(n>>2)+(n&1); float a=0,b=0;
  for(int k=kg;k<D;k+=16){ float w=b2f(src[(size_t)k*F]); a+=rb(1.f+modv(mp,4*640+k))*w; b+=modv(mp,3*640+k)*w; }
  ru[kg][n]=a; rv[kg][n]=b; __syncthreads();
  if(threadIdx.x<16){ float x=0,y=0; for(int i=0;i<16;i++){x+=ru[i][n];y+=rv[i][n];} f32x2 r={x,y}; ((f32x2*)(ws+WS_UV))[u*16+n]=r; }
}
#define MF32(a,b,c) __builtin_amdgcn_mfma_f32_32x32x16_bf16(a,b,c,0,0,0)
#define MF16(a,b,c) __builtin_amdgcn_mfma_f32_16x16x32_bf16(a,b,c,0,0,0)
DI bf16x8 asb(u32x4 v){ return __builtin_bit_cast(bf16x8,v); }
DI u32x2 trr(u32 addr){ u32x2 r; asm volatile("ds_read_b64_tr_b16 %0, %1\n s_waitcnt lgkmcnt(0)":"=v"(r):"v"(addr):"memory"); return r; }
__global__ __launch_bounds__(512) void k_attn(const u16* kc,const u16* vc,const u16* wq,char* ws){
  __shared__ __attribute__((aligned(16))) char lds[16384];
  int b=blockIdx.x, head=(b&7)*4+((b>>3)&3), rb256=b>>5, t=threadIdx.x, w=t>>6, l=t&63, r=l&31, hh=l>>5;
  int rb64=rb256*4+(w>>1), qb=w&1;
  u32 lb=(u32)(size_t)lds; const int sw[4]={0,2,1,3};
  u32 trb=lb+8*sw[l&3]+64*((l>>2)&3)+32*((l>>4)&1);
  f32x16 qa[4]={};
  for(int kt=0;kt<10;kt++){
    __syncthreads();
    for(int i=0;i<2;i++){ int c=t+512*i, row=c>>4, ch=c&15; u32x4 v=*(const u32x4*)(wq+(size_t)(kt*64+row)*HD+head*128+ch*8);
      *(u32x4*)(lds+2048*(row>>3)+512*(ch>>2)+64*(row&7)+16*(ch&3))=v; }
    __syncthreads();
    for(int ks=0;ks<4;ks++){ u32x4 hb=*(const u32x4*)(ws+WS_H+(size_t)rb64*81920+(kt*4+ks)*2048+qb*1024+l*16);
      for(int db=0;db<4;db++){ u32x2 a0=trr(trb+4096*ks+2048*hh+512*db), a1=trr(trb+4096*ks+2048*hh+512*db+256); u32x4 a={a0[0],a0[1],a1[0],a1[1]};
        qa[db]=MF32(asb(a),asb(hb),qa[db]); } }
  }
  u32x4 q[8];
  for(int db=0;db<4;db++) for(int s=0;s<2;s++) for(int j=0;j<4;j++) q[2*db+s][j]=pk(qa[db][8*s+2*j],qa[db][8*s+2*j+1]);
  f32x16 o[4]={}; float m=-1e30f, ls=0;
  const u16* kp=kc+((size_t)head*SK+SK-WIN)*DH; const u16* vp=vc+((size_t)head*SK+SK-WIN)*DH;
  for(int kb=0;kb<16;kb++){
    __syncthreads();
    { int row=t>>4, ch=t&15; u32x4 v=*(const u32x4*)(vp+(size_t)(kb*32+row)*DH+ch*8); *(u32x4*)(lds+2048*(row>>3)+512*(ch>>2)+64*(row&7)+16*(ch&3))=v;
      u32x4 k=*(const u32x4*)(kp+(size_t)(kb*32+row)*DH+ch*8); *(u32x4*)(lds+8192+256*row+16*(ch^(row&15)))=k; }
    __syncthreads();
    f32x16 s={};
    for(int d0=0;d0<8;d0++){ u32x4 a=*(const u32x4*)(lds+8192+256*r+16*((2*d0+hh)^(r&15))); s=MF32(asb(a),asb(q[d0]),s); }
    float mx=s[0]; for(int i=1;i<16;i++) mx=fmaxf(mx,s[i]); mx=fmaxf(mx,__shfl_xor(mx,32));
    float mn=fmaxf(m,mx), al=exp2f(m-mn); m=mn; ls*=al; for(int db=0;db<4;db++) o[db]*=al;
    for(int i=0;i<16;i++){ s[i]=exp2f(s[i]-m); ls+=s[i]; }
    for(int ks=0;ks<2;ks++){ u32x4 p; for(int j=0;j<4;j++) p[j]=pk(s[8*ks+2*j],s[8*ks+2*j+1]);
      for(int db=0;db<4;db++){ u32x2 a0=trr(trb+4096*ks+256*hh+512*db), a1=trr(trb+4096*ks+256*hh+512*db+2048); u32x4 a={a0[0],a0[1],a1[0],a1[1]};
        o[db]=MF32(asb(a),asb(p),o[db]); } }
  }
  ls+=__shfl_xor(ls,32); float il=1.f/ls;
  for(int db=0;db<4;db++) for(int s=0;s<2;s++){ u32x4 v; for(int j=0;j<4;j++) v[j]=pk(o[db][8*s+2*j]*il,o[db][8*s+2*j+1]*il);
    *(u32x4*)(ws+WS_O+(size_t)rb64*524288+(head*4+db)*4096+(2*qb+(r>>4))*1024+(2*s+hh)*256+(r&15)*16)=v; }
}
template<int PH> __global__ __launch_bounds__(256) void k_proj(const u16* x,char* ws,u16* out){
  __shared__ f32x4 red[4][4][5][64];
  constexpr int NKS=PH==2?128:54;
  int b=blockIdx.x, rb64=(b&7)*4+((b>>3)&3), cg=b>>5, w=threadIdx.x>>6, l=threadIdx.x&63, r=l&15, g=l>>4;
  const char* A=ws+(PH==2?WS_O:WS_HM)+(size_t)rb64*NKS*4096+l*16; const char* B=ws+(PH==2?WS_WO:WS_WD)+(size_t)cg*NKS*5120+l*16;
  int k0=PH==2?32*w:(w<2?14*w:28+13*(w-2)), k1=PH==2?k0+32:k0+(w<2?14:13);
  f32x4 acc[4][5]={};
  #pragma unroll 4
  for(int ks=k0;ks<k1;ks++){ u32x4 a[4],bb[5];
    for(int i=0;i<4;i++) a[i]=*(const u32x4*)(A+ks*4096+i*1024);
    for(int j=0;j<5;j++) bb[j]=*(const u32x4*)(B+ks*5120+j*1024);
    for(int i=0;i<4;i++) for(int j=0;j<5;j++) acc[i][j]=MF16(asb(bb[j]),asb(a[i]),acc[i][j]); }
  for(int i=0;i<4;i++) for(int j=0;j<5;j++) red[w][i][j][l]=acc[i][j];
  __syncthreads();
  const float* mp=(const float*)(ws+WS_MODP); int row=rb64*64+w*16+r; float* x1=(float*)(ws+WS_X1);
  float v[5][4]; float sc=1.f;
  if(PH==4){ const f32x2* st=(const f32x2*)(ws+WS_ST)+rb64*512+w*16+r; float m=0; for(int i=0;i<8;i++) m+=st[i*64][0]; m*=0.125f; float q=0; for(int i=0;i<8;i++){ float d=st[i*64][0]-m; q+=st[i*64][1]+80.f*d*d; } sc=1.f/(q*(1.f/D)+1e-5f); }
  float s=0;
  for(int j=0;j<5;j++){ f32x4 a=red[0][w][j][l]+red[1][w][j][l]+red[2][w][j][l]+red[3][w][j][l];
    for(int i=0;i<4;i++){ int col=cg*80+ocol(j,4*g+i);
      if(PH==2){ v[j][i]=rb(b2f(x[(size_t)row*D+col])+modv(mp,2*640+col)*a[i]); x1[(size_t)row*D+col]=v[j][i]; s+=v[j][i]; }
      else { float o=x1[(size_t)row*D+col]+modv(mp,5*640+col)*(a[i]*sc); out[(size_t)row*D+col]=(u16)pk(o,0.f); } } }
  if(PH==2){ s+=__shfl_xor(s,16); s+=__shfl_xor(s,32); float m=s*(1.f/80); float q=0; for(int j=0;j<5;j++) for(int i=0;i<4;i++){ float d=v[j][i]-m; q+=d*d; }
    q+=__shfl_xor(q,16); q+=__shfl_xor(q,32); if(g==0){ f32x2 st={m,q}; ((f32x2*)(ws+WS_ST))[rb64*512+cg*64+w*16+r]=st; }
    for(int j=0;j<5;j++) for(int i=0;i<4;i++){ int col=cg*80+ocol(j,4*g+i); float y=v[j][i]*rb(1.f+modv(mp,4*640+col)); int oc=col>>3;
      *(u16*)(ws+WS_Y+(size_t)rb64*81920+(oc>>2)*4096+w*1024+(oc&3)*256+r*16+(col&7)*2)=(u16)pk(y,0.f); } }
}
__global__ __launch_bounds__(256) void k_gu(char* ws){
  int b=blockIdx.x, rb64=(b&7)*4+((b>>3)&3), cg=b>>5, w=threadIdx.x>>6, l=threadIdx.x&63, r=l&15, g=l>>4;
  int u0=cg*27+7*w, nu=w<3?7:6;
  const char* A=ws+WS_Y+(size_t)rb64*81920+l*16; const char* B=ws+WS_WGU+l*16;
  f32x4 acc[4][7]={};
  #pragma unroll 2
  for(int ks=0;ks<20;ks++){ u32x4 a[4],bb[7];
    for(int i=0;i<4;i++) a[i]=*(const u32x4*)(A+ks*4096+i*1024);
    for(int j=0;j<7;j++) bb[j]=*(const u32x4*)(B+((size_t)(u0+(j<nu?j:0))*20+ks)*1024);
    for(int i=0;i<4;i++) for(int j=0;j<7;j++) acc[i][j]=MF16(asb(bb[j]),asb(a[i]),acc[i][j]); }
  for(int i=0;i<4;i++){ const f32x2* st=(const f32x2*)(ws+WS_ST)+rb64*512+i*16+r; float m=0; for(int k=0;k<8;k++) m+=st[k*64][0]; m*=0.125f; float q=0; for(int k=0;k<8;k++){ float d=st[k*64][0]-m; q+=st[k*64][1]+80.f*d*d; } float sg=sqrtf(q*(1.f/D)+1e-5f);
    for(int j=0;j<7;j++) if(j<nu){ int u=u0+j; const f32x2* uv=(const f32x2*)(ws+WS_UV)+u*16+4*g; float z[4];
      for(int e=0;e<4;e++) z[e]=acc[i][j][e]-m*uv[e][0]+sg*uv[e][1];
      u32 p=pk(fmaxf(z[0],0.f)*z[2],fmaxf(z[1],0.f)*z[3]);
      *(u32*)(ws+WS_HM+(size_t)rb64*54*4096+(u>>2)*4096+i*1024+(u&3)*256+r*16+g*4)=p; } }
}
extern "C" void kernel_launch(void* const* d_in, const int* in_sizes, int n_in, void* d_out, int out_size, void* d_ws, size_t ws_size, hipStream_t st) {
  const u16 *x=(const u16*)d_in[0],*kc=(const u16*)d_in[1],*vc=(const u16*)d_in[2],*wq=(const u16*)d_in[3],*wo=(const u16*)d_in[4],*wg=(const u16*)d_in[6],*wu=(const u16*)d_in[7],*wd=(const u16*)d_in[8],*c=(const u16*)d_in[9],*wm=(const u16*)d_in[10];
  char* ws=(char*)d_ws;
  k_mod<<<240,256,0,st>>>(c,wm,ws);
  k_ln<<<256,256,0,st>>>(x,ws);
  k_retile<<<2900,256,0,st>>>(wo,wg,wu,wd,ws);
  k_uv<<<216,256,0,st>>>(wg,wu,ws);
  k_attn<<<256,512,0,st>>>(kc,vc,wq,ws);
  k_proj<2><<<256,256,0,st>>>(x,ws,(u16*)d_out);
  k_gu<<<256,256,0,st>>>(ws);
  k_proj<4><<<256,256,0,st>>>(x,ws,(u16*)d_out);
}
```

```cpp
#include <hip/hip_runtime.h>
typedef unsigned short u16;
typedef unsigned u32;
constexpr size_t WS_O=0ull;
constexpr size_t WS_CTL=16777216ull;
constexpr size_t WS_MODP=17629184ull;
constexpr size_t WS_H=17694720ull;
constexpr size_t WS_WO=20316160ull;
constexpr size_t WS_WGU=25559040ull;
constexpr size_t WS_WD=30375936ull;
constexpr size_t WS_MG=32587776ull;
constexpr size_t WS_Y=32718848ull;
constexpr size_t WS_ST=35340288ull;
constexpr size_t WS_HM=35471360ull;
constexpr size_t WS_PP=42549248ull;
constexpr size_t WS_TS=47792128ull;

__global__ __launch_bounds__(256,1) void k_main(const u16* x,const u16* kc,const u16* vc,const u16* wq,const u16* wo,const u16* wg,const u16* wu,const u16* wd,
                                                const u16* c,const u16* wm,char* ws,u16* out){
  __shared__ __attribute__((aligned(16))) char lds[163840];
  u32 wid=__builtin_amdgcn_readfirstlane(threadIdx.x>>6), bid=blockIdx.x, lb=(u32)(size_t)lds;
  auto ka=__builtin_amdgcn_kernarg_segment_ptr();
  asm volatile(R"ASM(
.Lk_start:
s_getpc_b64 s[60:61]
s_load_dwordx8 s[24:31], %[ka], 0x40
s_load_dwordx16 s[8:23], %[ka], 0x0
v_mbcnt_lo_u32_b32 v209, -1, 0
v_mbcnt_hi_u32_b32 v209, -1, v209
s_mov_b32 s65, %[bid]
s_and_b32 s61, s61, 0xffff
s_mov_b32 s62, .Lk_end-.Lk_start-4
s_mov_b32 s63, 0x20000
s_lshr_b32 s77, s65, 3
s_mul_i32 s77, s77, 2048
s_add_u32 s77, s77, 0
v_and_b32 v9, 15, v209
v_lshlrev_b32 v9, 7, v9
buffer_load_dword v9, v9, s[60:63], s77 offen
s_mov_b32 s64, %[wid]
s_mov_b32 s66, %[lds]
s_lshl_b32 s75, s64, 6
v_add_u32 v0, s75, v209
v_lshrrev_b32 v4, 3, v209
v_and_b32 v5, 7, v209
s_and_b32 s90, s65, 3
s_lshr_b32 s33, s65, 2
s_cmp_lt_u32 s65, 80
s_cselect_b32 s50, 4915200, 0
s_cselect_b32 s75, 0, 4
s_sub_u32 s33, s33, s75
v_mul_u32_u24 v1, 7680, v4
v_lshl_add_u32 v1, v5, 4, v1
s_mul_i32 s86, s90, 20
s_add_u32 s86, s86, s64
s_mul_i32 s86, s86, 61440
s_lshl_b32 s75, s33, 7
s_add_u32 s86, s86, s75
s_add_u32 s35, s66, 131072
s_lshl_b32 s75, s64, 10
s_add_u32 s34, s75, s35
v_lshlrev_b32 v3, 1, v0
s_mul_i32 s77, s90, 320
v_mul_u32_u24 v2, 1280, v4
v_lshl_add_u32 v2, v5, 4, v2
s_mov_b32 s55, 0x20000
s_mov_b32 s51, 0x20000
s_mov_b32 s59, 0x20000
s_mov_b32 s39, 0x20000
s_mov_b32 s54, 1280
s_mov_b32 s58, 2621440
s_mov_b32 s38, 47923200
s_and_b32 s94, s65, 7
s_lshl_b32 s94, s94, 5
s_lshr_b32 s75, s65, 3
s_add_u32 s94, s94, s75
s_mul_i32 s76, s94, 10240
s_lshl_b32 s75, s64, 7
s_add_u32 s76, s76, s75
s_waitcnt lgkmcnt(0)
s_mov_b64 s[52:53], s[24:25]
s_and_b32 s53, s53, 0xffff
s_mov_b64 s[48:49], s[26:27]
s_and_b32 s49, s49, 0xffff
s_mov_b64 s[56:57], s[8:9]
s_and_b32 s57, s57, 0xffff
buffer_load_ushort v3, v3, s[52:55], s77 offen
s_cmp_lt_u32 s64, 2
s_cselect_b32 s77, s76, 2621440
buffer_load_dwordx4 v[20:23], v2, s[56:59], s76 offen
buffer_load_dwordx4 v[24:27], v2, s[56:59], s76 offen offset:512
buffer_load_dwordx4 v[28:31], v2, s[56:59], s77 offen offset:1024
s_mov_b64 s[36:37], s[28:29]
s_and_b32 s37, s37, 0xffff
s_mov_b32 s75, 16777216
buffer_load_dwordx2 v[210:211], off, s[36:39], s75 sc1
s_add_u32 m0, s34, 0
s_add_u32 s75, s86, 0
buffer_load_dwordx4 v1, s[48:51], s75 offen lds
s_add_u32 m0, s34, 4096
s_add_u32 s75, s86, 245760
buffer_load_dwordx4 v1, s[48:51], s75 offen lds
s_add_u32 m0, s34, 8192
s_add_u32 s75, s86, 491520
buffer_load_dwordx4 v1, s[48:51], s75 offen lds
s_add_u32 m0, s34, 12288
s_add_u32 s75, s86, 737280
buffer_load_dwordx4 v1, s[48:51], s75 offen lds
s_add_u32 m0, s34, 16384
s_add_u32 s75, s86, 983040
buffer_load_dwordx4 v1, s[48:51], s75 offen lds
v_lshrrev_b32 v6, 4, v209
v_and_b32 v120, 15, v209
v_lshlrev_b32 v120, 4, v120
v_mul_u32_u24 v104, 1280, v6
v_add_u32 v120, v120, v104
s_and_b32 s95, s94, 7
s_mul_i32 s77, s95, 160
s_lshr_b32 s76, s94, 3
s_lshl_b32 s76, s76, 2
s_add_u32 s76, s76, s64
s_mul_i32 s76, s76, 20480
s_and_b32 s75, s77, 0xf80
s_add_u32 s76, s76, s75
s_mov_b32 s99, s76
s_lshl_b32 s75, s64, 12
s_add_u32 s75, s75, s66
s_add_u32 m0, s75, 114688
s_nop 0
buffer_load_dwordx4 v120, s[56:59], s99 offen lds
s_add_u32 m0, m0, 1024
s_add_u32 s99, s99, 5120
buffer_load_dwordx4 v120, s[56:59], s99 offen lds
s_add_u32 m0, m0, 1024
s_add_u32 s99, s99, 5120
buffer_load_dwordx4 v120, s[56:59], s99 offen lds
s_add_u32 m0, m0, 1024
s_add_u32 s99, s99, 5120
buffer_load_dwordx4 v120, s[56:59], s99 offen lds
s_and_b32 s77, s77, 127
s_lshl_b32 s75, s64, 4
s_add_u32 s77, s77, s75
s_add_u32 s77, s77, s66
s_add_u32 s77, s77, 114688
v_lshl_add_u32 v121, v209, 8, s77
v_lshlrev_b32 v208, 4, v209
v_bfe_u32 v7, v209, 2, 2
v_and_b32 v8, 3, v209
v_lshlrev_b32 v112, 10, v6
v_lshl_add_u32 v112, v7, 7, v112
v_lshl_add_u32 v112, v8, 3, v112
s_lshl_b32 s75, s64, 5
s_add_u32 s75, s75, s35
v_add_u32 v112, s75, v112
v_lshl_add_u32 v113, v6, 4, s35
v_lshl_add_u32 v114, v0, 1, s35
v_lshlrev_b32 v115, 28, v6
v_add_u32 v244, v115, v208
v_lshl_add_u32 v116, v209, 2, v115
v_lshl_add_u32 v115, v209, 3, v115
s_mul_i32 s76, s90, 240
s_lshl_b32 s75, s33, 2
s_add_u32 s76, s76, s75
s_add_u32 s76, s76, s64
s_lshl_b32 s98, s76, 6
s_lshl_b32 s97, s76, 7
s_add_u32 s97, s97, 32587776
s_add_u32 s98, s98, 17629184
s_cmp_lt_u32 s65, 80
s_cselect_b32 s33, s97, 47923200
s_cmp_ge_u32 s65, 96
s_cselect_b32 s97, s97, 47923200
s_cselect_b32 s98, s98, 47923200
s_cselect_b32 s50, 4915200, 0
v_min_u32 v212, 1, v209
v_lshlrev_b32 v212, 30, v212
v_lshl_add_u32 v117, v5, 3, s35
s_lshl_b32 s75, s64, 6
s_add_u32 s75, s75, s35
v_lshl_add_u32 v118, v4, 3, s75
v_lshl_add_u32 v126, v4, 27, v208
s_lshl_b32 s87, s94, 7
s_add_u32 s87, s87, 16809984
s_cmp_eq_u32 s64, 0
s_cselect_b32 s87, s87, 47923200
s_cmp_lt_u32 s64, 2
s_cselect_b32 s75, 2, 1
v_min_u32 v119, s75, v4
v_lshlrev_b32 v119, 8, v119
v_lshl_add_u32 v119, v8, 4, v119
v_bfe_u32 v104, v209, 2, 1
v_cvt_f32_u32 v10, v104
v_mul_u32_u24 v104, 5120, v104
s_mul_i32 s75, s95, 640
s_lshl_b32 s76, s64, 6
s_add_u32 s75, s75, s76
v_add3_u32 v119, v119, v104, s75
s_lshl_b32 s75, s64, 9
s_add_u32 s75, s75, s35
v_mov_b32 v123, s75
v_lshl_add_u32 v122, v209, 3, s75
s_mov_b64 s[40:41], s[14:15]
s_and_b32 s41, s41, 0xffff
s_mov_b32 s42, 5242880
s_mov_b32 s43, 0x20000
s_and_b32 s75, s65, 7
s_lshl_b32 s75, s75, 2
s_bfe_u32 s76, s65, 0x20003
s_add_u32 s67, s75, s76
s_lshr_b32 s75, s65, 5
s_lshl_b32 s75, s75, 2
s_add_u32 s68, s75, s64
s_mul_i32 s69, s68, 81920
s_add_u32 s69, s69, 17694720
v_bfe_u32 v214, v209, 2, 3
v_lshrrev_b32 v215, 5, v209
v_and_b32 v216, 3, v209
v_lshlrev_b32 v217, 6, v215
v_lshl_add_u32 v217, v216, 4, v217
v_lshl_add_u32 v226, v214, 13, v217
s_lshl_b32 s70, s64, 17
s_lshl_b32 s75, s67, 8
s_add_u32 s70, s70, s75
s_lshl_b32 s71, s64, 12
s_add_u32 s71, s71, s66
s_lshl_b32 s75, s65, 2
s_add_u32 s75, s75, s64
s_lshl_b32 s75, s75, 8
s_add_u32 s75, s75, 16842752
v_lshlrev_b32 v18, 6, v209
v_mov_b32 v19, 0
s_mov_b64 exec, 15
buffer_atomic_add v19, v18, s[36:39], s75 offen sc1
s_mov_b64 exec, -1
v_min_u32 v13, 39, v209
v_lshrrev_b32 v14, 1, v13
v_and_b32 v13, 1, v13
v_lshlrev_b32 v13, 7, v13
v_lshl_add_u32 v13, v14, 18, v13
s_lshl_b32 s75, s68, 13
s_lshl_b32 s77, s67, 8
s_add_u32 s75, s75, s77
s_add_u32 s75, s75, 0
buffer_load_dword v13, v13, s[40:43], s75 offen
s_waitcnt vmcnt(15)
v_lshlrev_b32 v3, 16, v3
v_mul_f32 v109, 0xbfb8aa3b, v3
v_exp_f32 v109, v109
s_nop 0
v_add_f32 v109, 1.0, v109
v_rcp_f32 v109, v109
s_nop 0
v_mul_f32 v3, v3, v109
v_cvt_pk_bf16_f32 v3, v3, v3
ds_write_b16 v114, v3 offset:20480
s_waitcnt vmcnt(14)
v_lshlrev_b32 v32, 16, v20
v_and_b32 v33, 0xffff0000, v20
v_lshlrev_b32 v34, 16, v21
v_and_b32 v35, 0xffff0000, v21
v_lshlrev_b32 v36, 16, v22
v_and_b32 v37, 0xffff0000, v22
v_lshlrev_b32 v38, 16, v23
v_and_b32 v39, 0xffff0000, v23
s_waitcnt vmcnt(13)
v_lshlrev_b32 v40, 16, v24
v_and_b32 v41, 0xffff0000, v24
v_lshlrev_b32 v42, 16, v25
v_and_b32 v43, 0xffff0000, v25
v_lshlrev_b32 v44, 16, v26
v_and_b32 v45, 0xffff0000, v26
v_lshlrev_b32 v46, 16, v27
v_and_b32 v47, 0xffff0000, v27
s_waitcnt vmcnt(12)
v_lshlrev_b32 v48, 16, v28
v_and_b32 v49, 0xffff0000, v28
v_lshlrev_b32 v50, 16, v29
v_and_b32 v51, 0xffff0000, v29
v_lshlrev_b32 v52, 16, v30
v_and_b32 v53, 0xffff0000, v30
v_lshlrev_b32 v54, 16, v31
v_and_b32 v55, 0xffff0000, v31
v_pk_add_f32 v[106:107], v[32:33], v[34:35]
v_pk_mul_f32 v[108:109], v[32:33], v[32:33]
v_pk_fma_f32 v[108:109], v[34:35], v[34:35], v[108:109]
v_pk_add_f32 v[106:107], v[106:107], v[36:37]
v_pk_fma_f32 v[108:109], v[36:37], v[36:37], v[108:109]
v_pk_add_f32 v[106:107], v[106:107], v[38:39]
v_pk_fma_f32 v[108:109], v[38:39], v[38:39], v[108:109]
v_pk_add_f32 v[106:107], v[106:107], v[40:41]
v_pk_fma_f32 v[108:109], v[40:41], v[40:41], v[108:109]
v_pk_add_f32 v[106:107], v[106:107], v[42:43]
v_pk_fma_f32 v[108:109], v[42:43], v[42:43], v[108:109]
v_pk_add_f32 v[106:107], v[106:107], v[44:45]
v_pk_fma_f32 v[108:109], v[44:45], v[44:45], v[108:109]
v_pk_add_f32 v[106:107], v[106:107], v[46:47]
v_pk_fma_f32 v[108:109], v[46:47], v[46:47], v[108:109]
v_pk_add_f32 v[106:107], v[106:107], v[48:49]
v_pk_fma_f32 v[108:109], v[48:49], v[48:49], v[108:109]
v_pk_add_f32 v[106:107], v[106:107], v[50:51]
v_pk_fma_f32 v[108:109], v[50:51], v[50:51], v[108:109]
v_pk_add_f32 v[106:107], v[106:107], v[52:53]
v_pk_fma_f32 v[108:109], v[52:53], v[52:53], v[108:109]
v_pk_add_f32 v[106:107], v[106:107], v[54:55]
v_pk_fma_f32 v[108:109], v[54:55], v[54:55], v[108:109]
v_add_f32 v106, v106, v107
v_add_f32 v107, v108, v109
s_nop 0
v_add_f32_dpp v106, v106, v106 quad_perm:[1,0,3,2] row_mask:0xf bank_mask:0xf
v_add_f32_dpp v107, v107, v107 quad_perm:[1,0,3,2] row_mask:0xf bank_mask:0xf
s_nop 0
v_add_f32_dpp v106, v106, v106 quad_perm:[2,3,0,1] row_mask:0xf bank_mask:0xf
v_add_f32_dpp v107, v107, v107 quad_perm:[2,3,0,1] row_mask:0xf bank_mask:0xf
s_nop 0
v_add_f32_dpp v106, v106, v106 row_half_mirror row_mask:0xf bank_mask:0xf
v_add_f32_dpp v107, v107, v107 row_half_mirror row_mask:0xf bank_mask:0xf
ds_write_b64 v118, v[106:107] offset:20992
s_waitcnt vmcnt(11)
v_readfirstlane_b32 s80, v210
v_readfirstlane_b32 s81, v211
s_add_u32 s80, s80, 1
s_addc_u32 s81, s81, 0
v_mov_b32 v210, s80
v_mov_b32 v211, s81
v_mov_b32 v111, s80
s_movk_i32 s76, 0xff
v_mov_b32 v97, s80
v_mov_b32 v99, s80
s_waitcnt vmcnt(6)
s_waitcnt lgkmcnt(0)
s_barrier
ds_read_b64 v[20:21], v117 offset:20992
ds_read_b64 v[22:23], v117 offset:21056
ds_read_b64 v[24:25], v117 offset:21120
ds_read_b64 v[26:27], v117 offset:21184
ds_read_b128 v[76:79], v113 offset:20480
ds_read_b64_tr_b16 v[56:57], v112 offset:0
ds_read_b64_tr_b16 v[58:59], v112 offset:512
ds_read_b128 v[80:83], v113 offset:20544
ds_read_b64_tr_b16 v[60:61], v112 offset:4096
ds_read_b64_tr_b16 v[62:63], v112 offset:4608
ds_read_b128 v[84:87], v113 offset:20608
ds_read_b64_tr_b16 v[64:65], v112 offset:8192
ds_read_b64_tr_b16 v[66:67], v112 offset:8704
ds_read_b128 v[88:91], v113 offset:20672
ds_read_b64_tr_b16 v[68:69], v112 offset:12288
ds_read_b64_tr_b16 v[70:71], v112 offset:12800
ds_read_b128 v[92:95], v113 offset:20736
ds_read_b64_tr_b16 v[72:73], v112 offset:16384
ds_read_b64_tr_b16 v[74:75], v112 offset:16896
s_waitcnt lgkmcnt(15)
v_pk_add_f32 v[20:21], v[20:21], v[22:23]
v_pk_add_f32 v[24:25], v[24:25], v[26:27]
v_pk_add_f32 v[20:21], v[20:21], v[24:25]
v_mul_f32 v20, 0xbacccccd, v20
v_mul_f32 v21, 0x3acccccd, v21
v_fma_f32 v21, -v20, v20, v21
v_max_f32 v21, 0, v21
v_add_f32 v21, 0x3727c5ac, v21
v_rsq_f32 v22, v21
s_waitcnt lgkmcnt(12)
v_mfma_f32_16x16x32_bf16 v[104:107], v[76:79], v[56:59], 0
s_waitcnt lgkmcnt(9)
v_mfma_f32_16x16x32_bf16 v[104:107], v[80:83], v[60:63], v[104:107]
s_waitcnt lgkmcnt(6)
v_mfma_f32_16x16x32_bf16 v[104:107], v[84:87], v[64:67], v[104:107]
s_waitcnt lgkmcnt(3)
v_mfma_f32_16x16x32_bf16 v[104:107], v[88:91], v[68:71], v[104:107]
s_waitcnt lgkmcnt(0)
v_mfma_f32_16x16x32_bf16 v[104:107], v[92:95], v[72:75], v[104:107]
v_mul_f32 v23, v20, v22
v_bfi_b32 v96, s76, v211, v22
v_bfi_b32 v98, s76, v211, v23
buffer_store_dwordx4 v[96:99], v126, s[36:39], s87 offen sc1
s_nop 3
v_bfi_b32 v110, s76, v211, v104
buffer_store_dwordx2 v[110:111], v115, s[36:39], s33 offen sc1
s_cmp_ge_u32 s65, 96
s_cbranch_scc0 .Lk_ngb
s_add_u32 m0, s34, 0
s_add_u32 s75, s86, 0
buffer_load_dwordx4 v1, s[48:51], s75 offen lds
s_add_u32 m0, s34, 4096
s_add_u32 s75, s86, 245760
buffer_load_dwordx4 v1, s[48:51], s75 offen lds
s_add_u32 m0, s34, 8192
s_add_u32 s75, s86, 491520
buffer_load_dwordx4 v1, s[48:51], s75 offen lds
s_add_u32 m0, s34, 12288
s_add_u32 s75, s86, 737280
buffer_load_dwordx4 v1, s[48:51], s75 offen lds
s_add_u32 m0, s34, 16384
s_add_u32 s75, s86, 983040
buffer_load_dwordx4 v1, s[48:51], s75 offen lds
.Lk_ngb:
s_add_u32 m0, s71, 0
s_add_u32 s75, s70, 0
buffer_load_dwordx4 v226, s[40:43], s75 offen lds
s_add_u32 m0, s71, 1024
s_add_u32 s75, s70, 128
buffer_load_dwordx4 v226, s[40:43], s75 offen lds
s_add_u32 m0, s71, 2048
s_add_u32 s75, s70, 65536
buffer_load_dwordx4 v226, s[40:43], s75 offen lds
s_add_u32 m0, s71, 3072
s_add_u32 s75, s70, 65664
buffer_load_dwordx4 v226, s[40:43], s75 offen lds
s_add_u32 m0, s71, 16384
s_add_u32 s75, s70, 524288
buffer_load_dwordx4 v226, s[40:43], s75 offen lds
s_add_u32 m0, s71, 17408
s_add_u32 s75, s70, 524416
buffer_load_dwordx4 v226, s[40:43], s75 offen lds
s_add_u32 m0, s71, 18432
s_add_u32 s75, s70, 589824
buffer_load_dwordx4 v226, s[40:43], s75 offen lds
s_add_u32 m0, s71, 19456
s_add_u32 s75, s70, 589952
buffer_load_dwordx4 v226, s[40:43], s75 offen lds
s_waitcnt vmcnt(12)
s_barrier
ds_read_b128 v[20:23], v121 offset:0
ds_read_b128 v[24:27], v121 offset:64
ds_read_b128 v[28:31], v121 offset:128
s_lshr_b32 s99, s94, 3
s_lshl_b32 s87, s99, 10
s_add_u32 s87, s87, 16809984
s_mul_i32 s99, s99, 20
s_mul_i32 s75, s95, 10
s_add_u32 s75, s75, s64
s_lshr_b32 s77, s75, 2
s_add_u32 s99, s99, s77
s_lshl_b32 s99, s99, 4
s_and_b32 s75, s75, 3
s_add_u32 s99, s99, s75
s_lshl_b32 s99, s99, 8
s_add_u32 s99, s99, 17694720
v_and_b32 v124, 0xf0, v208
v_lshl_add_u32 v124, v6, 10, v124
v_lshlrev_b32 v125, 3, v5
v_lshlrev_b32 v126, 13, v5
s_and_b32 s14, s65, 7
s_lshl_b32 s14, s14, 7
s_add_u32 s14, s14, s68
s_lshl_b32 s14, s14, 6
s_add_u32 s14, s14, 17039360
s_lshl_b32 s88, s94, 3
s_lshl_b32 s75, s64, 14
s_add_u32 s88, s88, s75
s_add_u32 s88, s88, 17039360
s_waitcnt lgkmcnt(2)
v_lshlrev_b32 v32, 16, v20
v_and_b32 v33, 0xffff0000, v20
v_lshlrev_b32 v34, 16, v21
v_and_b32 v35, 0xffff0000, v21
v_lshlrev_b32 v36, 16, v22
v_and_b32 v37, 0xffff0000, v22
v_lshlrev_b32 v38, 16, v23
v_and_b32 v39, 0xffff0000, v23
s_waitcnt lgkmcnt(1)
v_lshlrev_b32 v40, 16, v24
v_and_b32 v41, 0xffff0000, v24
v_lshlrev_b32 v42, 16, v25
v_and_b32 v43, 0xffff0000, v25
v_lshlrev_b32 v44, 16, v26
v_and_b32 v45, 0xffff0000, v26
v_lshlrev_b32 v46, 16, v27
v_and_b32 v47, 0xffff0000, v27
s_waitcnt lgkmcnt(0)
v_lshlrev_b32 v48, 16, v28
v_and_b32 v49, 0xffff0000, v28
v_lshlrev_b32 v50, 16, v29
v_and_b32 v51, 0xffff0000, v29
v_lshlrev_b32 v52, 16, v30
v_and_b32 v53, 0xffff0000, v30
v_lshlrev_b32 v54, 16, v31
v_and_b32 v55, 0xffff0000, v31
v_bfe_u32 v214, v209, 2, 3
v_lshrrev_b32 v215, 5, v209
v_and_b32 v216, 3, v209
s_cmp_lt_u32 s64, 2
s_cselect_b32 s77, 32, 0
s_cselect_b32 s76, 8192, 0
s_and_b32 s75, s64, 1
s_lshl_b32 s72, s67, 19
s_add_u32 s72, s72, s76
s_lshl_b32 s76, s75, 12
s_add_u32 s72, s72, s76
s_add_u32 s72, s72, 384000
s_xor_b32 s75, s77, 32
v_lshlrev_b32 v223, 1, v209
v_and_b32 v223, s75, v223
v_lshlrev_b32 v222, 4, v216
v_xor_b32 v222, v222, v223
v_lshl_add_u32 v222, v215, 6, v222
v_lshl_add_u32 v222, v214, 8, v222
v_add_u32 v223, 128, v222
v_add_u32 v222, 1024, v222
v_xor_b32 v224, s77, v222
v_xor_b32 v225, s77, v223
v_lshrrev_b32 v217, 4, v209
v_lshlrev_b32 v218, 11, v217
v_and_b32 v219, 15, v209
v_lshl_add_u32 v218, v219, 4, v218
v_add_u32 v218, s66, v218
v_lshlrev_b32 v220, 11, v215
v_lshl_add_u32 v220, v214, 6, v220
buffer_load_dwordx4 v[72:75], v208, s[36:39], s87 offen sc1
s_mov_b32 s33, 32587776
buffer_load_dwordx4 v[56:59], v119, s[36:39], s33 offen sc1
s_mov_b32 s33, 32618496
buffer_load_dwordx4 v[60:63], v119, s[36:39], s33 offen sc1
s_mov_b32 s33, 32649216
buffer_load_dwordx4 v[64:67], v119, s[36:39], s33 offen sc1
s_mov_b32 s33, 32679936
buffer_load_dwordx4 v[68:71], v119, s[36:39], s33 offen sc1
v_lshl_add_u32 v220, v216, 3, v220
v_bfe_u32 v221, v209, 4, 1
v_lshl_add_u32 v220, v221, 5, v220
v_xor_b32 v221, 32, v220
v_add_u32 v220, s66, v220
v_add_u32 v221, s66, v221
v_bfe_u32 v214, v209, 3, 1
v_lshlrev_b32 v215, 1, v214
v_xor_b32 v215, v215, v217
v_lshlrev_b32 v215, 4, v215
v_lshl_add_u32 v215, v214, 11, v215
v_and_b32 v219, 7, v209
v_lshl_add_u32 v219, v219, 6, v215
v_add_u32 v219, s66, v219
s_lshl_b32 s73, s68, 19
s_lshl_b32 s75, s67, 14
s_add_u32 s73, s73, s75
s_lshl_b32 s75, s64, 10
s_add_u32 s73, s73, s75
s_and_b32 s75, s67, 8
s_lshl_b32 s75, s75, 8
v_add_u32 v193, s75, v208
s_xor_b32 s75, s75, 2048
v_add_u32 v192, s75, v208
s_xor_b32 s73, s73, s75
v_cmp_eq_u32 vcc, 0, v216
v_mov_b32 v233, 0x3f803f80
s_nop 0
v_cndmask_b32 v232, 0, v233, vcc
v_mov_b32 v233, v232
v_mov_b32 v234, v232
v_mov_b32 v235, v232
s_bfe_u32 s82, s65, 0x20001
s_lshl_b32 s82, s82, 2
s_bfe_u32 s75, s65, 0x20003
s_add_u32 s82, s82, s75
s_lshl_b32 s82, s82, 1
s_and_b32 s75, s65, 1
s_add_u32 s82, s82, s75
s_lshr_b32 s83, s65, 5
s_lshl_b32 s75, s83, 1
s_and_b32 s76, s82, 1
s_add_u32 s75, s75, s76
s_lshl_b32 s75, s75, 2
s_add_u32 s75, s75, s64
s_mul_i32 s85, s75, 81920
s_add_u32 s85, s85, 20316160
s_add_u32 s78, s85, 37888
s_mul_i32 s79, s64, 24576
s_add_u32 s79, s79, s66
s_add_u32 s79, s79, 65536
s_waitcnt vmcnt(3)
v_xor_b32 v20, s80, v57
v_xor_b32 v21, s81, v56
v_bitop3_b32 v20, v20, v59, s80 bitop3:0xf6
v_bitop3_b32 v21, v21, v58, s81 bitop3:0xf6
s_waitcnt vmcnt(2)
v_bitop3_b32 v20, v20, v61, s80 bitop3:0xf6
v_bitop3_b32 v21, v21, v60, s81 bitop3:0xf6
v_bitop3_b32 v20, v20, v63, s80 bitop3:0xf6
v_bitop3_b32 v21, v21, v62, s81 bitop3:0xf6
s_waitcnt vmcnt(1)
v_bitop3_b32 v20, v20, v65, s80 bitop3:0xf6
v_bitop3_b32 v21, v21, v64, s81 bitop3:0xf6
v_bitop3_b32 v20, v20, v67, s80 bitop3:0xf6
v_bitop3_b32 v21, v21, v66, s81 bitop3:0xf6
s_waitcnt vmcnt(0)
v_bitop3_b32 v20, v20, v69, s80 bitop3:0xf6
v_bitop3_b32 v21, v21, v68, s81 bitop3:0xf6
v_bitop3_b32 v20, v20, v71, s80 bitop3:0xf6
v_bitop3_b32 v21, v21, v70, s81 bitop3:0xf6
v_bitop3_b32 v20, v20, v73, s80 bitop3:0xf6
v_bitop3_b32 v21, v21, v72, s81 bitop3:0xf6
v_bitop3_b32 v20, v20, v75, s80 bitop3:0xf6
v_bitop3_b32 v21, v21, v74, s81 bitop3:0xf6
v_and_b32 v21, 0xff, v21
v_or_b32 v20, v20, v21
v_cmp_ne_u32 vcc, 0, v20
s_nop 0
s_cbranch_vccz .Lk_g1_ok
s_mov_b32 s89, 0
.Lk_g1:
buffer_load_dwordx4 v[72:75], v208, s[36:39], s87 offen sc1
s_mov_b32 s33, 32587776
buffer_load_dwordx4 v[56:59], v119, s[36:39], s33 offen sc1
s_mov_b32 s33, 32618496
buffer_load_dwordx4 v[60:63], v119, s[36:39], s33 offen sc1
s_mov_b32 s33, 32649216
buffer_load_dwordx4 v[64:67], v119, s[36:39], s33 offen sc1
s_mov_b32 s33, 32679936
buffer_load_dwordx4 v[68:71], v119, s[36:39], s33 offen sc1
s_waitcnt vmcnt(0)
v_xor_b32 v20, s80, v57
v_xor_b32 v21, s81, v56
v_bitop3_b32 v20, v20, v59, s80 bitop3:0xf6
v_bitop3_b32 v21, v21, v58, s81 bitop3:0xf6
v_bitop3_b32 v20, v20, v61, s80 bitop3:0xf6
v_bitop3_b32 v21, v21, v60, s81 bitop3:0xf6
v_bitop3_b32 v20, v20, v63, s80 bitop3:0xf6
v_bitop3_b32 v21, v21, v62, s81 bitop3:0xf6
v_bitop3_b32 v20, v20, v65, s80 bitop3:0xf6
v_bitop3_b32 v21, v21, v64, s81 bitop3:0xf6
v_bitop3_b32 v20, v20, v67, s80 bitop3:0xf6
v_bitop3_b32 v21, v21, v66, s81 bitop3:0xf6
v_bitop3_b32 v20, v20, v69, s80 bitop3:0xf6
v_bitop3_b32 v21, v21, v68, s81 bitop3:0xf6
v_bitop3_b32 v20, v20, v71, s80 bitop3:0xf6
v_bitop3_b32 v21, v21, v70, s81 bitop3:0xf6
v_bitop3_b32 v20, v20, v73, s80 bitop3:0xf6
v_bitop3_b32 v21, v21, v72, s81 bitop3:0xf6
v_bitop3_b32 v20, v20, v75, s80 bitop3:0xf6
v_bitop3_b32 v21, v21, v74, s81 bitop3:0xf6
v_and_b32 v21, 0xff, v21
v_or_b32 v20, v20, v21
v_cmp_ne_u32 vcc, 0, v20
s_nop 0
s_cbranch_vccz .Lk_g1_ok
s_add_u32 s89, s89, 1
s_cmp_lt_u32 s89, 0x20000
s_cbranch_scc1 .Lk_g1
.Lk_g1_ok:
v_and_b32 v56, 0xffffff00, v56
v_and_b32 v58, 0xffffff00, v58
v_and_b32 v60, 0xffffff00, v60
v_and_b32 v62, 0xffffff00, v62
v_and_b32 v64, 0xffffff00, v64
v_and_b32 v66, 0xffffff00, v66
v_and_b32 v68, 0xffffff00, v68
v_and_b32 v70, 0xffffff00, v70
v_add_f32 v56, v56, v60
v_add_f32 v58, v58, v62
v_add_f32 v56, v56, v64
v_add_f32 v58, v58, v66
v_add_f32 v56, v56, v68
v_add_f32 v58, v58, v70
v_cvt_pk_bf16_f32 v57, v56, v58
v_lshlrev_b32 v56, 16, v57
v_and_b32 v58, 0xffff0000, v57
v_add_f32 v56, v56, v10
v_add_f32 v58, v58, v10
v_cvt_pk_bf16_f32 v57, v56, v58
v_lshlrev_b32 v56, 16, v57
v_and_b32 v57, 0xffff0000, v57
v_mul_f32 v56, 1040356334, v56
v_mul_f32 v57, 1040356334, v57
ds_write_b64 v122, v[56:57] offset:22528
v_and_b32 v20, 0xffffff00, v72
v_and_b32 v21, 0xffffff00, v74
ds_read_b128 v[56:59], v123 offset:22528
ds_read_b128 v[60:63], v123 offset:22544
ds_read_b128 v[64:67], v123 offset:22560
ds_read_b128 v[68:71], v123 offset:22576
ds_read_b128 v[72:75], v123 offset:22592
ds_read_b128 v[76:79], v123 offset:22608
ds_read_b128 v[80:83], v123 offset:22624
ds_read_b128 v[84:87], v123 offset:22640
ds_read_b128 v[88:91], v123 offset:22656
ds_read_b128 v[92:95], v123 offset:22672
ds_read_b128 v[96:99], v123 offset:22688
ds_read_b128 v[100:103], v123 offset:22704
v_pk_fma_f32 v[32:33], v[32:33], v[20:21], v[20:21] op_sel:[0,0,1] op_sel_hi:[1,0,1]
v_pk_fma_f32 v[34:35], v[34:35], v[20:21], v[20:21] op_sel:[0,0,1] op_sel_hi:[1,0,1]
v_pk_fma_f32 v[36:37], v[36:37], v[20:21], v[20:21] op_sel:[0,0,1] op_sel_hi:[1,0,1]
v_pk_fma_f32 v[38:39], v[38:39], v[20:21], v[20:21] op_sel:[0,0,1] op_sel_hi:[1,0,1]
v_pk_fma_f32 v[40:41], v[40:41], v[20:21], v[20:21] op_sel:[0,0,1] op_sel_hi:[1,0,1]
v_pk_fma_f32 v[42:43], v[42:43], v[20:21], v[20:21] op_sel:[0,0,1] op_sel_hi:[1,0,1]
v_pk_fma_f32 v[44:45], v[44:45], v[20:21], v[20:21] op_sel:[0,0,1] op_sel_hi:[1,0,1]
v_pk_fma_f32 v[46:47], v[46:47], v[20:21], v[20:21] op_sel:[0,0,1] op_sel_hi:[1,0,1]
v_pk_fma_f32 v[48:49], v[48:49], v[20:21], v[20:21] op_sel:[0,0,1] op_sel_hi:[1,0,1]
v_pk_fma_f32 v[50:51], v[50:51], v[20:21], v[20:21] op_sel:[0,0,1] op_sel_hi:[1,0,1]
v_pk_fma_f32 v[52:53], v[52:53], v[20:21], v[20:21] op_sel:[0,0,1] op_sel_hi:[1,0,1]
v_pk_fma_f32 v[54:55], v[54:55], v[20:21], v[20:21] op_sel:[0,0,1] op_sel_hi:[1,0,1]
s_waitcnt lgkmcnt(9)
v_pk_fma_f32 v[32:33], v[32:33], v[64:65], v[56:57]
v_pk_fma_f32 v[34:35], v[34:35], v[66:67], v[58:59]
s_waitcnt lgkmcnt(8)
v_pk_fma_f32 v[36:37], v[36:37], v[68:69], v[60:61]
v_pk_fma_f32 v[38:39], v[38:39], v[70:71], v[62:63]
v_cvt_pk_bf16_f32 v32, v32, v33
v_cvt_pk_bf16_f32 v33, v34, v35
v_cvt_pk_bf16_f32 v34, v36, v37
v_cvt_pk_bf16_f32 v35, v38, v39
buffer_store_dwordx4 v[32:35], v124, s[36:39], s99 offen sc1
s_waitcnt lgkmcnt(5)
v_pk_fma_f32 v[40:41], v[40:41], v[80:81], v[72:73]
v_pk_fma_f32 v[42:43], v[42:43], v[82:83], v[74:75]
s_waitcnt lgkmcnt(4)
v_pk_fma_f32 v[44:45], v[44:45], v[84:85], v[76:77]
v_pk_fma_f32 v[46:47], v[46:47], v[86:87], v[78:79]
v_cvt_pk_bf16_f32 v40, v40, v41
v_cvt_pk_bf16_f32 v41, v42, v43
v_cvt_pk_bf16_f32 v42, v44, v45
v_cvt_pk_bf16_f32 v43, v46, v47
s_add_u32 s99, s99, 4096
buffer_store_dwordx4 v[40:43], v124, s[36:39], s99 offen sc1
s_waitcnt lgkmcnt(1)
v_pk_fma_f32 v[48:49], v[48:49], v[96:97], v[88:89]
v_pk_fma_f32 v[50:51], v[50:51], v[98:99], v[90:91]
s_waitcnt lgkmcnt(0)
v_pk_fma_f32 v[52:53], v[52:53], v[100:101], v[92:93]
v_pk_fma_f32 v[54:55], v[54:55], v[102:103], v[94:95]
v_cvt_pk_bf16_f32 v48, v48, v49
v_cvt_pk_bf16_f32 v49, v50, v51
v_cvt_pk_bf16_f32 v50, v52, v53
v_cvt_pk_bf16_f32 v51, v54, v55
s_add_u32 s99, s99, 4096
s_cmp_lt_u32 s64, 2
s_cselect_b32 s99, s99, 47923200
buffer_store_dwordx4 v[48:51], v124, s[36:39], s99 offen sc1
s_add_u32 m0, s71, 32768
s_add_u32 s75, s70, 1048576
buffer_load_dwordx4 v226, s[40:43], s75 offen lds
s_add_u32 m0, s71, 33792
s_add_u32 s75, s70, 1048704
buffer_load_dwordx4 v226, s[40:43], s75 offen lds
s_add_u32 m0, s71, 34816
s_add_u32 s75, s70, 1114112
buffer_load_dwordx4 v226, s[40:43], s75 offen lds
s_add_u32 m0, s71, 35840
s_add_u32 s75, s70, 1114240
buffer_load_dwordx4 v226, s[40:43], s75 offen lds
s_mov_b64 s[44:45], s[16:17]
s_and_b32 s45, s45, 0xffff
s_mov_b32 s46, 5242880
s_mov_b32 s47, 0x20000
v_mul_u32_u24 v204, 1280, v4
v_lshl_add_u32 v204, v5, 4, v204
s_lshr_b32 s75, s65, 1
s_lshl_b32 s76, s75, 2
s_add_u32 s76, s76, s64
s_mul_i32 s76, s76, 10240
s_and_b32 s77, s65, 1
s_mul_i32 s25, s77, 640
s_add_u32 s26, s76, s25
s_lshl_b32 s77, s77, 2
s_add_u32 s77, s77, s64
s_lshl_b32 s77, s77, 7
s_add_u32 s77, s77, s75
s_mul_i32 s84, s77, 5120
s_add_u32 s84, s84, 20316160
s_mul_i32 s75, s64, 5120
s_add_u32 s27, s75, s66
s_add_u32 s24, s27, s75
s_mul_i32 s75, s64, 160
v_mul_u32_u24 v15, 5120, v6
v_lshl_add_u32 v16, v7, 7, v15
s_add_u32 s76, s66, 131072
v_add_u32 v16, s76, v16
s_add_u32 s76, s75, 0
v_lshl_add_u32 v160, v8, 4, s76
v_lshrrev_b32 v17, 7, v160
v_and_b32 v160, 127, v160
v_lshl_add_u32 v160, v17, 10, v160
v_add_u32 v160, v160, v16
s_add_u32 s76, s75, 8
v_lshl_add_u32 v161, v8, 4, s76
v_lshrrev_b32 v17, 7, v161
v_and_b32 v161, 127, v161
v_lshl_add_u32 v161, v17, 10, v161
v_add_u32 v161, v161, v16
s_add_u32 s76, s75, 64
v_lshl_add_u32 v162, v8, 4, s76
v_lshrrev_b32 v17, 7, v162
v_and_b32 v162, 127, v162
v_lshl_add_u32 v162, v17, 10, v162
v_add_u32 v162, v162, v16
s_add_u32 s76, s75, 72
v_lshl_add_u32 v163, v8, 4, s76
v_lshrrev_b32 v17, 7, v163
v_and_b32 v163, 127, v163
v_lshl_add_u32 v163, v17, 10, v163
v_add_u32 v163, v163, v16
s_add_u32 s76, s75, 128
v_lshl_add_u32 v164, v8, 3, s76
v_lshrrev_b32 v17, 7, v164
v_and_b32 v164, 127, v164
v_lshl_add_u32 v164, v17, 10, v164
v_add_u32 v164, v164, v16
s_waitcnt vmcnt(4)
s_barrier
s_mov_b64 exec, 3
buffer_atomic_swap_x2 v[210:211], v126, s[36:39], s88 offen sc1
s_mov_b64 exec, -1
s_add_u32 m0, s71, 49152
s_add_u32 s75, s70, 1572864
buffer_load_dwordx4 v226, s[40:43], s75 offen lds
s_add_u32 m0, s71, 50176
s_add_u32 s75, s70, 1572992
buffer_load_dwordx4 v226, s[40:43], s75 offen lds
s_add_u32 m0, s71, 51200
s_add_u32 s75, s70, 1638400
buffer_load_dwordx4 v226, s[40:43], s75 offen lds
s_add_u32 m0, s71, 52224
s_add_u32 s75, s70, 1638528
buffer_load_dwordx4 v226, s[40:43], s75 offen lds
s_add_u32 m0, s71, 65536
s_add_u32 s75, s70, 2097152
buffer_load_dwordx4 v226, s[40:43], s75 offen lds
s_add_u32 m0, s71, 66560
s_add_u32 s75, s70, 2097280
buffer_load_dwordx4 v226, s[40:43], s75 offen lds
s_add_u32 m0, s71, 67584
s_add_u32 s75, s70, 2162688
buffer_load_dwordx4 v226, s[40:43], s75 offen lds
s_add_u32 m0, s71, 68608
s_add_u32 s75, s70, 2162816
buffer_load_dwordx4 v226, s[40:43], s75 offen lds
s_movk_i32 s76, 0xff
ds_read_b128 v[76:79], v113 offset:20480
ds_read_b64_tr_b16 v[56:57], v112 offset:0
ds_read_b64_tr_b16 v[58:59], v112 offset:512
ds_read_b128 v[80:83], v113 offset:20544
ds_read_b64_tr_b16 v[60:61], v112 offset:4096
ds_read_b64_tr_b16 v[62:63], v112 offset:4608
ds_read_b128 v[84:87], v113 offset:20608
ds_read_b64_tr_b16 v[64:65], v112 offset:8192
ds_read_b64_tr_b16 v[66:67], v112 offset:8704
ds_read_b128 v[88:91], v113 offset:20672
ds_read_b64_tr_b16 v[68:69], v112 offset:12288
ds_read_b64_tr_b16 v[70:71], v112 offset:12800
ds_read_b128 v[92:95], v113 offset:20736
ds_read_b64_tr_b16 v[72:73], v112 offset:16384
ds_read_b64_tr_b16 v[74:75], v112 offset:16896
s_waitcnt lgkmcnt(12)
v_mfma_f32_16x16x32_bf16 v[104:107], v[76:79], v[56:59], 0
s_waitcnt lgkmcnt(9)
v_mfma_f32_16x16x32_bf16 v[104:107], v[80:83], v[60:63], v[104:107]
s_waitcnt lgkmcnt(6)
v_mfma_f32_16x16x32_bf16 v[104:107], v[84:87], v[64:67], v[104:107]
s_waitcnt lgkmcnt(3)
v_mfma_f32_16x16x32_bf16 v[104:107], v[88:91], v[68:71], v[104:107]
s_waitcnt lgkmcnt(0)
v_mfma_f32_16x16x32_bf16 v[104:107], v[92:95], v[72:75], v[104:107]
s_nop 7
v_bfi_b32 v110, s76, v211, v104
buffer_store_dwordx2 v[110:111], v115, s[36:39], s97 offen sc1
v_and_b32 v104, 0xffffff00, v104
buffer_store_dword v104, v116, s[36:39], s98 offen sc1
v_mov_b64 v[20:21], 0
s_mov_b64 exec, 255
buffer_atomic_add_x2 v[20:21], v125, s[36:39], s14 offen sc0 sc1
s_mov_b64 exec, -1
s_add_u32 m0, s71, 81920
s_add_u32 s75, s70, 2621440
buffer_load_dwordx4 v226, s[40:43], s75 offen lds
s_add_u32 m0, s71, 82944
s_add_u32 s75, s70, 2621568
buffer_load_dwordx4 v226, s[40:43], s75 offen lds
s_add_u32 m0, s71, 83968
s_add_u32 s75, s70, 2686976
buffer_load_dwordx4 v226, s[40:43], s75 offen lds
s_add_u32 m0, s71, 84992
s_add_u32 s75, s70, 2687104
buffer_load_dwordx4 v226, s[40:43], s75 offen lds
s_add_u32 m0, s71, 98304
s_add_u32 s75, s70, 3145728
buffer_load_dwordx4 v226, s[40:43], s75 offen lds
s_add_u32 m0, s71, 99328
s_add_u32 s75, s70, 3145856
buffer_load_dwordx4 v226, s[40:43], s75 offen lds
s_add_u32 m0, s71, 100352
s_add_u32 s75, s70, 3211264
buffer_load_dwordx4 v226, s[40:43], s75 offen lds
s_add_u32 m0, s71, 101376
s_add_u32 s75, s70, 3211392
buffer_load_dwordx4 v226, s[40:43], s75 offen lds
s_waitcnt vmcnt(8)
v_cmp_ne_u64 vcc, s[80:81], v[20:21]
s_nop 0
s_and_b64 vcc, vcc, 255
s_cbranch_scc0 .Lk_h_ok
s_mov_b32 s89, 0
.Lk_h:
v_mov_b64 v[20:21], 0
s_mov_b64 exec, 255
buffer_atomic_add_x2 v[20:21], v125, s[36:39], s14 offen sc0 sc1
s_mov_b64 exec, -1
s_waitcnt vmcnt(0)
v_cmp_ne_u64 vcc, s[80:81], v[20:21]
s_nop 0
s_and_b64 vcc, vcc, 255
s_cbranch_scc0 .Lk_h_ok
s_add_u32 s89, s89, 1
s_cmp_lt_u32 s89, 0x20000
s_cbranch_scc1 .Lk_h
.Lk_h_ok:
buffer_load_dwordx4 a[0:3], v192, s[36:39], s69 offen offset:0 sc1
buffer_load_dwordx4 a[4:7], v192, s[36:39], s69 offen offset:1024 sc1
buffer_load_dwordx4 a[8:11], v193, s[36:39], s69 offen offset:0 sc1
buffer_load_dwordx4 a[12:15], v193, s[36:39], s69 offen offset:1024 sc1
s_add_u32 s69, s69, 4096
buffer_load_dwordx4 a[16:19], v192, s[36:39], s69 offen offset:0 sc1
buffer_load_dwordx4 a[20:23], v192, s[36:39], s69 offen offset:1024 sc1
buffer_load_dwordx4 a[24:27], v193, s[36:39], s69 offen offset:0 sc1
buffer_load_dwordx4 a[28:31], v193, s[36:39], s69 offen offset:1024 sc1
s_sub_u32 s35, 255, s65
s_and_b32 s90, s35, 3
v_mov_b32 v252, v204
v_subrev_u32 v15, 65536, v15
s_cmp_lt_u32 s35, 108
s_cbranch_scc0 .Lk_ywd
s_mov_b64 s[48:49], s[18:19]
s_and_b32 s49, s49, 0xffff
s_mov_b32 s50, 2211840
s_mov_b32 s51, 0x20000
s_mov_b64 s[52:53], s[20:21]
s_and_b32 s53, s53, 0xffff
s_mov_b32 s54, 2211840
s_mov_b32 s55, 0x20000
s_lshr_b32 s34, s35, 2
s_lshl_b32 s33, s34, 2
s_add_u32 s33, s33, s64
s_lshl_b32 s33, s33, 1
s_mul_i32 s33, s33, 21504
s_add_u32 s33, s33, 25559040
s_mul_i32 s75, s90, 5120
s_add_u32 s86, s33, s75
s_add_u32 s87, s86, 21504
s_lshl_b32 s75, s90, 8
s_add_u32 s95, s33, s75
s_add_u32 s95, s95, 20480
s_add_u32 s96, s95, 21504
s_mul_i32 s74, s90, 20
s_add_u32 s74, s74, s64
s_mul_i32 s74, s74, 27648
s_lshl_b32 s34, s34, 7
s_add_u32 s74, s74, s34
s_mov_b32 s91, s74
s_mov_b32 s25, 110592
s_mov_b32 s34, 16
v_mul_u32_u24 v252, 3456, v4
v_lshl_add_u32 v252, v5, 4, v252
v_lshrrev_b32 v17, 1, v8
v_lshl_add_u32 v16, v17, 10, v16
v_and_b32 v17, 1, v8
v_lshl_add_u32 v16, v17, 3, v16
s_lshl_b32 s75, s64, 5
v_add3_u32 v245, v16, v15, s75
v_add_u32 v246, 2048, v245
v_add_u32 v247, 4096, v245
v_add_u32 v248, 6144, v245
v_add_u32 v249, 8192, v245
s_branch .Lk_ygo
.Lk_ywd:
s_mov_b64 s[48:49], s[22:23]
s_and_b32 s49, s49, 0xffff
s_mov_b32 s50, 2211840
s_mov_b32 s51, 0x20000
s_sub_u32 s33, s35, 108
s_cmp_lt_u32 s33, 54
s_cselect_b32 s50, s50, 0
s_cselect_b32 s34, 0, 47923200
s_mov_b64 s[52:53], s[48:49]
s_mov_b64 s[54:55], s[50:51]
s_mul_i32 s86, s64, 54
s_add_u32 s86, s86, s33
s_mul_i32 s86, s86, 5120
s_add_u32 s86, s86, 30375936
s_add_u32 s86, s86, s34
s_add_u32 s87, s86, 1105920
s_mov_b32 s95, 47923200
s_mov_b32 s96, 47923200
s_lshr_b32 s75, s33, 2
s_mul_i32 s75, s75, 27
s_bfe_u32 s76, s33, 0x10001
s_mul_i32 s76, s76, 14
s_add_u32 s75, s75, s76
s_and_b32 s76, s33, 1
s_lshl_b32 s76, s76, 2
s_add_u32 s75, s75, s76
s_add_u32 s34, s75, s64
s_sub_u32 s75, s33, 32
s_and_b32 s75, s75, 31
s_lshl_b32 s75, s75, 2
s_add_u32 s75, s75, s64
s_mul_i32 s76, s75, 47
s_lshr_b32 s76, s76, 9
s_mul_i32 s77, s76, 11
s_sub_u32 s75, s75, s77
s_cmp_lt_u32 s75, 6
s_cselect_b32 s77, 4, 12
s_add_u32 s75, s75, s77
s_mul_i32 s76, s76, 27
s_add_u32 s75, s75, s76
s_add_u32 s75, s75, 4
s_cmp_lt_u32 s33, 32
s_cselect_b32 s74, s34, s75
s_mul_i32 s74, s74, 10240
s_add_u32 s91, s74, 128
s_movk_i32 s25, 0x100
s_movk_i32 s34, 5120
v_add_u32 v245, v160, v15
v_add_u32 v246, v161, v15
v_add_u32 v247, v162, v15
v_add_u32 v248, v163, v15
v_add_u32 v249, v164, v15
.Lk_ygo:
v_add_u32 v15, 1, v6
v_lshrrev_b32 v15, 1, v15
v_mul_u32_u24 v15, 5120, v15
v_and_b32 v16, 1, v6
v_lshlrev_b32 v16, 9, v16
v_sub_u32 v15, v16, v15
v_add_u32 v160, v160, v15
v_add_u32 v161, v161, v15
v_add_u32 v162, v162, v15
v_add_u32 v163, v163, v15
v_add_u32 v164, v164, v15
v_min_u32 v170, 159, v0
v_cmp_gt_u32 vcc, 80, v170
s_add_u32 s75, s66, 152576
v_lshl_add_u32 v171, v170, 2, s75
v_lshlrev_b32 v170, 4, v170
v_cndmask_b32 v189, 0, 1.0, vcc
v_mov_b32 v15, 6400
v_cndmask_b32 v15, 0, v15, vcc
s_mul_i32 s76, s90, 1280
v_add3_u32 v170, v170, v15, s76
v_and_b32 v188, 1, v209
v_mul_u32_u24 v188, 320, v188
v_lshl_add_u32 v188, v6, 4, v188
v_add_u32 v188, s75, v188
s_add_u32 s69, s69, 4096
buffer_load_dwordx4 a[32:35], v192, s[36:39], s69 offen offset:0 sc1
buffer_load_dwordx4 a[36:39], v192, s[36:39], s69 offen offset:1024 sc1
buffer_load_dwordx4 a[40:43], v193, s[36:39], s69 offen offset:0 sc1
buffer_load_dwordx4 a[44:47], v193, s[36:39], s69 offen offset:1024 sc1
s_barrier
ds_read_b64_tr_b16 v[128:129], v218 offset:0
ds_read_b64_tr_b16 v[130:131], v218 offset:256
ds_read_b64_tr_b16 v[132:133], v218 offset:8
ds_read_b64_tr_b16 v[134:135], v218 offset:264
ds_read_b64_tr_b16 v[136:137], v218 offset:512
ds_read_b64_tr_b16 v[138:139], v218 offset:768
ds_read_b64_tr_b16 v[140:141], v218 offset:520
ds_read_b64_tr_b16 v[142:143], v218 offset:776
ds_read_b64_tr_b16 v[144:145], v218 offset:1024
ds_read_b64_tr_b16 v[146:147], v218 offset:1280
ds_read_b64_tr_b16 v[148:149], v218 offset:1032
ds_read_b64_tr_b16 v[150:151], v218 offset:1288
ds_read_b64_tr_b16 v[152:153], v218 offset:1536
ds_read_b64_tr_b16 v[154:155], v218 offset:1792
ds_read_b64_tr_b16 v[156:157], v218 offset:1544
ds_read_b64_tr_b16 v[158:159], v218 offset:1800
s_waitcnt vmcnt(11) lgkmcnt(14)
v_mfma_f32_16x16x32_bf16 v[0:3], v[128:131], a[0:3], 0
s_add_u32 m0, s27, 131072
s_add_u32 s75, s26, 0
s_waitcnt vmcnt(10)
v_mfma_f32_16x16x32_bf16 v[32:35], v[128:131], a[4:7], 0
s_waitcnt vmcnt(9)
v_mfma_f32_16x16x32_bf16 v[64:67], v[128:131], a[8:11], 0
buffer_load_dwordx4 v204, s[44:47], s75 offen lds
s_waitcnt vmcnt(9)
v_mfma_f32_16x16x32_bf16 v[96:99], v[128:131], a[12:15], 0
s_add_u32 m0, s27, 132096
s_add_u32 s75, s26, 128
s_waitcnt lgkmcnt(12)
v_mfma_f32_16x16x32_bf16 v[100:103], v[132:135], a[12:15], 0
ds_read_b64_tr_b16 v[128:129], v218 offset:8192
ds_read_b64_tr_b16 v[130:131], v218 offset:8448
v_mfma_f32_16x16x32_bf16 v[68:71], v[132:135], a[8:11], 0
v_mfma_f32_16x16x32_bf16 v[36:39], v[132:135], a[4:7], 0
buffer_load_dwordx4 v204, s[44:47], s75 offen lds
v_mfma_f32_16x16x32_bf16 v[4:7], v[132:135], a[0:3], 0
s_add_u32 s69, s69, 4096
ds_read_b64_tr_b16 v[132:133], v218 offset:8200
s_waitcnt lgkmcnt(13)
v_mfma_f32_16x16x32_bf16 v[8:11], v[136:139], a[0:3], 0
ds_read_b64_tr_b16 v[134:135], v218 offset:8456
v_mfma_f32_16x16x32_bf16 v[40:43], v[136:139], a[4:7], 0
v_mfma_f32_16x16x32_bf16 v[72:75], v[136:139], a[8:11], 0
buffer_load_dwordx4 a[48:51], v192, s[36:39], s69 offen offset:0 sc1
v_mfma_f32_16x16x32_bf16 v[104:107], v[136:139], a[12:15], 0
ds_read_b64_tr_b16 v[136:137], v218 offset:8704
ds_read_b64_tr_b16 v[138:139], v218 offset:8960
s_waitcnt lgkmcnt(14)
v_mfma_f32_16x16x32_bf16 v[108:111], v[140:143], a[12:15], 0
v_mfma_f32_16x16x32_bf16 v[76:79], v[140:143], a[8:11], 0
v_mfma_f32_16x16x32_bf16 v[44:47], v[140:143], a[4:7], 0
buffer_load_dwordx4 a[52:55], v192, s[36:39], s69 offen offset:1024 sc1
v_mfma_f32_16x16x32_bf16 v[12:15], v[140:143], a[0:3], 0
ds_read_b64_tr_b16 v[140:141], v218 offset:8712
ds_read_b64_tr_b16 v[142:143], v218 offset:8968
s_waitcnt lgkmcnt(14)
v_mfma_f32_16x16x32_bf16 v[16:19], v[144:147], a[0:3], 0
s_add_u32 m0, s27, 133120
s_add_u32 s75, s26, 256
v_mfma_f32_16x16x32_bf16 v[48:51], v[144:147], a[4:7], 0
v_mfma_f32_16x16x32_bf16 v[80:83], v[144:147], a[8:11], 0
v_mfma_f32_16x16x32_bf16 v[112:115], v[144:147], a[12:15], 0
ds_read_b64_tr_b16 v[144:145], v218 offset:9216
ds_read_b64_tr_b16 v[146:147], v218 offset:9472
s_waitcnt lgkmcnt(14)
v_mfma_f32_16x16x32_bf16 v[116:119], v[148:151], a[12:15], 0
buffer_load_dwordx4 v204, s[44:47], s75 offen lds
v_mfma_f32_16x16x32_bf16 v[84:87], v[148:151], a[8:11], 0
s_add_u32 m0, s27, 134144
s_add_u32 s75, s26, 384
v_mfma_f32_16x16x32_bf16 v[52:55], v[148:151], a[4:7], 0
v_mfma_f32_16x16x32_bf16 v[20:23], v[148:151], a[0:3], 0
ds_read_b64_tr_b16 v[148:149], v218 offset:9224
ds_read_b64_tr_b16 v[150:151], v218 offset:9480
s_waitcnt lgkmcnt(14)
v_mfma_f32_16x16x32_bf16 v[24:27], v[152:155], a[0:3], 0
v_mfma_f32_16x16x32_bf16 v[56:59], v[152:155], a[4:7], 0
buffer_load_dwordx4 v204, s[44:47], s75 offen lds
v_mfma_f32_16x16x32_bf16 v[88:91], v[152:155], a[8:11], 0
v_mfma_f32_16x16x32_bf16 v[120:123], v[152:155], a[12:15], 0
ds_read_b64_tr_b16 v[152:153], v218 offset:9728
ds_read_b64_tr_b16 v[154:155], v218 offset:9984
s_waitcnt lgkmcnt(14)
v_mfma_f32_16x16x32_bf16 v[124:127], v[156:159], a[12:15], 0
v_mfma_f32_16x16x32_bf16 v[92:95], v[156:159], a[8:11], 0
v_mfma_f32_16x16x32_bf16 v[60:63], v[156:159], a[4:7], 0
buffer_load_dwordx4 a[56:59], v193, s[36:39], s69 offen offset:0 sc1
v_mfma_f32_16x16x32_bf16 v[28:31], v[156:159], a[0:3], 0
s_waitcnt vmcnt(11) lgkmcnt(6)
ds_read_b64_tr_b16 v[156:157], v218 offset:9736
v_mfma_f32_16x16x32_bf16 v[0:3], v[128:131], a[16:19], v[0:3]
ds_read_b64_tr_b16 v[158:159], v218 offset:9992
s_add_u32 m0, s27, 135168
v_mfma_f32_16x16x32_bf16 v[32:35], v[128:131], a[20:23], v[32:35]
s_add_u32 s75, s26, 512
v_mfma_f32_16x16x32_bf16 v[64:67], v[128:131], a[24:27], v[64:67]
v_mfma_f32_16x16x32_bf16 v[96:99], v[128:131], a[28:31], v[96:99]
ds_read_b64_tr_b16 v[128:129], v218 offset:16384
ds_read_b64_tr_b16 v[130:131], v218 offset:16640
v_mfma_f32_16x16x32_bf16 v[100:103], v[132:135], a[28:31], v[100:103]
buffer_load_dwordx4 v204, s[44:47], s75 offen lds
v_mfma_f32_16x16x32_bf16 v[68:71], v[132:135], a[24:27], v[68:71]
v_mfma_f32_16x16x32_bf16 v[36:39], v[132:135], a[20:23], v[36:39]
v_mfma_f32_16x16x32_bf16 v[4:7], v[132:135], a[16:19], v[4:7]
ds_read_b64_tr_b16 v[132:133], v218 offset:16392
ds_read_b64_tr_b16 v[134:135], v218 offset:16648
v_mfma_f32_16x16x32_bf16 v[8:11], v[136:139], a[16:19], v[8:11]
v_mfma_f32_16x16x32_bf16 v[40:43], v[136:139], a[20:23], v[40:43]
buffer_load_dwordx4 a[60:63], v193, s[36:39], s69 offen offset:1024 sc1
v_mfma_f32_16x16x32_bf16 v[72:75], v[136:139], a[24:27], v[72:75]
s_add_u32 s69, s69, 4096
v_mfma_f32_16x16x32_bf16 v[104:107], v[136:139], a[28:31], v[104:107]
ds_read_b64_tr_b16 v[136:137], v218 offset:16896
ds_read_b64_tr_b16 v[138:139], v218 offset:17152
v_mfma_f32_16x16x32_bf16 v[108:111], v[140:143], a[28:31], v[108:111]
v_mfma_f32_16x16x32_bf16 v[76:79], v[140:143], a[24:27], v[76:79]
v_mfma_f32_16x16x32_bf16 v[44:47], v[140:143], a[20:23], v[44:47]
buffer_load_dwordx4 a[64:67], v192, s[36:39], s69 offen offset:0 sc1
v_mfma_f32_16x16x32_bf16 v[12:15], v[140:143], a[16:19], v[12:15]
s_waitcnt lgkmcnt(6)
ds_read_b64_tr_b16 v[140:141], v218 offset:16904
v_mfma_f32_16x16x32_bf16 v[16:19], v[144:147], a[16:19], v[16:19]
ds_read_b64_tr_b16 v[142:143], v218 offset:17160
v_mfma_f32_16x16x32_bf16 v[48:51], v[144:147], a[20:23], v[48:51]
v_mfma_f32_16x16x32_bf16 v[80:83], v[144:147], a[24:27], v[80:83]
v_mfma_f32_16x16x32_bf16 v[112:115], v[144:147], a[28:31], v[112:115]
ds_read_b64_tr_b16 v[144:145], v218 offset:17408
ds_read_b64_tr_b16 v[146:147], v218 offset:17664
v_mfma_f32_16x16x32_bf16 v[116:119], v[148:151], a[28:31], v[116:119]
buffer_load_dwordx4 a[68:71], v192, s[36:39], s69 offen offset:1024 sc1
v_mfma_f32_16x16x32_bf16 v[84:87], v[148:151], a[24:27], v[84:87]
v_mfma_f32_16x16x32_bf16 v[52:55], v[148:151], a[20:23], v[52:55]
v_mfma_f32_16x16x32_bf16 v[20:23], v[148:151], a[16:19], v[20:23]
ds_read_b64_tr_b16 v[148:149], v218 offset:17416
ds_read_b64_tr_b16 v[150:151], v218 offset:17672
v_mfma_f32_16x16x32_bf16 v[24:27], v[152:155], a[16:19], v[24:27]
v_mfma_f32_16x16x32_bf16 v[56:59], v[152:155], a[20:23], v[56:59]
buffer_load_dwordx4 a[72:75], v193, s[36:39], s69 offen offset:0 sc1
v_mfma_f32_16x16x32_bf16 v[88:91], v[152:155], a[24:27], v[88:91]
v_mfma_f32_16x16x32_bf16 v[120:123], v[152:155], a[28:31], v[120:123]
ds_read_b64_tr_b16 v[152:153], v218 offset:17920
ds_read_b64_tr_b16 v[154:155], v218 offset:18176
v_mfma_f32_16x16x32_bf16 v[124:127], v[156:159], a[28:31], v[124:127]
v_mfma_f32_16x16x32_bf16 v[92:95], v[156:159], a[24:27], v[92:95]
v_mfma_f32_16x16x32_bf16 v[60:63], v[156:159], a[20:23], v[60:63]
buffer_load_dwordx4 a[76:79], v193, s[36:39], s69 offen offset:1024 sc1
v_mfma_f32_16x16x32_bf16 v[28:31], v[156:159], a[16:19], v[28:31]
s_waitcnt vmcnt(13) lgkmcnt(6)
ds_read_b64_tr_b16 v[156:157], v218 offset:17928
v_mfma_f32_16x16x32_bf16 v[0:3], v[128:131], a[32:35], v[0:3]
ds_read_b64_tr_b16 v[158:159], v218 offset:18184
s_add_u32 s69, s69, 4096
v_mfma_f32_16x16x32_bf16 v[32:35], v[128:131], a[36:39], v[32:35]
v_mfma_f32_16x16x32_bf16 v[64:67], v[128:131], a[40:43], v[64:67]
v_mfma_f32_16x16x32_bf16 v[96:99], v[128:131], a[44:47], v[96:99]
ds_read_b64_tr_b16 v[128:129], v218 offset:24576
ds_read_b64_tr_b16 v[130:131], v218 offset:24832
v_mfma_f32_16x16x32_bf16 v[100:103], v[132:135], a[44:47], v[100:103]
buffer_load_dwordx4 a[80:83], v192, s[36:39], s69 offen offset:0 sc1
v_mfma_f32_16x16x32_bf16 v[68:71], v[132:135], a[40:43], v[68:71]
v_mfma_f32_16x16x32_bf16 v[36:39], v[132:135], a[36:39], v[36:39]
v_mfma_f32_16x16x32_bf16 v[4:7], v[132:135], a[32:35], v[4:7]
ds_read_b64_tr_b16 v[132:133], v218 offset:24584
ds_read_b64_tr_b16 v[134:135], v218 offset:24840
v_mfma_f32_16x16x32_bf16 v[8:11], v[136:139], a[32:35], v[8:11]
v_mfma_f32_16x16x32_bf16 v[40:43], v[136:139], a[36:39], v[40:43]
buffer_load_dwordx4 a[84:87], v192, s[36:39], s69 offen offset:1024 sc1
v_mfma_f32_16x16x32_bf16 v[72:75], v[136:139], a[40:43], v[72:75]
v_mfma_f32_16x16x32_bf16 v[104:107], v[136:139], a[44:47], v[104:107]
ds_read_b64_tr_b16 v[136:137], v218 offset:25088
ds_read_b64_tr_b16 v[138:139], v218 offset:25344
v_mfma_f32_16x16x32_bf16 v[108:111], v[140:143], a[44:47], v[108:111]
v_mfma_f32_16x16x32_bf16 v[76:79], v[140:143], a[40:43], v[76:79]
v_mfma_f32_16x16x32_bf16 v[44:47], v[140:143], a[36:39], v[44:47]
buffer_load_dwordx4 a[88:91], v193, s[36:39], s69 offen offset:0 sc1
v_mfma_f32_16x16x32_bf16 v[12:15], v[140:143], a[32:35], v[12:15]
s_waitcnt lgkmcnt(6)
ds_read_b64_tr_b16 v[140:141], v218 offset:25096
v_mfma_f32_16x16x32_bf16 v[16:19], v[144:147], a[32:35], v[16:19]
ds_read_b64_tr_b16 v[142:143], v218 offset:25352
s_mov_b32 s76, 32601856
v_mfma_f32_16x16x32_bf16 v[48:51], v[144:147], a[36:39], v[48:51]
v_mfma_f32_16x16x32_bf16 v[80:83], v[144:147], a[40:43], v[80:83]
v_mfma_f32_16x16x32_bf16 v[112:115], v[144:147], a[44:47], v[112:115]
ds_read_b64_tr_b16 v[144:145], v218 offset:25600
ds_read_b64_tr_b16 v[146:147], v218 offset:25856
v_mfma_f32_16x16x32_bf16 v[116:119], v[148:151], a[44:47], v[116:119]
buffer_load_dwordx4 v[172:175], v170, s[36:39], s76 offen sc1
v_mfma_f32_16x16x32_bf16 v[84:87], v[148:151], a[40:43], v[84:87]
s_mov_b32 s76, 32632576
v_mfma_f32_16x16x32_bf16 v[52:55], v[148:151], a[36:39], v[52:55]
v_mfma_f32_16x16x32_bf16 v[20:23], v[148:151], a[32:35], v[20:23]
ds_read_b64_tr_b16 v[148:149], v218 offset:25608
ds_read_b64_tr_b16 v[150:151], v218 offset:25864
v_mfma_f32_16x16x32_bf16 v[24:27], v[152:155], a[32:35], v[24:27]
v_mfma_f32_16x16x32_bf16 v[56:59], v[152:155], a[36:39], v[56:59]
buffer_load_dwordx4 v[176:179], v170, s[36:39], s76 offen sc1
v_mfma_f32_16x16x32_bf16 v[88:91], v[152:155], a[40:43], v[88:91]
s_mov_b32 s76, 32663296
v_mfma_f32_16x16x32_bf16 v[120:123], v[152:155], a[44:47], v[120:123]
ds_read_b64_tr_b16 v[152:153], v218 offset:26112
ds_read_b64_tr_b16 v[154:155], v218 offset:26368
v_mfma_f32_16x16x32_bf16 v[124:127], v[156:159], a[44:47], v[124:127]
v_mfma_f32_16x16x32_bf16 v[92:95], v[156:159], a[40:43], v[92:95]
v_mfma_f32_16x16x32_bf16 v[60:63], v[156:159], a[36:39], v[60:63]
buffer_load_dwordx4 v[180:183], v170, s[36:39], s76 offen sc1
v_mfma_f32_16x16x32_bf16 v[28:31], v[156:159], a[32:35], v[28:31]
s_waitcnt vmcnt(10) lgkmcnt(6)
ds_read_b64_tr_b16 v[156:157], v218 offset:26120
v_mfma_f32_16x16x32_bf16 v[0:3], v[128:131], a[48:51], v[0:3]
ds_read_b64_tr_b16 v[158:159], v218 offset:26376
s_mov_b32 s76, 32694016
v_mfma_f32_16x16x32_bf16 v[32:35], v[128:131], a[52:55], v[32:35]
v_mfma_f32_16x16x32_bf16 v[64:67], v[128:131], a[56:59], v[64:67]
v_mfma_f32_16x16x32_bf16 v[96:99], v[128:131], a[60:63], v[96:99]
v_mfma_f32_16x16x32_bf16 v[100:103], v[132:135], a[60:63], v[100:103]
buffer_load_dwordx4 v[184:187], v170, s[36:39], s76 offen sc1
v_mfma_f32_16x16x32_bf16 v[68:71], v[132:135], a[56:59], v[68:71]
v_mfma_f32_16x16x32_bf16 v[36:39], v[132:135], a[52:55], v[36:39]
s_barrier
v_mfma_f32_16x16x32_bf16 v[4:7], v[132:135], a[48:51], v[4:7]
ds_read_b64_tr_b16 v[128:129], v218 offset:32768
ds_read_b64_tr_b16 v[130:131], v218 offset:33024
v_mfma_f32_16x16x32_bf16 v[8:11], v[136:139], a[48:51], v[8:11]
ds_read_b64_tr_b16 v[132:133], v218 offset:32776
ds_read_b64_tr_b16 v[134:135], v218 offset:33032
v_mfma_f32_16x16x32_bf16 v[40:43], v[136:139], a[52:55], v[40:43]
ds_read_b64_tr_b16 a[192:193], v160 offset:0
ds_read_b64_tr_b16 a[194:195], v160 offset:10240
v_mfma_f32_16x16x32_bf16 v[72:75], v[136:139], a[56:59], v[72:75]
buffer_load_dwordx4 a[92:95], v193, s[36:39], s69 offen offset:1024 sc1
v_mfma_f32_16x16x32_bf16 v[104:107], v[136:139], a[60:63], v[104:107]
s_add_u32 s69, s69, 4096
ds_read_b64_tr_b16 v[136:137], v218 offset:33280
v_mfma_f32_16x16x32_bf16 v[108:111], v[140:143], a[60:63], v[108:111]
ds_read_b64_tr_b16 v[138:139], v218 offset:33536
ds_read_b64_tr_b16 a[196:197], v161 offset:0
v_mfma_f32_16x16x32_bf16 v[76:79], v[140:143], a[56:59], v[76:79]
ds_read_b64_tr_b16 a[198:199], v161 offset:10240
ds_read_b64_tr_b16 a[200:201], v162 offset:0
v_mfma_f32_16x16x32_bf16 v[44:47], v[140:143], a[52:55], v[44:47]
buffer_load_dwordx4 a[96:99], v192, s[36:39], s69 offen offset:0 sc1
v_mfma_f32_16x16x32_bf16 v[12:15], v[140:143], a[48:51], v[12:15]
s_waitcnt lgkmcnt(11)
ds_read_b64_tr_b16 v[140:141], v218 offset:33288
v_mfma_f32_16x16x32_bf16 v[16:19], v[144:147], a[48:51], v[16:19]
ds_read_b64_tr_b16 v[142:143], v218 offset:33544
ds_read_b64_tr_b16 a[202:203], v162 offset:10240
v_mfma_f32_16x16x32_bf16 v[48:51], v[144:147], a[52:55], v[48:51]
ds_read_b64_tr_b16 a[204:205], v163 offset:0
ds_read_b64_tr_b16 a[206:207], v163 offset:10240
v_mfma_f32_16x16x32_bf16 v[80:83], v[144:147], a[56:59], v[80:83]
ds_read_b64_tr_b16 a[208:209], v164 offset:0
ds_read_b64_tr_b16 a[210:211], v164 offset:10240
v_mfma_f32_16x16x32_bf16 v[112:115], v[144:147], a[60:63], v[112:115]
ds_read_b64_tr_b16 v[144:145], v218 offset:33792
ds_read_b64_tr_b16 v[146:147], v218 offset:34048
v_mfma_f32_16x16x32_bf16 v[116:119], v[148:151], a[60:63], v[116:119]
buffer_load_dwordx4 a[100:103], v192, s[36:39], s69 offen offset:1024 sc1
v_mfma_f32_16x16x32_bf16 v[84:87], v[148:151], a[56:59], v[84:87]
v_mfma_f32_16x16x32_bf16 v[52:55], v[148:151], a[52:55], v[52:55]
v_mfma_f32_16x16x32_bf16 v[20:23], v[148:151], a[48:51], v[20:23]
ds_read_b64_tr_b16 v[148:149], v218 offset:33800
ds_read_b64_tr_b16 v[150:151], v218 offset:34056
v_mfma_f32_16x16x32_bf16 v[24:27], v[152:155], a[48:51], v[24:27]
v_mfma_f32_16x16x32_bf16 v[56:59], v[152:155], a[52:55], v[56:59]
buffer_load_dwordx4 a[104:107], v193, s[36:39], s69 offen offset:0 sc1
v_mfma_f32_16x16x32_bf16 v[88:91], v[152:155], a[56:59], v[88:91]
v_mfma_f32_16x16x32_bf16 v[120:123], v[152:155], a[60:63], v[120:123]
ds_read_b64_tr_b16 v[152:153], v218 offset:34304
ds_read_b64_tr_b16 v[154:155], v218 offset:34560
v_mfma_f32_16x16x32_bf16 v[124:127], v[156:159], a[60:63], v[124:127]
v_mfma_f32_16x16x32_bf16 v[92:95], v[156:159], a[56:59], v[92:95]
v_mfma_f32_16x16x32_bf16 v[60:63], v[156:159], a[52:55], v[60:63]
buffer_load_dwordx4 a[108:111], v193, s[36:39], s69 offen offset:1024 sc1
v_mfma_f32_16x16x32_bf16 v[28:31], v[156:159], a[48:51], v[28:31]
s_waitcnt vmcnt(12) lgkmcnt(11)
ds_read_b64_tr_b16 v[156:157], v218 offset:34312
v_mfma_f32_16x16x32_bf16 v[0:3], v[128:131], a[64:67], v[0:3]
ds_read_b64_tr_b16 v[158:159], v218 offset:34568
s_add_u32 s69, s69, 4096
v_mfma_f32_16x16x32_bf16 v[32:35], v[128:131], a[68:71], v[32:35]
v_mfma_f32_16x16x32_bf16 v[64:67], v[128:131], a[72:75], v[64:67]
v_mfma_f32_16x16x32_bf16 v[96:99], v[128:131], a[76:79], v[96:99]
ds_read_b64_tr_b16 v[128:129], v218 offset:40960
ds_read_b64_tr_b16 v[130:131], v218 offset:41216
v_mfma_f32_16x16x32_bf16 v[100:103], v[132:135], a[76:79], v[100:103]
buffer_load_dwordx4 a[112:115], v192, s[36:39], s69 offen offset:0 sc1
v_mfma_f32_16x16x32_bf16 v[68:71], v[132:135], a[72:75], v[68:71]
v_mfma_f32_16x16x32_bf16 v[36:39], v[132:135], a[68:71], v[36:39]
v_mfma_f32_16x16x32_bf16 v[4:7], v[132:135], a[64:67], v[4:7]
ds_read_b64_tr_b16 v[132:133], v218 offset:40968
ds_read_b64_tr_b16 v[134:135], v218 offset:41224
v_mfma_f32_16x16x32_bf16 v[8:11], v[136:139], a[64:67], v[8:11]
v_mfma_f32_16x16x32_bf16 v[40:43], v[136:139], a[68:71], v[40:43]
buffer_load_dwordx4 a[116:119], v192, s[36:39], s69 offen offset:1024 sc1
v_mfma_f32_16x16x32_bf16 v[72:75], v[136:139], a[72:75], v[72:75]
v_mfma_f32_16x16x32_bf16 v[104:107], v[136:139], a[76:79], v[104:107]
ds_read_b64_tr_b16 v[136:137], v218 offset:41472
ds_read_b64_tr_b16 v[138:139], v218 offset:41728
v_mfma_f32_16x16x32_bf16 v[108:111], v[140:143], a[76:79], v[108:111]
v_mfma_f32_16x16x32_bf16 v[76:79], v[140:143], a[72:75], v[76:79]
v_mfma_f32_16x16x32_bf16 v[44:47], v[140:143], a[68:71], v[44:47]
buffer_load_dwordx4 a[120:123], v193, s[36:39], s69 offen offset:0 sc1
v_mfma_f32_16x16x32_bf16 v[12:15], v[140:143], a[64:67], v[12:15]
s_waitcnt lgkmcnt(6)
ds_read_b64_tr_b16 v[140:141], v218 offset:41480
v_mfma_f32_16x16x32_bf16 v[16:19], v[144:147], a[64:67], v[16:19]
ds_read_b64_tr_b16 v[142:143], v218 offset:41736
v_mfma_f32_16x16x32_bf16 v[48:51], v[144:147], a[68:71], v[48:51]
v_mfma_f32_16x16x32_bf16 v[80:83], v[144:147], a[72:75], v[80:83]
buffer_store_dwordx4 a[192:195], v208, s[36:39], s84 offen offset:0 sc1
v_mfma_f32_16x16x32_bf16 v[112:115], v[144:147], a[76:79], v[112:115]
ds_read_b64_tr_b16 v[144:145], v218 offset:41984
ds_read_b64_tr_b16 v[146:147], v218 offset:42240
v_mfma_f32_16x16x32_bf16 v[116:119], v[148:151], a[76:79], v[116:119]
v_mfma_f32_16x16x32_bf16 v[84:87], v[148:151], a[72:75], v[84:87]
v_mfma_f32_16x16x32_bf16 v[52:55], v[148:151], a[68:71], v[52:55]
buffer_load_dwordx4 a[124:127], v193, s[36:39], s69 offen offset:1024 sc1
v_mfma_f32_16x16x32_bf16 v[20:23], v[148:151], a[64:67], v[20:23]
s_add_u32 s69, s69, 4096
ds_read_b64_tr_b16 v[148:149], v218 offset:41992
v_mfma_f32_16x16x32_bf16 v[24:27], v[152:155], a[64:67], v[24:27]
ds_read_b64_tr_b16 v[150:151], v218 offset:42248
v_mfma_f32_16x16x32_bf16 v[56:59], v[152:155], a[68:71], v[56:59]
v_mfma_f32_16x16x32_bf16 v[88:91], v[152:155], a[72:75], v[88:91]
buffer_load_dwordx4 a[128:131], v192, s[36:39], s69 offen offset:0 sc1
v_mfma_f32_16x16x32_bf16 v[120:123], v[152:155], a[76:79], v[120:123]
ds_read_b64_tr_b16 v[152:153], v218 offset:42496
ds_read_b64_tr_b16 v[154:155], v218 offset:42752
v_mfma_f32_16x16x32_bf16 v[124:127], v[156:159], a[76:79], v[124:127]
v_mfma_f32_16x16x32_bf16 v[92:95], v[156:159], a[72:75], v[92:95]
v_mfma_f32_16x16x32_bf16 v[60:63], v[156:159], a[68:71], v[60:63]
buffer_load_dwordx4 a[132:135], v192, s[36:39], s69 offen offset:1024 sc1
v_mfma_f32_16x16x32_bf16 v[28:31], v[156:159], a[64:67], v[28:31]
s_waitcnt vmcnt(11) lgkmcnt(6)
ds_read_b64_tr_b16 v[156:157], v218 offset:42504
v_mfma_f32_16x16x32_bf16 v[0:3], v[128:131], a[80:83], v[0:3]
ds_read_b64_tr_b16 v[158:159], v218 offset:42760
v_xor_b32 v190, s80, v173
v_mfma_f32_16x16x32_bf16 v[32:35], v[128:131], a[84:87], v[32:35]
v_xor_b32 v191, s81, v172
v_bitop3_b32 v190, v190, v175, s80 bitop3:0xf6
v_mfma_f32_16x16x32_bf16 v[64:67], v[128:131], a[88:91], v[64:67]
v_bitop3_b32 v191, v191, v174, s81 bitop3:0xf6
v_bitop3_b32 v190, v190, v177, s80 bitop3:0xf6
v_mfma_f32_16x16x32_bf16 v[96:99], v[128:131], a[92:95], v[96:99]
ds_read_b64_tr_b16 v[128:129], v218 offset:49152
ds_read_b64_tr_b16 v[130:131], v218 offset:49408
v_mfma_f32_16x16x32_bf16 v[100:103], v[132:135], a[92:95], v[100:103]
buffer_load_dwordx4 a[136:139], v193, s[36:39], s69 offen offset:0 sc1
v_mfma_f32_16x16x32_bf16 v[68:71], v[132:135], a[88:91], v[68:71]
v_bitop3_b32 v191, v191, v176, s81 bitop3:0xf6
v_bitop3_b32 v190, v190, v179, s80 bitop3:0xf6
v_mfma_f32_16x16x32_bf16 v[36:39], v[132:135], a[84:87], v[36:39]
v_bitop3_b32 v191, v191, v178, s81 bitop3:0xf6
v_bitop3_b32 v190, v190, v181, s80 bitop3:0xf6
v_mfma_f32_16x16x32_bf16 v[4:7], v[132:135], a[80:83], v[4:7]
ds_read_b64_tr_b16 v[132:133], v218 offset:49160
ds_read_b64_tr_b16 v[134:135], v218 offset:49416
v_mfma_f32_16x16x32_bf16 v[8:11], v[136:139], a[80:83], v[8:11]
v_bitop3_b32 v191, v191, v180, s81 bitop3:0xf6
v_bitop3_b32 v190, v190, v183, s80 bitop3:0xf6
v_mfma_f32_16x16x32_bf16 v[40:43], v[136:139], a[84:87], v[40:43]
buffer_load_dwordx4 a[140:143], v193, s[36:39], s69 offen offset:1024 sc1
v_mfma_f32_16x16x32_bf16 v[72:75], v[136:139], a[88:91], v[72:75]
s_add_u32 s69, s69, 4096
v_bitop3_b32 v191, v191, v182, s81 bitop3:0xf6
v_mfma_f32_16x16x32_bf16 v[104:107], v[136:139], a[92:95], v[104:107]
ds_read_b64_tr_b16 v[136:137], v218 offset:49664
ds_read_b64_tr_b16 v[138:139], v218 offset:49920
v_mfma_f32_16x16x32_bf16 v[108:111], v[140:143], a[92:95], v[108:111]
v_bitop3_b32 v190, v190, v185, s80 bitop3:0xf6
v_bitop3_b32 v191, v191, v184, s81 bitop3:0xf6
v_mfma_f32_16x16x32_bf16 v[76:79], v[140:143], a[88:91], v[76:79]
v_bitop3_b32 v190, v190, v187, s80 bitop3:0xf6
v_bitop3_b32 v191, v191, v186, s81 bitop3:0xf6
v_mfma_f32_16x16x32_bf16 v[44:47], v[140:143], a[84:87], v[44:47]
buffer_load_dwordx4 a[144:147], v192, s[36:39], s69 offen offset:0 sc1
v_mfma_f32_16x16x32_bf16 v[12:15], v[140:143], a[80:83], v[12:15]
s_waitcnt lgkmcnt(6)
ds_read_b64_tr_b16 v[140:141], v218 offset:49672
v_mfma_f32_16x16x32_bf16 v[16:19], v[144:147], a[80:83], v[16:19]
ds_read_b64_tr_b16 v[142:143], v218 offset:49928
v_and_b32 v191, 0xff, v191
v_mfma_f32_16x16x32_bf16 v[48:51], v[144:147], a[84:87], v[48:51]
v_or_b32 v190, v190, v191
v_mfma_f32_16x16x32_bf16 v[80:83], v[144:147], a[88:91], v[80:83]
v_mfma_f32_16x16x32_bf16 v[112:115], v[144:147], a[92:95], v[112:115]
ds_read_b64_tr_b16 v[144:145], v218 offset:50176
ds_read_b64_tr_b16 v[146:147], v218 offset:50432
v_mfma_f32_16x16x32_bf16 v[116:119], v[148:151], a[92:95], v[116:119]
buffer_store_dwordx4 a[196:199], v208, s[36:39], s84 offen offset:1024 sc1
v_mfma_f32_16x16x32_bf16 v[84:87], v[148:151], a[88:91], v[84:87]
v_mfma_f32_16x16x32_bf16 v[52:55], v[148:151], a[84:87], v[52:55]
v_mfma_f32_16x16x32_bf16 v[20:23], v[148:151], a[80:83], v[20:23]
ds_read_b64_tr_b16 v[148:149], v218 offset:50184
ds_read_b64_tr_b16 v[150:151], v218 offset:50440
v_mfma_f32_16x16x32_bf16 v[24:27], v[152:155], a[80:83], v[24:27]
v_mfma_f32_16x16x32_bf16 v[56:59], v[152:155], a[84:87], v[56:59]
buffer_load_dwordx4 a[148:151], v192, s[36:39], s69 offen offset:1024 sc1
v_mfma_f32_16x16x32_bf16 v[88:91], v[152:155], a[88:91], v[88:91]
v_mfma_f32_16x16x32_bf16 v[120:123], v[152:155], a[92:95], v[120:123]
ds_read_b64_tr_b16 v[152:153], v218 offset:50688
ds_read_b64_tr_b16 v[154:155], v218 offset:50944
v_mfma_f32_16x16x32_bf16 v[124:127], v[156:159], a[92:95], v[124:127]
v_mfma_f32_16x16x32_bf16 v[92:95], v[156:159], a[88:91], v[92:95]
v_mfma_f32_16x16x32_bf16 v[60:63], v[156:159], a[84:87], v[60:63]
v_cmp_ne_u32 vcc, 0, v190
s_nop 0
s_cbranch_vccz .Lk_g2_ok
s_mov_b32 s89, 0
.Lk_g2:
s_mov_b32 s76, 32601856
buffer_load_dwordx4 v[172:175], v170, s[36:39], s76 offen sc1
s_mov_b32 s76, 32632576
buffer_load_dwordx4 v[176:179], v170, s[36:39], s76 offen sc1
s_mov_b32 s76, 32663296
buffer_load_dwordx4 v[180:183], v170, s[36:39], s76 offen sc1
s_mov_b32 s76, 32694016
buffer_load_dwordx4 v[184:187], v170, s[36:39], s76 offen sc1
s_waitcnt vmcnt(0)
v_xor_b32 v190, s80, v173
v_xor_b32 v191, s81, v172
v_bitop3_b32 v190, v190, v175, s80 bitop3:0xf6
v_bitop3_b32 v191, v191, v174, s81 bitop3:0xf6
v_bitop3_b32 v190, v190, v177, s80 bitop3:0xf6
v_bitop3_b32 v191, v191, v176, s81 bitop3:0xf6
v_bitop3_b32 v190, v190, v179, s80 bitop3:0xf6
v_bitop3_b32 v191, v191, v178, s81 bitop3:0xf6
v_bitop3_b32 v190, v190, v181, s80 bitop3:0xf6
v_bitop3_b32 v191, v191, v180, s81 bitop3:0xf6
v_bitop3_b32 v190, v190, v183, s80 bitop3:0xf6
v_bitop3_b32 v191, v191, v182, s81 bitop3:0xf6
v_bitop3_b32 v190, v190, v185, s80 bitop3:0xf6
v_bitop3_b32 v191, v191, v184, s81 bitop3:0xf6
v_bitop3_b32 v190, v190, v187, s80 bitop3:0xf6
v_bitop3_b32 v191, v191, v186, s81 bitop3:0xf6
v_and_b32 v191, 0xff, v191
v_or_b32 v190, v190, v191
v_cmp_ne_u32 vcc, 0, v190
s_nop 0
s_cbranch_vccz .Lk_g2_ok
s_add_u32 s89, s89, 1
s_cmp_lt_u32 s89, 0x20000
s_cbranch_scc1 .Lk_g2
.Lk_g2_ok:
v_mfma_f32_16x16x32_bf16 v[28:31], v[156:159], a[80:83], v[28:31]
s_waitcnt vmcnt(12) lgkmcnt(6)
ds_read_b64_tr_b16 v[156:157], v218 offset:50696
v_mfma_f32_16x16x32_bf16 v[0:3], v[128:131], a[96:99], v[0:3]
buffer_load_dwordx4 a[152:155], v193, s[36:39], s69 offen offset:0 sc1
v_mfma_f32_16x16x32_bf16 v[32:35], v[128:131], a[100:103], v[32:35]
ds_read_b64_tr_b16 v[158:159], v218 offset:50952
v_and_b32 v172, 0xffffff00, v172
v_mfma_f32_16x16x32_bf16 v[64:67], v[128:131], a[104:107], v[64:67]
v_and_b32 v174, 0xffffff00, v174
v_and_b32 v176, 0xffffff00, v176
v_mfma_f32_16x16x32_bf16 v[96:99], v[128:131], a[108:111], v[96:99]
ds_read_b64_tr_b16 v[128:129], v218 offset:57344
ds_read_b64_tr_b16 v[130:131], v218 offset:57600
v_mfma_f32_16x16x32_bf16 v[100:103], v[132:135], a[108:111], v[100:103]
v_and_b32 v178, 0xffffff00, v178
v_and_b32 v180, 0xffffff00, v180
v_mfma_f32_16x16x32_bf16 v[68:71], v[132:135], a[104:107], v[68:71]
buffer_load_dwordx4 a[156:159], v193, s[36:39], s69 offen offset:1024 sc1
v_mfma_f32_16x16x32_bf16 v[36:39], v[132:135], a[100:103], v[36:39]
s_add_u32 s69, s69, 4096
v_and_b32 v182, 0xffffff00, v182
v_mfma_f32_16x16x32_bf16 v[4:7], v[132:135], a[96:99], v[4:7]
ds_read_b64_tr_b16 v[132:133], v218 offset:57352
ds_read_b64_tr_b16 v[134:135], v218 offset:57608
v_mfma_f32_16x16x32_bf16 v[8:11], v[136:139], a[96:99], v[8:11]
v_and_b32 v184, 0xffffff00, v184
v_and_b32 v186, 0xffffff00, v186
v_mfma_f32_16x16x32_bf16 v[40:43], v[136:139], a[100:103], v[40:43]
buffer_load_dwordx4 a[160:163], v192, s[36:39], s69 offen offset:0 sc1
v_mfma_f32_16x16x32_bf16 v[72:75], v[136:139], a[104:107], v[72:75]
v_add_f32 v172, v172, v176
v_add_f32 v174, v174, v178
v_mfma_f32_16x16x32_bf16 v[104:107], v[136:139], a[108:111], v[104:107]
ds_read_b64_tr_b16 v[136:137], v218 offset:57856
ds_read_b64_tr_b16 v[138:139], v218 offset:58112
v_mfma_f32_16x16x32_bf16 v[108:111], v[140:143], a[108:111], v[108:111]
v_add_f32 v172, v172, v180
v_add_f32 v174, v174, v182
v_mfma_f32_16x16x32_bf16 v[76:79], v[140:143], a[104:107], v[76:79]
v_add_f32 v172, v172, v184
v_add_f32 v174, v174, v186
v_mfma_f32_16x16x32_bf16 v[44:47], v[140:143], a[100:103], v[44:47]
buffer_load_dwordx4 a[164:167], v192, s[36:39], s69 offen offset:1024 sc1
v_mfma_f32_16x16x32_bf16 v[12:15], v[140:143], a[96:99], v[12:15]
s_waitcnt lgkmcnt(6)
ds_read_b64_tr_b16 v[140:141], v218 offset:57864
v_mfma_f32_16x16x32_bf16 v[16:19], v[144:147], a[96:99], v[16:19]
ds_read_b64_tr_b16 v[142:143], v218 offset:58120
v_cvt_pk_bf16_f32 v173, v172, v174
v_mfma_f32_16x16x32_bf16 v[48:51], v[144:147], a[100:103], v[48:51]
v_lshlrev_b32 v172, 16, v173
v_and_b32 v174, 0xffff0000, v173
v_mfma_f32_16x16x32_bf16 v[80:83], v[144:147], a[104:107], v[80:83]
v_add_f32 v172, v172, v189
v_add_f32 v174, v174, v189
v_mfma_f32_16x16x32_bf16 v[112:115], v[144:147], a[108:111], v[112:115]
ds_read_b64_tr_b16 v[144:145], v218 offset:58368
ds_read_b64_tr_b16 v[146:147], v218 offset:58624
v_mfma_f32_16x16x32_bf16 v[116:119], v[148:151], a[108:111], v[116:119]
buffer_store_dwordx4 a[200:203], v208, s[36:39], s84 offen offset:2048 sc1
v_mfma_f32_16x16x32_bf16 v[84:87], v[148:151], a[104:107], v[84:87]
v_cvt_pk_bf16_f32 v173, v172, v174
v_mfma_f32_16x16x32_bf16 v[52:55], v[148:151], a[100:103], v[52:55]
ds_write_b32 v171, v173
v_mfma_f32_16x16x32_bf16 v[20:23], v[148:151], a[96:99], v[20:23]
ds_read_b64_tr_b16 v[148:149], v218 offset:58376
ds_read_b64_tr_b16 v[150:151], v218 offset:58632
v_mfma_f32_16x16x32_bf16 v[24:27], v[152:155], a[96:99], v[24:27]
v_mfma_f32_16x16x32_bf16 v[56:59], v[152:155], a[100:103], v[56:59]
buffer_load_dwordx4 a[168:171], v193, s[36:39], s69 offen offset:0 sc1
v_mfma_f32_16x16x32_bf16 v[88:91], v[152:155], a[104:107], v[88:91]
v_mfma_f32_16x16x32_bf16 v[120:123], v[152:155], a[108:111], v[120:123]
ds_read_b64_tr_b16 v[152:153], v218 offset:58880
ds_read_b64_tr_b16 v[154:155], v218 offset:59136
v_mfma_f32_16x16x32_bf16 v[124:127], v[156:159], a[108:111], v[124:127]
v_mfma_f32_16x16x32_bf16 v[92:95], v[156:159], a[104:107], v[92:95]
v_mfma_f32_16x16x32_bf16 v[60:63], v[156:159], a[100:103], v[60:63]
buffer_load_dwordx4 a[172:175], v193, s[36:39], s69 offen offset:1024 sc1
v_mfma_f32_16x16x32_bf16 v[28:31], v[156:159], a[96:99], v[28:31]
s_waitcnt vmcnt(14) lgkmcnt(7)
ds_read_b64_tr_b16 v[156:157], v218 offset:58888
v_mfma_f32_16x16x32_bf16 v[0:3], v[128:131], a[112:115], v[0:3]
ds_read_b64_tr_b16 v[158:159], v218 offset:59144
s_add_u32 s69, s69, 4096
v_mfma_f32_16x16x32_bf16 v[32:35], v[128:131], a[116:119], v[32:35]
v_mfma_f32_16x16x32_bf16 v[64:67], v[128:131], a[120:123], v[64:67]
v_mfma_f32_16x16x32_bf16 v[96:99], v[128:131], a[124:127], v[96:99]
v_mfma_f32_16x16x32_bf16 v[100:103], v[132:135], a[124:127], v[100:103]
buffer_load_dwordx4 a[176:179], v192, s[36:39], s69 offen offset:0 sc1
v_mfma_f32_16x16x32_bf16 v[68:71], v[132:135], a[120:123], v[68:71]
v_mfma_f32_16x16x32_bf16 v[36:39], v[132:135], a[116:119], v[36:39]
v_add_u32 v218, 0x10000, v218
ds_read_b64_tr_b16 v[128:129], v218 offset:0
v_mfma_f32_16x16x32_bf16 v[4:7], v[132:135], a[112:115], v[4:7]
ds_read_b64_tr_b16 v[130:131], v218 offset:256
ds_read_b64_tr_b16 v[132:133], v218 offset:8
v_mfma_f32_16x16x32_bf16 v[8:11], v[136:139], a[112:115], v[8:11]
ds_read_b64_tr_b16 v[134:135], v218 offset:264
v_mfma_f32_16x16x32_bf16 v[40:43], v[136:139], a[116:119], v[40:43]
buffer_load_dwordx4 a[180:183], v192, s[36:39], s69 offen offset:1024 sc1
v_mfma_f32_16x16x32_bf16 v[72:75], v[136:139], a[120:123], v[72:75]
v_mfma_f32_16x16x32_bf16 v[104:107], v[136:139], a[124:127], v[104:107]
ds_read_b64_tr_b16 v[136:137], v218 offset:512
ds_read_b64_tr_b16 v[138:139], v218 offset:768
v_mfma_f32_16x16x32_bf16 v[108:111], v[140:143], a[124:127], v[108:111]
v_mfma_f32_16x16x32_bf16 v[76:79], v[140:143], a[120:123], v[76:79]
v_mfma_f32_16x16x32_bf16 v[44:47], v[140:143], a[116:119], v[44:47]
buffer_load_dwordx4 a[184:187], v193, s[36:39], s69 offen offset:0 sc1
v_mfma_f32_16x16x32_bf16 v[12:15], v[140:143], a[112:115], v[12:15]
s_waitcnt lgkmcnt(6)
ds_read_b64_tr_b16 v[140:141], v218 offset:520
v_mfma_f32_16x16x32_bf16 v[16:19], v[144:147], a[112:115], v[16:19]
ds_read_b64_tr_b16 v[142:143], v218 offset:776
v_mfma_f32_16x16x32_bf16 v[48:51], v[144:147], a[116:119], v[48:51]
s_mov_b32 s46, 16777216
v_mfma_f32_16x16x32_bf16 v[80:83], v[144:147], a[120:123], v[80:83]
s_cmp_lt_u32 s64, 2
s_cselect_b64 s[44:45], s[10:11], s[12:13]
v_mfma_f32_16x16x32_bf16 v[112:115], v[144:147], a[124:127], v[112:115]
s_and_b32 s45, s45, 0xffff
ds_read_b64_tr_b16 v[144:145], v218 offset:1024
v_mfma_f32_16x16x32_bf16 v[116:119], v[148:151], a[124:127], v[116:119]
ds_read_b64_tr_b16 v[146:147], v218 offset:1280
v_mfma_f32_16x16x32_bf16 v[84:87], v[148:151], a[120:123], v[84:87]
buffer_store_dwordx4 a[204:207], v208, s[36:39], s84 offen offset:3072 sc1
v_mfma_f32_16x16x32_bf16 v[52:55], v[148:151], a[116:119], v[52:55]
v_mfma_f32_16x16x32_bf16 v[20:23], v[148:151], a[112:115], v[20:23]
ds_read_b64_tr_b16 v[148:149], v218 offset:1032
ds_read_b64_tr_b16 v[150:151], v218 offset:1288
v_mfma_f32_16x16x32_bf16 v[24:27], v[152:155], a[112:115], v[24:27]
v_mfma_f32_16x16x32_bf16 v[56:59], v[152:155], a[116:119], v[56:59]
buffer_load_dwordx4 a[188:191], v193, s[36:39], s69 offen offset:1024 sc1
v_mfma_f32_16x16x32_bf16 v[88:91], v[152:155], a[120:123], v[88:91]
s_add_u32 m0, s71, 114688
s_add_u32 s75, s70, 3670016
v_mfma_f32_16x16x32_bf16 v[120:123], v[152:155], a[124:127], v[120:123]
ds_read_b64_tr_b16 v[152:153], v218 offset:1536
ds_read_b64_tr_b16 v[154:155], v218 offset:1792
v_mfma_f32_16x16x32_bf16 v[124:127], v[156:159], a[124:127], v[124:127]
v_mfma_f32_16x16x32_bf16 v[92:95], v[156:159], a[120:123], v[92:95]
v_mfma_f32_16x16x32_bf16 v[60:63], v[156:159], a[116:119], v[60:63]
buffer_load_dwordx4 v226, s[40:43], s75 offen lds
v_mfma_f32_16x16x32_bf16 v[28:31], v[156:159], a[112:115], v[28:31]
s_waitcnt vmcnt(16) lgkmcnt(6)
ds_read_b64_tr_b16 v[156:157], v218 offset:1544
v_mfma_f32_16x16x32_bf16 v[0:3], v[128:131], a[128:131], v[0:3]
ds_read_b64_tr_b16 v[158:159], v218 offset:1800
s_add_u32 m0, s71, 115712
v_mfma_f32_16x16x32_bf16 v[32:35], v[128:131], a[132:135], v[32:35]
s_add_u32 s75, s70, 3670144
v_mfma_f32_16x16x32_bf16 v[64:67], v[128:131], a[136:139], v[64:67]
v_mfma_f32_16x16x32_bf16 v[96:99], v[128:131], a[140:143], v[96:99]
ds_read_b64_tr_b16 v[128:129], v218 offset:8192
ds_read_b64_tr_b16 v[130:131], v218 offset:8448
v_mfma_f32_16x16x32_bf16 v[100:103], v[132:135], a[140:143], v[100:103]
buffer_load_dwordx4 v226, s[40:43], s75 offen lds
v_mfma_f32_16x16x32_bf16 v[68:71], v[132:135], a[136:139], v[68:71]
s_add_u32 m0, s71, 116736
s_add_u32 s75, s70, 3735552
v_mfma_f32_16x16x32_bf16 v[36:39], v[132:135], a[132:135], v[36:39]
v_mfma_f32_16x16x32_bf16 v[4:7], v[132:135], a[128:131], v[4:7]
ds_read_b64_tr_b16 v[132:133], v218 offset:8200
ds_read_b64_tr_b16 v[134:135], v218 offset:8456
v_mfma_f32_16x16x32_bf16 v[8:11], v[136:139], a[128:131], v[8:11]
v_mfma_f32_16x16x32_bf16 v[40:43], v[136:139], a[132:135], v[40:43]
buffer_load_dwordx4 v226, s[40:43], s75 offen lds
v_mfma_f32_16x16x32_bf16 v[72:75], v[136:139], a[136:139], v[72:75]
s_add_u32 m0, s71, 117760
s_add_u32 s75, s70, 3735680
v_mfma_f32_16x16x32_bf16 v[104:107], v[136:139], a[140:143], v[104:107]
ds_read_b64_tr_b16 v[136:137], v218 offset:8704
ds_read_b64_tr_b16 v[138:139], v218 offset:8960
v_mfma_f32_16x16x32_bf16 v[108:111], v[140:143], a[140:143], v[108:111]
v_mfma_f32_16x16x32_bf16 v[76:79], v[140:143], a[136:139], v[76:79]
v_mfma_f32_16x16x32_bf16 v[44:47], v[140:143], a[132:135], v[44:47]
buffer_load_dwordx4 v226, s[40:43], s75 offen lds
v_mfma_f32_16x16x32_bf16 v[12:15], v[140:143], a[128:131], v[12:15]
s_waitcnt lgkmcnt(6)
ds_read_b64_tr_b16 v[140:141], v218 offset:8712
v_mfma_f32_16x16x32_bf16 v[16:19], v[144:147], a[128:131], v[16:19]
ds_read_b64_tr_b16 v[142:143], v218 offset:8968
s_add_u32 s84, s84, 4096
v_mfma_f32_16x16x32_bf16 v[48:51], v[144:147], a[132:135], v[48:51]
v_mfma_f32_16x16x32_bf16 v[80:83], v[144:147], a[136:139], v[80:83]
v_mfma_f32_16x16x32_bf16 v[112:115], v[144:147], a[140:143], v[112:115]
ds_read_b64_tr_b16 v[144:145], v218 offset:9216
ds_read_b64_tr_b16 v[146:147], v218 offset:9472
v_mfma_f32_16x16x32_bf16 v[116:119], v[148:151], a[140:143], v[116:119]
buffer_store_dwordx4 a[208:211], v208, s[36:39], s84 offen offset:0 sc1
v_mfma_f32_16x16x32_bf16 v[84:87], v[148:151], a[136:139], v[84:87]
s_add_u32 s69, s69, 4096
v_mfma_f32_16x16x32_bf16 v[52:55], v[148:151], a[132:135], v[52:55]
v_mfma_f32_16x16x32_bf16 v[20:23], v[148:151], a[128:131], v[20:23]
ds_read_b64_tr_b16 v[148:149], v218 offset:9224
ds_read_b64_tr_b16 v[150:151], v218 offset:9480
v_mfma_f32_16x16x32_bf16 v[24:27], v[152:155], a[128:131], v[24:27]
v_mfma_f32_16x16x32_bf16 v[56:59], v[152:155], a[132:135], v[56:59]
buffer_load_dwordx4 a[64:67], v192, s[36:39], s69 offen offset:0 sc1
v_mfma_f32_16x16x32_bf16 v[88:91], v[152:155], a[136:139], v[88:91]
v_mfma_f32_16x16x32_bf16 v[120:123], v[152:155], a[140:143], v[120:123]
ds_read_b64_tr_b16 v[152:153], v218 offset:9728
ds_read_b64_tr_b16 v[154:155], v218 offset:9984
v_mfma_f32_16x16x32_bf16 v[124:127], v[156:159], a[140:143], v[124:127]
v_mfma_f32_16x16x32_bf16 v[92:95], v[156:159], a[136:139], v[92:95]
v_mfma_f32_16x16x32_bf16 v[60:63], v[156:159], a[132:135], v[60:63]
buffer_load_dwordx4 a[68:71], v192, s[36:39], s69 offen offset:1024 sc1
v_mfma_f32_16x16x32_bf16 v[28:31], v[156:159], a[128:131], v[28:31]
s_waitcnt vmcnt(17) lgkmcnt(6)
ds_read_b64_tr_b16 v[156:157], v218 offset:9736
v_mfma_f32_16x16x32_bf16 v[0:3], v[128:131], a[144:147], v[0:3]
ds_read_b64_tr_b16 v[158:159], v218 offset:9992
v_mfma_f32_16x16x32_bf16 v[32:35], v[128:131], a[148:151], v[32:35]
v_mfma_f32_16x16x32_bf16 v[64:67], v[128:131], a[152:155], v[64:67]
buffer_load_dwordx4 a[72:75], v193, s[36:39], s69 offen offset:0 sc1
v_mfma_f32_16x16x32_bf16 v[96:99], v[128:131], a[156:159], v[96:99]
v_mfma_f32_16x16x32_bf16 v[100:103], v[132:135], a[156:159], v[100:103]
v_mfma_f32_16x16x32_bf16 v[68:71], v[132:135], a[152:155], v[68:71]
v_mfma_f32_16x16x32_bf16 v[36:39], v[132:135], a[148:151], v[36:39]
s_barrier
v_mfma_f32_16x16x32_bf16 v[4:7], v[132:135], a[144:147], v[4:7]
ds_read_b64_tr_b16 v[128:129], v218 offset:16384
ds_read_b64_tr_b16 v[130:131], v218 offset:16640
v_mfma_f32_16x16x32_bf16 v[8:11], v[136:139], a[144:147], v[8:11]
buffer_load_dwordx4 a[76:79], v193, s[36:39], s69 offen offset:1024 sc1
v_mfma_f32_16x16x32_bf16 v[40:43], v[136:139], a[148:151], v[40:43]
s_add_u32 s69, s69, 4096
ds_read_b64_tr_b16 v[132:133], v218 offset:16392
v_mfma_f32_16x16x32_bf16 v[72:75], v[136:139], a[152:155], v[72:75]
ds_read_b64_tr_b16 v[134:135], v218 offset:16648
v_mfma_f32_16x16x32_bf16 v[104:107], v[136:139], a[156:159], v[104:107]
buffer_load_dwordx4 a[80:83], v192, s[36:39], s69 offen offset:0 sc1
v_mfma_f32_16x16x32_bf16 v[108:111], v[140:143], a[156:159], v[108:111]
ds_read_b64_tr_b16 v[136:137], v218 offset:16896
ds_read_b64_tr_b16 v[138:139], v218 offset:17152
v_mfma_f32_16x16x32_bf16 v[76:79], v[140:143], a[152:155], v[76:79]
v_mfma_f32_16x16x32_bf16 v[44:47], v[140:143], a[148:151], v[44:47]
buffer_load_dwordx4 a[84:87], v192, s[36:39], s69 offen offset:1024 sc1
v_mfma_f32_16x16x32_bf16 v[12:15], v[140:143], a[144:147], v[12:15]
s_waitcnt lgkmcnt(6)
ds_read_b64_tr_b16 v[140:141], v218 offset:16904
v_mfma_f32_16x16x32_bf16 v[16:19], v[144:147], a[144:147], v[16:19]
ds_read_b64_tr_b16 v[142:143], v218 offset:17160
v_mfma_f32_16x16x32_bf16 v[48:51], v[144:147], a[148:151], v[48:51]
v_mfma_f32_16x16x32_bf16 v[80:83], v[144:147], a[152:155], v[80:83]
v_mfma_f32_16x16x32_bf16 v[112:115], v[144:147], a[156:159], v[112:115]
ds_read_b64_tr_b16 v[144:145], v218 offset:17408
ds_read_b64_tr_b16 v[146:147], v218 offset:17664
v_mfma_f32_16x16x32_bf16 v[116:119], v[148:151], a[156:159], v[116:119]
buffer_load_dwordx4 a[88:91], v193, s[36:39], s69 offen offset:0 sc1
v_mfma_f32_16x16x32_bf16 v[84:87], v[148:151], a[152:155], v[84:87]
v_mfma_f32_16x16x32_bf16 v[52:55], v[148:151], a[148:151], v[52:55]
v_mfma_f32_16x16x32_bf16 v[20:23], v[148:151], a[144:147], v[20:23]
ds_read_b64_tr_b16 v[148:149], v218 offset:17416
ds_read_b64_tr_b16 v[150:151], v218 offset:17672
v_mfma_f32_16x16x32_bf16 v[24:27], v[152:155], a[144:147], v[24:27]
v_mfma_f32_16x16x32_bf16 v[56:59], v[152:155], a[148:151], v[56:59]
buffer_load_dwordx4 a[92:95], v193, s[36:39], s69 offen offset:1024 sc1
v_mfma_f32_16x16x32_bf16 v[88:91], v[152:155], a[152:155], v[88:91]
s_add_u32 m0, s71, 131072
s_add_u32 s75, s70, 4194304
v_mfma_f32_16x16x32_bf16 v[120:123], v[152:155], a[156:159], v[120:123]
ds_read_b64_tr_b16 v[152:153], v218 offset:17920
ds_read_b64_tr_b16 v[154:155], v218 offset:18176
v_mfma_f32_16x16x32_bf16 v[124:127], v[156:159], a[156:159], v[124:127]
v_mfma_f32_16x16x32_bf16 v[92:95], v[156:159], a[152:155], v[92:95]
v_mfma_f32_16x16x32_bf16 v[60:63], v[156:159], a[148:151], v[60:63]
buffer_load_dwordx4 v226, s[40:43], s75 offen lds
v_mfma_f32_16x16x32_bf16 v[28:31], v[156:159], a[144:147], v[28:31]
s_waitcnt vmcnt(19) lgkmcnt(6)
ds_read_b64_tr_b16 v[156:157], v218 offset:17928
v_mfma_f32_16x16x32_bf16 v[0:3], v[128:131], a[160:163], v[0:3]
ds_read_b64_tr_b16 v[158:159], v218 offset:18184
s_add_u32 m0, s71, 132096
v_mfma_f32_16x16x32_bf16 v[32:35], v[128:131], a[164:167], v[32:35]
s_add_u32 s75, s70, 4194432
v_mfma_f32_16x16x32_bf16 v[64:67], v[128:131], a[168:171], v[64:67]
v_mfma_f32_16x16x32_bf16 v[96:99], v[128:131], a[172:175], v[96:99]
ds_read_b64_tr_b16 v[128:129], v218 offset:24576
ds_read_b64_tr_b16 v[130:131], v218 offset:24832
v_mfma_f32_16x16x32_bf16 v[100:103], v[132:135], a[172:175], v[100:103]
buffer_load_dwordx4 v226, s[40:43], s75 offen lds
v_mfma_f32_16x16x32_bf16 v[68:71], v[132:135], a[168:171], v[68:71]
s_add_u32 m0, s71, 133120
s_add_u32 s75, s70, 4259840
v_mfma_f32_16x16x32_bf16 v[36:39], v[132:135], a[164:167], v[36:39]
v_mfma_f32_16x16x32_bf16 v[4:7], v[132:135], a[160:163], v[4:7]
ds_read_b64_tr_b16 v[132:133], v218 offset:24584
ds_read_b64_tr_b16 v[134:135], v218 offset:24840
v_mfma_f32_16x16x32_bf16 v[8:11], v[136:139], a[160:163], v[8:11]
v_mfma_f32_16x16x32_bf16 v[40:43], v[136:139], a[164:167], v[40:43]
buffer_load_dwordx4 v226, s[40:43], s75 offen lds
v_mfma_f32_16x16x32_bf16 v[72:75], v[136:139], a[168:171], v[72:75]
s_add_u32 m0, s71, 134144
s_add_u32 s75, s70, 4259968
v_mfma_f32_16x16x32_bf16 v[104:107], v[136:139], a[172:175], v[104:107]
ds_read_b64_tr_b16 v[136:137], v218 offset:25088
ds_read_b64_tr_b16 v[138:139], v218 offset:25344
v_mfma_f32_16x16x32_bf16 v[108:111], v[140:143], a[172:175], v[108:111]
v_mfma_f32_16x16x32_bf16 v[76:79], v[140:143], a[168:171], v[76:79]
v_mfma_f32_16x16x32_bf16 v[44:47], v[140:143], a[164:167], v[44:47]
buffer_load_dwordx4 v226, s[40:43], s75 offen lds
v_mfma_f32_16x16x32_bf16 v[12:15], v[140:143], a[160:163], v[12:15]
s_waitcnt lgkmcnt(6)
ds_read_b64_tr_b16 v[140:141], v218 offset:25096
v_mfma_f32_16x16x32_bf16 v[16:19], v[144:147], a[160:163], v[16:19]
ds_read_b64_tr_b16 v[142:143], v218 offset:25352
s_add_u32 s69, s69, 4096
v_mfma_f32_16x16x32_bf16 v[48:51], v[144:147], a[164:167], v[48:51]
v_mfma_f32_16x16x32_bf16 v[80:83], v[144:147], a[168:171], v[80:83]
v_mfma_f32_16x16x32_bf16 v[112:115], v[144:147], a[172:175], v[112:115]
ds_read_b64_tr_b16 v[144:145], v218 offset:25600
ds_read_b64_tr_b16 v[146:147], v218 offset:25856
v_mfma_f32_16x16x32_bf16 v[116:119], v[148:151], a[172:175], v[116:119]
buffer_load_dwordx4 a[96:99], v192, s[36:39], s69 offen offset:0 sc1
v_mfma_f32_16x16x32_bf16 v[84:87], v[148:151], a[168:171], v[84:87]
v_mfma_f32_16x16x32_bf16 v[52:55], v[148:151], a[164:167], v[52:55]
v_mfma_f32_16x16x32_bf16 v[20:23], v[148:151], a[160:163], v[20:23]
ds_read_b64_tr_b16 v[148:149], v218 offset:25608
ds_read_b64_tr_b16 v[150:151], v218 offset:25864
v_mfma_f32_16x16x32_bf16 v[24:27], v[152:155], a[160:163], v[24:27]
v_mfma_f32_16x16x32_bf16 v[56:59], v[152:155], a[164:167], v[56:59]
buffer_load_dwordx4 a[100:103], v192, s[36:39], s69 offen offset:1024 sc1
v_mfma_f32_16x16x32_bf16 v[88:91], v[152:155], a[168:171], v[88:91]
v_mfma_f32_16x16x32_bf16 v[120:123], v[152:155], a[172:175], v[120:123]
ds_read_b64_tr_b16 v[152:153], v218 offset:26112
ds_read_b64_tr_b16 v[154:155], v218 offset:26368
v_mfma_f32_16x16x32_bf16 v[124:127], v[156:159], a[172:175], v[124:127]
v_mfma_f32_16x16x32_bf16 v[92:95], v[156:159], a[168:171], v[92:95]
v_mfma_f32_16x16x32_bf16 v[60:63], v[156:159], a[164:167], v[60:63]
buffer_load_dwordx4 a[104:107], v193, s[36:39], s69 offen offset:0 sc1
v_mfma_f32_16x16x32_bf16 v[28:31], v[156:159], a[160:163], v[28:31]
s_waitcnt vmcnt(20) lgkmcnt(6)
ds_read_b64_tr_b16 v[156:157], v218 offset:26120
v_mfma_f32_16x16x32_bf16 v[0:3], v[128:131], a[176:179], v[0:3]
ds_read_b64_tr_b16 v[158:159], v218 offset:26376
v_mfma_f32_16x16x32_bf16 v[32:35], v[128:131], a[180:183], v[32:35]
v_mfma_f32_16x16x32_bf16 v[64:67], v[128:131], a[184:187], v[64:67]
v_mfma_f32_16x16x32_bf16 v[96:99], v[128:131], a[188:191], v[96:99]
ds_read_b64_tr_b16 v[128:129], v218 offset:32768
ds_read_b64_tr_b16 v[130:131], v218 offset:33024
v_mfma_f32_16x16x32_bf16 v[100:103], v[132:135], a[188:191], v[100:103]
buffer_load_dwordx4 a[108:111], v193, s[36:39], s69 offen offset:1024 sc1
v_mfma_f32_16x16x32_bf16 v[68:71], v[132:135], a[184:187], v[68:71]
s_add_u32 s69, s69, 4096
v_mfma_f32_16x16x32_bf16 v[36:39], v[132:135], a[180:183], v[36:39]
v_mfma_f32_16x16x32_bf16 v[4:7], v[132:135], a[176:179], v[4:7]
ds_read_b64_tr_b16 v[132:133], v218 offset:32776
ds_read_b64_tr_b16 v[134:135], v218 offset:33032
v_mfma_f32_16x16x32_bf16 v[8:11], v[136:139], a[176:179], v[8:11]
ds_read_b128 a[192:195], v188 offset:0
ds_read_b128 a[196:199], v188 offset:64
v_mfma_f32_16x16x32_bf16 v[40:43], v[136:139], a[180:183], v[40:43]
buffer_load_dwordx4 a[112:115], v192, s[36:39], s69 offen offset:0 sc1
v_mfma_f32_16x16x32_bf16 v[72:75], v[136:139], a[184:187], v[72:75]
ds_read_b128 a[200:203], v188 offset:128
ds_read_b128 a[204:207], v188 offset:192
v_mfma_f32_16x16x32_bf16 v[104:107], v[136:139], a[188:191], v[104:107]
ds_read_b64_tr_b16 v[136:137], v218 offset:33280
ds_read_b64_tr_b16 v[138:139], v218 offset:33536
v_mfma_f32_16x16x32_bf16 v[108:111], v[140:143], a[188:191], v[108:111]
ds_read_b128 a[208:211], v188 offset:256
v_mfma_f32_16x16x32_bf16 v[76:79], v[140:143], a[184:187], v[76:79]
v_mfma_f32_16x16x32_bf16 v[44:47], v[140:143], a[180:183], v[44:47]
buffer_load_dwordx4 a[116:119], v192, s[36:39], s69 offen offset:1024 sc1
v_mfma_f32_16x16x32_bf16 v[12:15], v[140:143], a[176:179], v[12:15]
s_waitcnt lgkmcnt(11)
ds_read_b64_tr_b16 v[140:141], v218 offset:33288
v_mfma_f32_16x16x32_bf16 v[16:19], v[144:147], a[176:179], v[16:19]
ds_read_b64_tr_b16 v[142:143], v218 offset:33544
v_mfma_f32_16x16x32_bf16 v[48:51], v[144:147], a[180:183], v[48:51]
v_mfma_f32_16x16x32_bf16 v[80:83], v[144:147], a[184:187], v[80:83]
v_mfma_f32_16x16x32_bf16 v[112:115], v[144:147], a[188:191], v[112:115]
ds_read_b64_tr_b16 v[144:145], v218 offset:33792
ds_read_b64_tr_b16 v[146:147], v218 offset:34048
v_mfma_f32_16x16x32_bf16 v[116:119], v[148:151], a[188:191], v[116:119]
buffer_load_dwordx4 a[120:123], v193, s[36:39], s69 offen offset:0 sc1
v_mfma_f32_16x16x32_bf16 v[84:87], v[148:151], a[184:187], v[84:87]
v_mfma_f32_16x16x32_bf16 v[52:55], v[148:151], a[180:183], v[52:55]
v_mfma_f32_16x16x32_bf16 v[20:23], v[148:151], a[176:179], v[20:23]
ds_read_b64_tr_b16 v[148:149], v218 offset:33800
ds_read_b64_tr_b16 v[150:151], v218 offset:34056
v_mfma_f32_16x16x32_bf16 v[24:27], v[152:155], a[176:179], v[24:27]
v_mfma_f32_16x16x32_bf16 v[56:59], v[152:155], a[180:183], v[56:59]
buffer_load_dwordx4 a[124:127], v193, s[36:39], s69 offen offset:1024 sc1
v_mfma_f32_16x16x32_bf16 v[88:91], v[152:155], a[184:187], v[88:91]
s_add_u32 m0, s71, 49152
v_mfma_f32_16x16x32_bf16 v[120:123], v[152:155], a[188:191], v[120:123]
ds_read_b64_tr_b16 v[152:153], v218 offset:34304
ds_read_b64_tr_b16 v[154:155], v218 offset:34560
v_mfma_f32_16x16x32_bf16 v[124:127], v[156:159], a[188:191], v[124:127]
v_mfma_f32_16x16x32_bf16 v[92:95], v[156:159], a[184:187], v[92:95]
v_mfma_f32_16x16x32_bf16 v[60:63], v[156:159], a[180:183], v[60:63]
buffer_load_dwordx4 v222, s[44:47], s72 offen offset:0 lds
v_mfma_f32_16x16x32_bf16 v[28:31], v[156:159], a[176:179], v[28:31]
s_waitcnt vmcnt(17) lgkmcnt(6)
ds_read_b64_tr_b16 v[156:157], v218 offset:34312
v_mfma_f32_16x16x32_bf16 v[0:3], v[128:131], a[64:67], v[0:3]
ds_read_b64_tr_b16 v[158:159], v218 offset:34568
v_mfma_f32_16x16x32_bf16 v[32:35], v[128:131], a[68:71], v[32:35]
v_mfma_f32_16x16x32_bf16 v[64:67], v[128:131], a[72:75], v[64:67]
v_mfma_f32_16x16x32_bf16 v[96:99], v[128:131], a[76:79], v[96:99]
ds_read_b64_tr_b16 v[128:129], v218 offset:40960
ds_read_b64_tr_b16 v[130:131], v218 offset:41216
v_mfma_f32_16x16x32_bf16 v[100:103], v[132:135], a[76:79], v[100:103]
buffer_load_dwordx4 v223, s[44:47], s72 offen offset:1024 lds
v_mfma_f32_16x16x32_bf16 v[68:71], v[132:135], a[72:75], v[68:71]
v_mfma_f32_16x16x32_bf16 v[36:39], v[132:135], a[68:71], v[36:39]
v_mfma_f32_16x16x32_bf16 v[4:7], v[132:135], a[64:67], v[4:7]
ds_read_b64_tr_b16 v[132:133], v218 offset:40968
ds_read_b64_tr_b16 v[134:135], v218 offset:41224
v_mfma_f32_16x16x32_bf16 v[8:11], v[136:139], a[64:67], v[8:11]
v_mfma_f32_16x16x32_bf16 v[40:43], v[136:139], a[68:71], v[40:43]
buffer_load_dwordx4 v224, s[44:47], s72 offen offset:2048 lds
v_mfma_f32_16x16x32_bf16 v[72:75], v[136:139], a[72:75], v[72:75]
v_mfma_f32_16x16x32_bf16 v[104:107], v[136:139], a[76:79], v[104:107]
ds_read_b64_tr_b16 v[136:137], v218 offset:41472
ds_read_b64_tr_b16 v[138:139], v218 offset:41728
v_mfma_f32_16x16x32_bf16 v[108:111], v[140:143], a[76:79], v[108:111]
v_mfma_f32_16x16x32_bf16 v[76:79], v[140:143], a[72:75], v[76:79]
v_mfma_f32_16x16x32_bf16 v[44:47], v[140:143], a[68:71], v[44:47]
buffer_load_dwordx4 v225, s[44:47], s72 offen offset:3072 lds
v_mfma_f32_16x16x32_bf16 v[12:15], v[140:143], a[64:67], v[12:15]
s_waitcnt lgkmcnt(6)
s_add_u32 s72, s72, 8192
v_mfma_f32_16x16x32_bf16 v[16:19], v[144:147], a[64:67], v[16:19]
ds_read_b64_tr_b16 v[140:141], v218 offset:41480
ds_read_b64_tr_b16 v[142:143], v218 offset:41736
v_mfma_f32_16x16x32_bf16 v[48:51], v[144:147], a[68:71], v[48:51]
s_add_u32 s69, s69, 4096
v_mfma_f32_16x16x32_bf16 v[80:83], v[144:147], a[72:75], v[80:83]
v_mfma_f32_16x16x32_bf16 v[112:115], v[144:147], a[76:79], v[112:115]
ds_read_b64_tr_b16 v[144:145], v218 offset:41984
ds_read_b64_tr_b16 v[146:147], v218 offset:42240
v_mfma_f32_16x16x32_bf16 v[116:119], v[148:151], a[76:79], v[116:119]
buffer_load_dwordx4 a[128:131], v192, s[36:39], s69 offen offset:0 sc1
v_mfma_f32_16x16x32_bf16 v[84:87], v[148:151], a[72:75], v[84:87]
v_mfma_f32_16x16x32_bf16 v[52:55], v[148:151], a[68:71], v[52:55]
v_mfma_f32_16x16x32_bf16 v[20:23], v[148:151], a[64:67], v[20:23]
ds_read_b64_tr_b16 v[148:149], v218 offset:41992
ds_read_b64_tr_b16 v[150:151], v218 offset:42248
v_mfma_f32_16x16x32_bf16 v[24:27], v[152:155], a[64:67], v[24:27]
v_mfma_f32_16x16x32_bf16 v[56:59], v[152:155], a[68:71], v[56:59]
buffer_load_dwordx4 a[132:135], v192, s[36:39], s69 offen offset:1024 sc1
v_mfma_f32_16x16x32_bf16 v[88:91], v[152:155], a[72:75], v[88:91]
v_mfma_f32_16x16x32_bf16 v[120:123], v[152:155], a[76:79], v[120:123]
ds_read_b64_tr_b16 v[152:153], v218 offset:42496
ds_read_b64_tr_b16 v[154:155], v218 offset:42752
v_mfma_f32_16x16x32_bf16 v[124:127], v[156:159], a[76:79], v[124:127]
v_mfma_f32_16x16x32_bf16 v[92:95], v[156:159], a[72:75], v[92:95]
v_mfma_f32_16x16x32_bf16 v[60:63], v[156:159], a[68:71], v[60:63]
buffer_load_dwordx4 a[136:139], v193, s[36:39], s69 offen offset:0 sc1
v_mfma_f32_16x16x32_bf16 v[28:31], v[156:159], a[64:67], v[28:31]
s_waitcnt vmcnt(19) lgkmcnt(6)
ds_read_b64_tr_b16 v[156:157], v218 offset:42504
v_mfma_f32_16x16x32_bf16 v[0:3], v[128:131], a[80:83], v[0:3]
ds_read_b64_tr_b16 v[158:159], v218 offset:42760
v_mfma_f32_16x16x32_bf16 v[32:35], v[128:131], a[84:87], v[32:35]
v_mfma_f32_16x16x32_bf16 v[64:67], v[128:131], a[88:91], v[64:67]
v_mfma_f32_16x16x32_bf16 v[96:99], v[128:131], a[92:95], v[96:99]
v_mfma_f32_16x16x32_bf16 v[100:103], v[132:135], a[92:95], v[100:103]
buffer_load_dwordx4 a[140:143], v193, s[36:39], s69 offen offset:1024 sc1
v_mfma_f32_16x16x32_bf16 v[68:71], v[132:135], a[88:91], v[68:71]
s_add_u32 m0, s71, 0
v_mfma_f32_16x16x32_bf16 v[36:39], v[132:135], a[84:87], v[36:39]
s_waitcnt vmcnt(16)
s_barrier
v_mfma_f32_16x16x32_bf16 v[4:7], v[132:135], a[80:83], v[4:7]
ds_read_b64_tr_b16 v[128:129], v218 offset:49152
ds_read_b64_tr_b16 v[130:131], v218 offset:49408
v_mfma_f32_16x16x32_bf16 v[8:11], v[136:139], a[80:83], v[8:11]
ds_read_b64_tr_b16 v[132:133], v218 offset:49160
ds_read_b64_tr_b16 v[134:135], v218 offset:49416
v_mfma_f32_16x16x32_bf16 v[40:43], v[136:139], a[84:87], v[40:43]
buffer_load_dwordx4 v222, s[44:47], s72 offen offset:0 lds
v_mfma_f32_16x16x32_bf16 v[72:75], v[136:139], a[88:91], v[72:75]
v_mfma_f32_16x16x32_bf16 v[104:107], v[136:139], a[92:95], v[104:107]
ds_read_b64_tr_b16 v[136:137], v218 offset:49664
ds_read_b64_tr_b16 v[138:139], v218 offset:49920
v_mfma_f32_16x16x32_bf16 v[108:111], v[140:143], a[92:95], v[108:111]
v_mfma_f32_16x16x32_bf16 v[76:79], v[140:143], a[88:91], v[76:79]
v_mfma_f32_16x16x32_bf16 v[44:47], v[140:143], a[84:87], v[44:47]
buffer_load_dwordx4 v223, s[44:47], s72 offen offset:1024 lds
v_mfma_f32_16x16x32_bf16 v[12:15], v[140:143], a[80:83], v[12:15]
s_waitcnt lgkmcnt(6)
s_lshl_b32 s76, s65, 3
v_mfma_f32_16x16x32_bf16 v[16:19], v[144:147], a[80:83], v[16:19]
s_add_u32 s76, s76, 16781312
ds_read_b64_tr_b16 v[140:141], v218 offset:49672
v_mfma_f32_16x16x32_bf16 v[48:51], v[144:147], a[84:87], v[48:51]
s_cmp_eq_u32 s64, 0
s_cselect_b32 s76, s76, 47923200
v_mfma_f32_16x16x32_bf16 v[80:83], v[144:147], a[88:91], v[80:83]
ds_read_b64_tr_b16 v[142:143], v218 offset:49928
v_mfma_f32_16x16x32_bf16 v[112:115], v[144:147], a[92:95], v[112:115]
buffer_store_dwordx2 v[210:211], v212, s[36:39], s76 offen sc1
v_mfma_f32_16x16x32_bf16 v[116:119], v[148:151], a[92:95], v[116:119]
buffer_load_dwordx4 v224, s[44:47], s72 offen offset:2048 lds
v_mfma_f32_16x16x32_bf16 v[84:87], v[148:151], a[88:91], v[84:87]
ds_read_b64_tr_b16 v[144:145], v218 offset:50176
ds_read_b64_tr_b16 v[146:147], v218 offset:50432
v_mfma_f32_16x16x32_bf16 v[52:55], v[148:151], a[84:87], v[52:55]
buffer_load_dwordx4 v225, s[44:47], s72 offen offset:3072 lds
v_mfma_f32_16x16x32_bf16 v[20:23], v[148:151], a[80:83], v[20:23]
s_add_u32 s72, s72, 8192
s_add_u32 m0, s71, 147456
v_mfma_f32_16x16x32_bf16 v[24:27], v[152:155], a[80:83], v[24:27]
s_add_u32 s75, s70, 4718592
ds_read_b64_tr_b16 v[148:149], v218 offset:50184
v_mfma_f32_16x16x32_bf16 v[56:59], v[152:155], a[84:87], v[56:59]
ds_read_b64_tr_b16 v[150:151], v218 offset:50440
v_mfma_f32_16x16x32_bf16 v[88:91], v[152:155], a[88:91], v[88:91]
buffer_load_dwordx4 v226, s[40:43], s75 offen lds
v_mfma_f32_16x16x32_bf16 v[120:123], v[152:155], a[92:95], v[120:123]
s_add_u32 m0, s71, 148480
s_add_u32 s75, s70, 4718720
v_mfma_f32_16x16x32_bf16 v[124:127], v[156:159], a[92:95], v[124:127]
ds_read_b64_tr_b16 v[152:153], v218 offset:50688
ds_read_b64_tr_b16 v[154:155], v218 offset:50944
v_mfma_f32_16x16x32_bf16 v[92:95], v[156:159], a[88:91], v[92:95]
v_mfma_f32_16x16x32_bf16 v[60:63], v[156:159], a[84:87], v[60:63]
buffer_load_dwordx4 v226, s[40:43], s75 offen lds
v_mfma_f32_16x16x32_bf16 v[28:31], v[156:159], a[80:83], v[28:31]
s_waitcnt vmcnt(19) lgkmcnt(6)
ds_read_b64_tr_b16 v[156:157], v218 offset:50696
v_mfma_f32_16x16x32_bf16 v[0:3], v[128:131], a[96:99], v[0:3]
ds_read_b64_tr_b16 v[158:159], v218 offset:50952
s_add_u32 m0, s71, 149504
v_mfma_f32_16x16x32_bf16 v[32:35], v[128:131], a[100:103], v[32:35]
s_add_u32 s75, s70, 4784128
v_mfma_f32_16x16x32_bf16 v[64:67], v[128:131], a[104:107], v[64:67]
v_mfma_f32_16x16x32_bf16 v[96:99], v[128:131], a[108:111], v[96:99]
ds_read_b64_tr_b16 v[128:129], v218 offset:57344
ds_read_b64_tr_b16 v[130:131], v218 offset:57600
v_mfma_f32_16x16x32_bf16 v[100:103], v[132:135], a[108:111], v[100:103]
buffer_load_dwordx4 v226, s[40:43], s75 offen lds
v_mfma_f32_16x16x32_bf16 v[68:71], v[132:135], a[104:107], v[68:71]
s_add_u32 m0, s71, 150528
s_add_u32 s75, s70, 4784256
v_mfma_f32_16x16x32_bf16 v[36:39], v[132:135], a[100:103], v[36:39]
v_mfma_f32_16x16x32_bf16 v[4:7], v[132:135], a[96:99], v[4:7]
ds_read_b64_tr_b16 v[132:133], v218 offset:57352
ds_read_b64_tr_b16 v[134:135], v218 offset:57608
v_mfma_f32_16x16x32_bf16 v[8:11], v[136:139], a[96:99], v[8:11]
v_mfma_f32_16x16x32_bf16 v[40:43], v[136:139], a[100:103], v[40:43]
buffer_load_dwordx4 v226, s[40:43], s75 offen lds
v_mfma_f32_16x16x32_bf16 v[72:75], v[136:139], a[104:107], v[72:75]
s_add_u32 s69, s69, 4096
v_mfma_f32_16x16x32_bf16 v[104:107], v[136:139], a[108:111], v[104:107]
ds_read_b64_tr_b16 v[136:137], v218 offset:57856
ds_read_b64_tr_b16 v[138:139], v218 offset:58112
v_mfma_f32_16x16x32_bf16 v[108:111], v[140:143], a[108:111], v[108:111]
v_mfma_f32_16x16x32_bf16 v[76:79], v[140:143], a[104:107], v[76:79]
v_mfma_f32_16x16x32_bf16 v[44:47], v[140:143], a[100:103], v[44:47]
buffer_load_dwordx4 a[144:147], v192, s[36:39], s69 offen offset:0 sc1
v_mfma_f32_16x16x32_bf16 v[12:15], v[140:143], a[96:99], v[12:15]
s_waitcnt lgkmcnt(6)
ds_read_b64_tr_b16 v[140:141], v218 offset:57864
v_mfma_f32_16x16x32_bf16 v[16:19], v[144:147], a[96:99], v[16:19]
ds_read_b64_tr_b16 v[142:143], v218 offset:58120
v_mfma_f32_16x16x32_bf16 v[48:51], v[144:147], a[100:103], v[48:51]
v_mfma_f32_16x16x32_bf16 v[80:83], v[144:147], a[104:107], v[80:83]
v_mfma_f32_16x16x32_bf16 v[112:115], v[144:147], a[108:111], v[112:115]
ds_read_b64_tr_b16 v[144:145], v218 offset:58368
ds_read_b64_tr_b16 v[146:147], v218 offset:58624
v_mfma_f32_16x16x32_bf16 v[116:119], v[148:151], a[108:111], v[116:119]
buffer_load_dwordx4 a[148:151], v192, s[36:39], s69 offen offset:1024 sc1
v_mfma_f32_16x16x32_bf16 v[84:87], v[148:151], a[104:107], v[84:87]
v_mfma_f32_16x16x32_bf16 v[52:55], v[148:151], a[100:103], v[52:55]
v_mfma_f32_16x16x32_bf16 v[20:23], v[148:151], a[96:99], v[20:23]
ds_read_b64_tr_b16 v[148:149], v218 offset:58376
ds_read_b64_tr_b16 v[150:151], v218 offset:58632
v_mfma_f32_16x16x32_bf16 v[24:27], v[152:155], a[96:99], v[24:27]
v_mfma_f32_16x16x32_bf16 v[56:59], v[152:155], a[100:103], v[56:59]
buffer_load_dwordx4 a[152:155], v193, s[36:39], s69 offen offset:0 sc1
v_mfma_f32_16x16x32_bf16 v[88:91], v[152:155], a[104:107], v[88:91]
v_mfma_f32_16x16x32_bf16 v[120:123], v[152:155], a[108:111], v[120:123]
ds_read_b64_tr_b16 v[152:153], v218 offset:58880
ds_read_b64_tr_b16 v[154:155], v218 offset:59136
v_mfma_f32_16x16x32_bf16 v[124:127], v[156:159], a[108:111], v[124:127]
v_mfma_f32_16x16x32_bf16 v[92:95], v[156:159], a[104:107], v[92:95]
v_mfma_f32_16x16x32_bf16 v[60:63], v[156:159], a[100:103], v[60:63]
buffer_load_dwordx4 a[156:159], v193, s[36:39], s69 offen offset:1024 sc1
v_mfma_f32_16x16x32_bf16 v[28:31], v[156:159], a[96:99], v[28:31]
s_waitcnt vmcnt(21) lgkmcnt(6)
s_lshr_b32 s77, s65, 3
v_mfma_f32_16x16x32_bf16 v[0:3], v[128:131], a[112:115], v[0:3]
s_mul_i32 s77, s77, 1024
s_add_u32 s77, s77, .Lk_fbret-.Lk_start-24576
v_mfma_f32_16x16x32_bf16 v[32:35], v[128:131], a[116:119], v[32:35]
ds_read_b64_tr_b16 v[156:157], v218 offset:58888
ds_read_b64_tr_b16 v[158:159], v218 offset:59144
v_mfma_f32_16x16x32_bf16 v[64:67], v[128:131], a[120:123], v[64:67]
v_and_b32 v206, 7, v209
v_lshlrev_b32 v206, 7, v206
v_mfma_f32_16x16x32_bf16 v[96:99], v[128:131], a[124:127], v[96:99]
buffer_load_dword v206, v206, s[60:63], s77 offen
v_mfma_f32_16x16x32_bf16 v[100:103], v[132:135], a[124:127], v[100:103]
s_add_u32 s69, s69, 4096
v_mfma_f32_16x16x32_bf16 v[68:71], v[132:135], a[120:123], v[68:71]
buffer_load_dwordx4 a[160:163], v192, s[36:39], s69 offen offset:0 sc1
v_mfma_f32_16x16x32_bf16 v[36:39], v[132:135], a[116:119], v[36:39]
v_add_u32 v218, 0x10000, v218
ds_read_b64_tr_b16 v[128:129], v218 offset:0
v_mfma_f32_16x16x32_bf16 v[4:7], v[132:135], a[112:115], v[4:7]
ds_read_b64_tr_b16 v[130:131], v218 offset:256
ds_read_b64_tr_b16 v[132:133], v218 offset:8
v_mfma_f32_16x16x32_bf16 v[8:11], v[136:139], a[112:115], v[8:11]
ds_read_b64_tr_b16 v[134:135], v218 offset:264
v_mfma_f32_16x16x32_bf16 v[40:43], v[136:139], a[116:119], v[40:43]
buffer_load_dwordx4 a[164:167], v192, s[36:39], s69 offen offset:1024 sc1
v_mfma_f32_16x16x32_bf16 v[72:75], v[136:139], a[120:123], v[72:75]
v_mfma_f32_16x16x32_bf16 v[104:107], v[136:139], a[124:127], v[104:107]
ds_read_b64_tr_b16 v[136:137], v218 offset:512
ds_read_b64_tr_b16 v[138:139], v218 offset:768
v_mfma_f32_16x16x32_bf16 v[108:111], v[140:143], a[124:127], v[108:111]
v_mfma_f32_16x16x32_bf16 v[76:79], v[140:143], a[120:123], v[76:79]
v_mfma_f32_16x16x32_bf16 v[44:47], v[140:143], a[116:119], v[44:47]
buffer_load_dwordx4 a[168:171], v193, s[36:39], s69 offen offset:0 sc1
v_mfma_f32_16x16x32_bf16 v[12:15], v[140:143], a[112:115], v[12:15]
s_waitcnt lgkmcnt(6)
ds_read_b64_tr_b16 v[140:141], v218 offset:520
v_mfma_f32_16x16x32_bf16 v[16:19], v[144:147], a[112:115], v[16:19]
ds_read_b64_tr_b16 v[142:143], v218 offset:776
v_mfma_f32_16x16x32_bf16 v[48:51], v[144:147], a[116:119], v[48:51]
v_mfma_f32_16x16x32_bf16 v[80:83], v[144:147], a[120:123], v[80:83]
v_mfma_f32_16x16x32_bf16 v[112:115], v[144:147], a[124:127], v[112:115]
ds_read_b64_tr_b16 v[144:145], v218 offset:1024
ds_read_b64_tr_b16 v[146:147], v218 offset:1280
v_mfma_f32_16x16x32_bf16 v[116:119], v[148:151], a[124:127], v[116:119]
buffer_load_dwordx4 a[172:175], v193, s[36:39], s69 offen offset:1024 sc1
v_mfma_f32_16x16x32_bf16 v[84:87], v[148:151], a[120:123], v[84:87]
s_add_u32 s69, s69, 4096
v_mfma_f32_16x16x32_bf16 v[52:55], v[148:151], a[116:119], v[52:55]
v_mfma_f32_16x16x32_bf16 v[20:23], v[148:151], a[112:115], v[20:23]
ds_read_b64_tr_b16 v[148:149], v218 offset:1032
ds_read_b64_tr_b16 v[150:151], v218 offset:1288
v_mfma_f32_16x16x32_bf16 v[24:27], v[152:155], a[112:115], v[24:27]
v_mfma_f32_16x16x32_bf16 v[56:59], v[152:155], a[116:119], v[56:59]
buffer_load_dwordx4 a[176:179], v192, s[36:39], s69 offen offset:0 sc1
v_mfma_f32_16x16x32_bf16 v[88:91], v[152:155], a[120:123], v[88:91]
v_mfma_f32_16x16x32_bf16 v[120:123], v[152:155], a[124:127], v[120:123]
ds_read_b64_tr_b16 v[152:153], v218 offset:1536
ds_read_b64_tr_b16 v[154:155], v218 offset:1792
v_mfma_f32_16x16x32_bf16 v[124:127], v[156:159], a[124:127], v[124:127]
v_mfma_f32_16x16x32_bf16 v[92:95], v[156:159], a[120:123], v[92:95]
v_mfma_f32_16x16x32_bf16 v[60:63], v[156:159], a[116:119], v[60:63]
buffer_load_dwordx4 a[180:183], v192, s[36:39], s69 offen offset:1024 sc1
v_mfma_f32_16x16x32_bf16 v[28:31], v[156:159], a[112:115], v[28:31]
s_waitcnt vmcnt(20) lgkmcnt(6)
ds_read_b64_tr_b16 v[156:157], v218 offset:1544
v_mfma_f32_16x16x32_bf16 v[0:3], v[128:131], a[128:131], v[0:3]
ds_read_b64_tr_b16 v[158:159], v218 offset:1800
v_mfma_f32_16x16x32_bf16 v[32:35], v[128:131], a[132:135], v[32:35]
v_mfma_f32_16x16x32_bf16 v[64:67], v[128:131], a[136:139], v[64:67]
v_mfma_f32_16x16x32_bf16 v[96:99], v[128:131], a[140:143], v[96:99]
ds_read_b64_tr_b16 v[128:129], v218 offset:8192
ds_read_b64_tr_b16 v[130:131], v218 offset:8448
v_mfma_f32_16x16x32_bf16 v[100:103], v[132:135], a[140:143], v[100:103]
buffer_load_dwordx4 a[184:187], v193, s[36:39], s69 offen offset:0 sc1
v_mfma_f32_16x16x32_bf16 v[68:71], v[132:135], a[136:139], v[68:71]
v_mfma_f32_16x16x32_bf16 v[36:39], v[132:135], a[132:135], v[36:39]
v_mfma_f32_16x16x32_bf16 v[4:7], v[132:135], a[128:131], v[4:7]
ds_read_b64_tr_b16 v[132:133], v218 offset:8200
ds_read_b64_tr_b16 v[134:135], v218 offset:8456
v_mfma_f32_16x16x32_bf16 v[8:11], v[136:139], a[128:131], v[8:11]
v_mfma_f32_16x16x32_bf16 v[40:43], v[136:139], a[132:135], v[40:43]
buffer_load_dwordx4 a[188:191], v193, s[36:39], s69 offen offset:1024 sc1
v_mfma_f32_16x16x32_bf16 v[72:75], v[136:139], a[136:139], v[72:75]
v_min_u32 v227, 59, v209
v_lshlrev_b32 v227, 11, v227
v_mfma_f32_16x16x32_bf16 v[104:107], v[136:139], a[140:143], v[104:107]
s_lshr_b32 s77, s68, 2
s_lshl_b32 s77, s77, 1
v_mfma_f32_16x16x32_bf16 v[108:111], v[140:143], a[140:143], v[108:111]
s_and_b32 s75, s64, 1
s_add_u32 s77, s77, s75
v_mfma_f32_16x16x32_bf16 v[76:79], v[140:143], a[136:139], v[76:79]
s_lshl_b32 s77, s77, 7
s_lshl_b32 s75, s67, 19
v_mfma_f32_16x16x32_bf16 v[44:47], v[140:143], a[132:135], v[44:47]
s_add_u32 s77, s77, s75
s_add_u32 s77, s77, 401408
v_mfma_f32_16x16x32_bf16 v[12:15], v[140:143], a[128:131], v[12:15]
s_waitcnt lgkmcnt(4)
ds_read_b64_tr_b16 v[136:137], v218 offset:8704
v_mfma_f32_16x16x32_bf16 v[16:19], v[144:147], a[128:131], v[16:19]
ds_read_b64_tr_b16 v[138:139], v218 offset:8960
ds_read_b64_tr_b16 v[140:141], v218 offset:8712
v_mfma_f32_16x16x32_bf16 v[48:51], v[144:147], a[132:135], v[48:51]
buffer_load_dword v227, v227, s[44:47], s77 offen
v_mfma_f32_16x16x32_bf16 v[80:83], v[144:147], a[136:139], v[80:83]
ds_read_b64_tr_b16 v[142:143], v218 offset:8968
v_mfma_f32_16x16x32_bf16 v[112:115], v[144:147], a[140:143], v[112:115]
ds_read_b64_tr_b16 v[144:145], v218 offset:9216
ds_read_b64_tr_b16 v[146:147], v218 offset:9472
v_mfma_f32_16x16x32_bf16 v[116:119], v[148:151], a[140:143], v[116:119]
v_mfma_f32_16x16x32_bf16 v[84:87], v[148:151], a[136:139], v[84:87]
v_mfma_f32_16x16x32_bf16 v[52:55], v[148:151], a[132:135], v[52:55]
v_mfma_f32_16x16x32_bf16 v[20:23], v[148:151], a[128:131], v[20:23]
ds_read_b64_tr_b16 v[148:149], v218 offset:9224
ds_read_b64_tr_b16 v[150:151], v218 offset:9480
v_mfma_f32_16x16x32_bf16 v[24:27], v[152:155], a[128:131], v[24:27]
v_mfma_f32_16x16x32_bf16 v[56:59], v[152:155], a[132:135], v[56:59]
v_mfma_f32_16x16x32_bf16 v[88:91], v[152:155], a[136:139], v[88:91]
v_mfma_f32_16x16x32_bf16 v[120:123], v[152:155], a[140:143], v[120:123]
ds_read_b64_tr_b16 v[152:153], v218 offset:9728
ds_read_b64_tr_b16 v[154:155], v218 offset:9984
v_mfma_f32_16x16x32_bf16 v[124:127], v[156:159], a[140:143], v[124:127]
v_mfma_f32_16x16x32_bf16 v[92:95], v[156:159], a[136:139], v[92:95]
v_mfma_f32_16x16x32_bf16 v[60:63], v[156:159], a[132:135], v[60:63]
v_mfma_f32_16x16x32_bf16 v[28:31], v[156:159], a[128:131], v[28:31]
s_waitcnt vmcnt(10) lgkmcnt(6)
ds_read_b64_tr_b16 v[156:157], v218 offset:9736
v_mfma_f32_16x16x32_bf16 v[0:3], v[128:131], a[144:147], v[0:3]
ds_read_b64_tr_b16 v[158:159], v218 offset:9992
v_mfma_f32_16x16x32_bf16 v[32:35], v[128:131], a[148:151], v[32:35]
v_mfma_f32_16x16x32_bf16 v[64:67], v[128:131], a[152:155], v[64:67]
v_mfma_f32_16x16x32_bf16 v[96:99], v[128:131], a[156:159], v[96:99]
v_mfma_f32_16x16x32_bf16 v[100:103], v[132:135], a[156:159], v[100:103]
v_mfma_f32_16x16x32_bf16 v[68:71], v[132:135], a[152:155], v[68:71]
v_mfma_f32_16x16x32_bf16 v[36:39], v[132:135], a[148:151], v[36:39]
s_barrier
v_mfma_f32_16x16x32_bf16 v[4:7], v[132:135], a[144:147], v[4:7]
ds_read_b64_tr_b16 v[128:129], v218 offset:16384
ds_read_b64_tr_b16 v[130:131], v218 offset:16640
v_mfma_f32_16x16x32_bf16 v[8:11], v[136:139], a[144:147], v[8:11]
ds_read_b64_tr_b16 v[132:133], v218 offset:16392
ds_read_b64_tr_b16 v[134:135], v218 offset:16648
v_mfma_f32_16x16x32_bf16 v[40:43], v[136:139], a[148:151], v[40:43]
v_mfma_f32_16x16x32_bf16 v[72:75], v[136:139], a[152:155], v[72:75]
v_mfma_f32_16x16x32_bf16 v[104:107], v[136:139], a[156:159], v[104:107]
ds_read_b64_tr_b16 v[136:137], v218 offset:16896
ds_read_b64_tr_b16 v[138:139], v218 offset:17152
v_mfma_f32_16x16x32_bf16 v[108:111], v[140:143], a[156:159], v[108:111]
v_mfma_f32_16x16x32_bf16 v[76:79], v[140:143], a[152:155], v[76:79]
v_mfma_f32_16x16x32_bf16 v[44:47], v[140:143], a[148:151], v[44:47]
v_mfma_f32_16x16x32_bf16 v[12:15], v[140:143], a[144:147], v[12:15]
s_waitcnt lgkmcnt(6)
ds_read_b64_tr_b16 v[140:141], v218 offset:16904
v_mfma_f32_16x16x32_bf16 v[16:19], v[144:147], a[144:147], v[16:19]
ds_read_b64_tr_b16 v[142:143], v218 offset:17160
v_mfma_f32_16x16x32_bf16 v[48:51], v[144:147], a[148:151], v[48:51]
v_mfma_f32_16x16x32_bf16 v[80:83], v[144:147], a[152:155], v[80:83]
v_mfma_f32_16x16x32_bf16 v[112:115], v[144:147], a[156:159], v[112:115]
ds_read_b64_tr_b16 v[144:145], v218 offset:17408
ds_read_b64_tr_b16 v[146:147], v218 offset:17664
v_mfma_f32_16x16x32_bf16 v[116:119], v[148:151], a[156:159], v[116:119]
v_mfma_f32_16x16x32_bf16 v[84:87], v[148:151], a[152:155], v[84:87]
v_mfma_f32_16x16x32_bf16 v[52:55], v[148:151], a[148:151], v[52:55]
v_mfma_f32_16x16x32_bf16 v[20:23], v[148:151], a[144:147], v[20:23]
ds_read_b64_tr_b16 v[148:149], v218 offset:17416
ds_read_b64_tr_b16 v[150:151], v218 offset:17672
v_mfma_f32_16x16x32_bf16 v[24:27], v[152:155], a[144:147], v[24:27]
v_mfma_f32_16x16x32_bf16 v[56:59], v[152:155], a[148:151], v[56:59]
v_mfma_f32_16x16x32_bf16 v[88:91], v[152:155], a[152:155], v[88:91]
v_mfma_f32_16x16x32_bf16 v[120:123], v[152:155], a[156:159], v[120:123]
ds_read_b64_tr_b16 v[152:153], v218 offset:17920
ds_read_b64_tr_b16 v[154:155], v218 offset:18176
v_mfma_f32_16x16x32_bf16 v[124:127], v[156:159], a[156:159], v[124:127]
v_mfma_f32_16x16x32_bf16 v[92:95], v[156:159], a[152:155], v[92:95]
v_mfma_f32_16x16x32_bf16 v[60:63], v[156:159], a[148:151], v[60:63]
s_add_u32 m0, s71, 16384
v_mfma_f32_16x16x32_bf16 v[28:31], v[156:159], a[144:147], v[28:31]
ds_read_b64_tr_b16 v[156:157], v218 offset:17928
ds_read_b64_tr_b16 v[158:159], v218 offset:18184
s_waitcnt vmcnt(8) lgkmcnt(14)
v_mfma_f32_16x16x32_bf16 v[0:3], v[128:131], a[160:163], v[0:3]
ds_read_b64_tr_b16 v[160:161], v218 offset:24576
ds_read_b64_tr_b16 v[162:163], v218 offset:24832
s_waitcnt vmcnt(7)
v_mfma_f32_16x16x32_bf16 v[32:35], v[128:131], a[164:167], v[32:35]
ds_read_b64_tr_b16 v[164:165], v218 offset:24584
ds_read_b64_tr_b16 v[166:167], v218 offset:24840
s_waitcnt lgkmcnt(15)
v_mfma_f32_16x16x32_bf16 v[4:7], v[132:135], a[160:163], v[4:7]
ds_read_b64_tr_b16 v[168:169], v218 offset:25088
ds_read_b64_tr_b16 v[170:171], v218 offset:25344
v_mfma_f32_16x16x32_bf16 v[36:39], v[132:135], a[164:167], v[36:39]
ds_read_b64_tr_b16 v[172:173], v218 offset:25096
ds_read_b64_tr_b16 v[174:175], v218 offset:25352
s_waitcnt lgkmcnt(15)
v_mfma_f32_16x16x32_bf16 v[8:11], v[136:139], a[160:163], v[8:11]
buffer_load_dwordx4 v222, s[44:47], s72 offen offset:0 lds
v_mfma_f32_16x16x32_bf16 v[40:43], v[136:139], a[164:167], v[40:43]
ds_read_b64_tr_b16 v[176:177], v218 offset:25600
ds_read_b64_tr_b16 v[178:179], v218 offset:25856
v_mfma_f32_16x16x32_bf16 v[12:15], v[140:143], a[160:163], v[12:15]
s_barrier
v_mfma_f32_16x16x32_bf16 v[44:47], v[140:143], a[164:167], v[44:47]
ds_read_b64_tr_b16 v[180:181], v218 offset:25608
ds_read_b64_tr_b16 v[182:183], v218 offset:25864
s_waitcnt lgkmcnt(15)
v_mfma_f32_16x16x32_bf16 v[16:19], v[144:147], a[160:163], v[16:19]
ds_read_b64_tr_b16 v[184:185], v218 offset:26112
ds_read_b64_tr_b16 v[186:187], v218 offset:26368
v_mfma_f32_16x16x32_bf16 v[48:51], v[144:147], a[164:167], v[48:51]
ds_read_b64_tr_b16 v[188:189], v218 offset:26120
ds_read_b64_tr_b16 v[190:191], v218 offset:26376
v_mfma_f32_16x16x32_bf16 v[20:23], v[148:151], a[160:163], v[20:23]
buffer_load_dwordx4 v223, s[44:47], s72 offen offset:1024 lds
v_mfma_f32_16x16x32_bf16 v[52:55], v[148:151], a[164:167], v[52:55]
ds_read_b128 a[64:67], v219 offset:49152
ds_read_b128 a[68:71], v219 offset:53248
s_waitcnt lgkmcnt(15)
v_mfma_f32_16x16x32_bf16 v[24:27], v[152:155], a[160:163], v[24:27]
ds_read_b128 a[72:75], v219 offset:49664
ds_read_b128 a[76:79], v219 offset:53760
v_mfma_f32_16x16x32_bf16 v[56:59], v[152:155], a[164:167], v[56:59]
buffer_load_dwordx4 v224, s[44:47], s72 offen offset:2048 lds
v_mfma_f32_16x16x32_bf16 v[28:31], v[156:159], a[160:163], v[28:31]
ds_read_b128 a[80:83], v219 offset:50176
ds_read_b128 a[84:87], v219 offset:54272
v_mfma_f32_16x16x32_bf16 v[60:63], v[156:159], a[164:167], v[60:63]
ds_read_b128 a[88:91], v219 offset:50688
ds_read_b128 a[92:95], v219 offset:54784
s_waitcnt vmcnt(7)
v_mfma_f32_16x16x32_bf16 v[0:3], v[160:163], a[176:179], v[0:3]
buffer_load_dwordx4 v225, s[44:47], s72 offen offset:3072 lds
s_waitcnt vmcnt(7)
v_mfma_f32_16x16x32_bf16 v[32:35], v[160:163], a[180:183], v[32:35]
s_add_u32 s72, s72, 8192
s_add_u32 m0, s71, 32768
s_waitcnt lgkmcnt(15)
v_mfma_f32_16x16x32_bf16 v[4:7], v[164:167], a[176:179], v[4:7]
v_mfma_f32_16x16x32_bf16 v[36:39], v[164:167], a[180:183], v[36:39]
v_mfma_f32_16x16x32_bf16 v[8:11], v[168:171], a[176:179], v[8:11]
buffer_load_dwordx4 v222, s[44:47], s72 offen offset:0 lds
v_mfma_f32_16x16x32_bf16 v[40:43], v[168:171], a[180:183], v[40:43]
v_mfma_f32_16x16x32_bf16 v[12:15], v[172:175], a[176:179], v[12:15]
v_mfma_f32_16x16x32_bf16 v[44:47], v[172:175], a[180:183], v[44:47]
s_waitcnt lgkmcnt(14)
v_mfma_f32_16x16x32_bf16 v[16:19], v[176:179], a[176:179], v[16:19]
v_cvt_pk_bf16_f32 v230, v0, v1
v_accvgpr_write_b32 a0, v230
v_mfma_f32_16x16x32_bf16 v[48:51], v[176:179], a[180:183], v[48:51]
v_cvt_pk_bf16_f32 v231, v2, v3
v_accvgpr_write_b32 a1, v231
s_waitcnt lgkmcnt(12)
v_mfma_f32_16x16x32_bf16 v[20:23], v[180:183], a[176:179], v[20:23]
v_cvt_pk_bf16_f32 v230, v4, v5
v_accvgpr_write_b32 a2, v230
v_mfma_f32_16x16x32_bf16 v[52:55], v[180:183], a[180:183], v[52:55]
v_cvt_pk_bf16_f32 v231, v6, v7
v_accvgpr_write_b32 a3, v231
s_waitcnt lgkmcnt(10)
v_mfma_f32_16x16x32_bf16 v[24:27], v[184:187], a[176:179], v[24:27]
v_cvt_pk_bf16_f32 v230, v32, v33
v_accvgpr_write_b32 a16, v230
v_mfma_f32_16x16x32_bf16 v[56:59], v[184:187], a[180:183], v[56:59]
v_cvt_pk_bf16_f32 v231, v34, v35
v_accvgpr_write_b32 a17, v231
s_waitcnt lgkmcnt(8)
v_mfma_f32_16x16x32_bf16 v[28:31], v[188:191], a[176:179], v[28:31]
v_cvt_pk_bf16_f32 v230, v36, v37
v_accvgpr_write_b32 a18, v230
v_mfma_f32_16x16x32_bf16 v[60:63], v[188:191], a[180:183], v[60:63]
buffer_load_dwordx4 v223, s[44:47], s72 offen offset:1024 lds
v_mfma_f32_16x16x32_bf16 v[64:67], v[128:131], a[168:171], v[64:67]
v_cvt_pk_bf16_f32 v231, v38, v39
v_accvgpr_write_b32 a19, v231
v_mfma_f32_16x16x32_bf16 v[96:99], v[128:131], a[172:175], v[96:99]
v_cvt_pk_bf16_f32 v230, v8, v9
v_accvgpr_write_b32 a4, v230
v_mfma_f32_16x16x32_bf16 v[68:71], v[132:135], a[168:171], v[68:71]
v_cvt_pk_bf16_f32 v231, v10, v11
v_accvgpr_write_b32 a5, v231
v_mfma_f32_16x16x32_bf16 v[100:103], v[132:135], a[172:175], v[100:103]
v_cvt_pk_bf16_f32 v230, v12, v13
v_accvgpr_write_b32 a6, v230
v_mfma_f32_16x16x32_bf16 v[72:75], v[136:139], a[168:171], v[72:75]
buffer_load_dwordx4 v224, s[44:47], s72 offen offset:2048 lds
v_mfma_f32_16x16x32_bf16 v[104:107], v[136:139], a[172:175], v[104:107]
v_cvt_pk_bf16_f32 v231, v14, v15
v_accvgpr_write_b32 a7, v231
v_mfma_f32_16x16x32_bf16 v[76:79], v[140:143], a[168:171], v[76:79]
v_cvt_pk_bf16_f32 v230, v40, v41
v_accvgpr_write_b32 a20, v230
v_mfma_f32_16x16x32_bf16 v[108:111], v[140:143], a[172:175], v[108:111]
v_cvt_pk_bf16_f32 v231, v42, v43
v_accvgpr_write_b32 a21, v231
v_mfma_f32_16x16x32_bf16 v[80:83], v[144:147], a[168:171], v[80:83]
v_cvt_pk_bf16_f32 v230, v44, v45
v_accvgpr_write_b32 a22, v230
v_mfma_f32_16x16x32_bf16 v[112:115], v[144:147], a[172:175], v[112:115]
v_cvt_pk_bf16_f32 v231, v46, v47
v_accvgpr_write_b32 a23, v231
v_mfma_f32_16x16x32_bf16 v[84:87], v[148:151], a[168:171], v[84:87]
buffer_load_dwordx4 v225, s[44:47], s72 offen offset:3072 lds
s_add_u32 s72, s72, 8192
v_mfma_f32_16x16x32_bf16 v[116:119], v[148:151], a[172:175], v[116:119]
v_cvt_pk_bf16_f32 v230, v16, v17
v_accvgpr_write_b32 a8, v230
v_mfma_f32_16x16x32_bf16 v[88:91], v[152:155], a[168:171], v[88:91]
v_cvt_pk_bf16_f32 v231, v18, v19
v_accvgpr_write_b32 a9, v231
v_mfma_f32_16x16x32_bf16 v[120:123], v[152:155], a[172:175], v[120:123]
v_cvt_pk_bf16_f32 v230, v20, v21
v_accvgpr_write_b32 a10, v230
v_mfma_f32_16x16x32_bf16 v[92:95], v[156:159], a[168:171], v[92:95]
v_cvt_pk_bf16_f32 v231, v22, v23
v_accvgpr_write_b32 a11, v231
v_mfma_f32_16x16x32_bf16 v[124:127], v[156:159], a[172:175], v[124:127]
v_cvt_pk_bf16_f32 v230, v48, v49
v_accvgpr_write_b32 a24, v230
s_waitcnt vmcnt(10)
v_mfma_f32_16x16x32_bf16 v[64:67], v[160:163], a[184:187], v[64:67]
v_cvt_pk_bf16_f32 v231, v50, v51
v_accvgpr_write_b32 a25, v231
s_waitcnt vmcnt(9)
v_mfma_f32_16x16x32_bf16 v[96:99], v[160:163], a[188:191], v[96:99]
v_cvt_pk_bf16_f32 v230, v52, v53
v_accvgpr_write_b32 a26, v230
v_mfma_f32_16x16x32_bf16 v[68:71], v[164:167], a[184:187], v[68:71]
v_cvt_pk_bf16_f32 v231, v54, v55
v_accvgpr_write_b32 a27, v231
v_mfma_f32_16x16x32_bf16 v[100:103], v[164:167], a[188:191], v[100:103]
v_cvt_pk_bf16_f32 v230, v24, v25
v_accvgpr_write_b32 a12, v230
v_mfma_f32_16x16x32_bf16 v[72:75], v[168:171], a[184:187], v[72:75]
v_cvt_pk_bf16_f32 v231, v26, v27
v_accvgpr_write_b32 a13, v231
v_mfma_f32_16x16x32_bf16 v[104:107], v[168:171], a[188:191], v[104:107]
v_cvt_pk_bf16_f32 v230, v28, v29
v_accvgpr_write_b32 a14, v230
v_mfma_f32_16x16x32_bf16 v[76:79], v[172:175], a[184:187], v[76:79]
v_cvt_pk_bf16_f32 v231, v30, v31
v_accvgpr_write_b32 a15, v231
v_mfma_f32_16x16x32_bf16 v[108:111], v[172:175], a[188:191], v[108:111]
v_cvt_pk_bf16_f32 v230, v56, v57
v_accvgpr_write_b32 a28, v230
v_mfma_f32_16x16x32_bf16 v[80:83], v[176:179], a[184:187], v[80:83]
v_cvt_pk_bf16_f32 v231, v58, v59
v_accvgpr_write_b32 a29, v231
v_mfma_f32_16x16x32_bf16 v[112:115], v[176:179], a[188:191], v[112:115]
v_cvt_pk_bf16_f32 v230, v60, v61
v_accvgpr_write_b32 a30, v230
v_mfma_f32_16x16x32_bf16 v[84:87], v[180:183], a[184:187], v[84:87]
v_cvt_pk_bf16_f32 v231, v62, v63
v_accvgpr_write_b32 a31, v231
v_mfma_f32_16x16x32_bf16 v[116:119], v[180:183], a[188:191], v[116:119]
v_cvt_pk_bf16_f32 v230, v64, v65
v_accvgpr_write_b32 a32, v230
v_mfma_f32_16x16x32_bf16 v[88:91], v[184:187], a[184:187], v[88:91]
v_cvt_pk_bf16_f32 v231, v66, v67
v_accvgpr_write_b32 a33, v231
v_mfma_f32_16x16x32_bf16 v[120:123], v[184:187], a[188:191], v[120:123]
v_cvt_pk_bf16_f32 v230, v68, v69
v_accvgpr_write_b32 a34, v230
v_mfma_f32_16x16x32_bf16 v[92:95], v[188:191], a[184:187], v[92:95]
v_cvt_pk_bf16_f32 v231, v70, v71
v_accvgpr_write_b32 a35, v231
v_mfma_f32_16x16x32_bf16 v[124:127], v[188:191], a[188:191], v[124:127]
s_waitcnt lgkmcnt(4)
v_mfma_f32_16x16x32_bf16 v[128:131], a[64:67], a[0:3], 0
v_cvt_pk_bf16_f32 v230, v96, v97
v_accvgpr_write_b32 a48, v230
v_mfma_f32_16x16x32_bf16 v[136:139], a[64:67], a[16:19], 0
v_cvt_pk_bf16_f32 v231, v98, v99
v_accvgpr_write_b32 a49, v231
v_mfma_f32_16x16x32_bf16 v[140:143], a[68:71], a[16:19], 0
v_cvt_pk_bf16_f32 v230, v100, v101
v_accvgpr_write_b32 a50, v230
v_mfma_f32_16x16x32_bf16 v[132:135], a[68:71], a[0:3], 0
v_cvt_pk_bf16_f32 v231, v102, v103
v_accvgpr_write_b32 a51, v231
v_mfma_f32_16x16x32_bf16 v[128:131], a[72:75], a[4:7], v[128:131]
v_cvt_pk_bf16_f32 v230, v72, v73
v_accvgpr_write_b32 a36, v230
v_mfma_f32_16x16x32_bf16 v[136:139], a[72:75], a[20:23], v[136:139]
v_cvt_pk_bf16_f32 v231, v74, v75
v_accvgpr_write_b32 a37, v231
v_mfma_f32_16x16x32_bf16 v[140:143], a[76:79], a[20:23], v[140:143]
v_cvt_pk_bf16_f32 v230, v76, v77
v_accvgpr_write_b32 a38, v230
v_mfma_f32_16x16x32_bf16 v[132:135], a[76:79], a[4:7], v[132:135]
s_waitcnt lgkmcnt(0)
v_mfma_f32_16x16x32_bf16 v[128:131], a[80:83], a[8:11], v[128:131]
v_cvt_pk_bf16_f32 v231, v78, v79
v_accvgpr_write_b32 a39, v231
v_mfma_f32_16x16x32_bf16 v[136:139], a[80:83], a[24:27], v[136:139]
v_cvt_pk_bf16_f32 v230, v104, v105
v_accvgpr_write_b32 a52, v230
v_mfma_f32_16x16x32_bf16 v[140:143], a[84:87], a[24:27], v[140:143]
v_cvt_pk_bf16_f32 v231, v106, v107
v_accvgpr_write_b32 a53, v231
v_mfma_f32_16x16x32_bf16 v[132:135], a[84:87], a[8:11], v[132:135]
v_cvt_pk_bf16_f32 v230, v108, v109
v_accvgpr_write_b32 a54, v230
v_mfma_f32_16x16x32_bf16 v[128:131], a[88:91], a[12:15], v[128:131]
v_cvt_pk_bf16_f32 v231, v110, v111
v_accvgpr_write_b32 a55, v231
v_mfma_f32_16x16x32_bf16 v[136:139], a[88:91], a[28:31], v[136:139]
v_cvt_pk_bf16_f32 v230, v80, v81
v_accvgpr_write_b32 a40, v230
v_mfma_f32_16x16x32_bf16 v[140:143], a[92:95], a[28:31], v[140:143]
v_cvt_pk_bf16_f32 v231, v82, v83
v_accvgpr_write_b32 a41, v231
v_mfma_f32_16x16x32_bf16 v[132:135], a[92:95], a[12:15], v[132:135]
v_cvt_pk_bf16_f32 v230, v84, v85
v_accvgpr_write_b32 a42, v230
v_mfma_f32_16x16x32_bf16 v[144:147], a[64:67], a[32:35], 0
v_cvt_pk_bf16_f32 v231, v86, v87
v_accvgpr_write_b32 a43, v231
v_mfma_f32_16x16x32_bf16 v[152:155], a[64:67], a[48:51], 0
v_cvt_pk_bf16_f32 v230, v112, v113
v_accvgpr_write_b32 a56, v230
v_mfma_f32_16x16x32_bf16 v[156:159], a[68:71], a[48:51], 0
v_cvt_pk_bf16_f32 v231, v114, v115
v_accvgpr_write_b32 a57, v231
v_mfma_f32_16x16x32_bf16 v[148:151], a[68:71], a[32:35], 0
v_cvt_pk_bf16_f32 v230, v116, v117
v_accvgpr_write_b32 a58, v230
v_mfma_f32_16x16x32_bf16 v[144:147], a[72:75], a[36:39], v[144:147]
v_cvt_pk_bf16_f32 v231, v118, v119
v_accvgpr_write_b32 a59, v231
v_mfma_f32_16x16x32_bf16 v[152:155], a[72:75], a[52:55], v[152:155]
v_cvt_pk_bf16_f32 v230, v88, v89
v_accvgpr_write_b32 a44, v230
v_mfma_f32_16x16x32_bf16 v[156:159], a[76:79], a[52:55], v[156:159]
v_cvt_pk_bf16_f32 v231, v90, v91
v_accvgpr_write_b32 a45, v231
v_mfma_f32_16x16x32_bf16 v[148:151], a[76:79], a[36:39], v[148:151]
v_lshlrev_b32 v226, 3, v209
s_lshl_b32 s33, s64, 9
s_add_u32 s33, s33, 16781312
buffer_load_dwordx2 v[228:229], v226, s[36:39], s33 offen sc1
v_mfma_f32_16x16x32_bf16 v[144:147], a[80:83], a[40:43], v[144:147]
v_cvt_pk_bf16_f32 v230, v92, v93
v_accvgpr_write_b32 a46, v230
v_mfma_f32_16x16x32_bf16 v[152:155], a[80:83], a[56:59], v[152:155]
v_cvt_pk_bf16_f32 v231, v94, v95
v_accvgpr_write_b32 a47, v231
v_mfma_f32_16x16x32_bf16 v[156:159], a[84:87], a[56:59], v[156:159]
v_cvt_pk_bf16_f32 v230, v120, v121
v_accvgpr_write_b32 a60, v230
v_mfma_f32_16x16x32_bf16 v[148:151], a[84:87], a[40:43], v[148:151]
ds_read_b128 a[64:67], v219 offset:0
s_add_u32 m0, s24, 65536
v_mfma_f32_16x16x32_bf16 v[144:147], a[88:91], a[44:47], v[144:147]
v_cvt_pk_bf16_f32 v231, v122, v123
v_accvgpr_write_b32 a61, v231
v_cvt_pk_bf16_f32 v230, v124, v125
v_accvgpr_write_b32 a62, v230
v_cvt_pk_bf16_f32 v231, v126, v127
v_accvgpr_write_b32 a63, v231
s_add_u32 s74, s74, 0
buffer_load_dwordx4 v252, s[48:51], s74 offen lds
v_mfma_f32_16x16x32_bf16 v[152:155], a[88:91], a[60:63], v[152:155]
ds_read_b128 a[68:71], v219 offset:4096
ds_read_b128 a[72:75], v219 offset:512
v_mfma_f32_16x16x32_bf16 v[156:159], a[92:95], a[60:63], v[156:159]
v_max3_f32 v214, v128, v129, v130
v_max3_f32 v214, v214, v131, v132
v_mfma_f32_16x16x32_bf16 v[148:151], a[92:95], a[44:47], v[148:151]
v_max3_f32 v214, v214, v133, v134
v_max_f32 v214, v214, v135
v_mov_b32 v216, v214
v_max3_f32 v215, v136, v137, v138
v_max3_f32 v215, v215, v139, v140
v_max3_f32 v215, v215, v141, v142
v_max_f32 v215, v215, v143
v_mov_b32 v217, v215
v_permlane16_swap_b32 v214, v216
s_nop 0
v_permlane16_swap_b32 v215, v217
v_max_f32 v214, v214, v216
v_max_f32 v215, v215, v217
v_mov_b32 v216, v214
v_mov_b32 v217, v215
s_nop 0
v_permlane32_swap_b32 v214, v216
v_permlane32_swap_b32 v215, v217
v_max_f32 v214, v214, v216
v_max_f32 v215, v215, v217
v_sub_f32 v176, 0, v214
v_sub_f32 v177, 0, v214
v_sub_f32 v178, 0, v214
v_sub_f32 v179, 0, v214
v_sub_f32 v180, 0, v215
v_sub_f32 v181, 0, v215
v_sub_f32 v182, 0, v215
v_sub_f32 v183, 0, v215
v_sub_f32 v128, v128, v214
v_sub_f32 v129, v129, v214
v_sub_f32 v130, v130, v214
v_sub_f32 v131, v131, v214
v_sub_f32 v132, v132, v214
v_sub_f32 v133, v133, v214
v_sub_f32 v134, v134, v214
v_sub_f32 v135, v135, v214
v_sub_f32 v136, v136, v215
v_sub_f32 v137, v137, v215
v_sub_f32 v138, v138, v215
v_sub_f32 v139, v139, v215
v_sub_f32 v140, v140, v215
v_sub_f32 v141, v141, v215
v_sub_f32 v142, v142, v215
v_sub_f32 v143, v143, v215
v_exp_f32 v128, v128
v_exp_f32 v129, v129
v_exp_f32 v130, v130
v_exp_f32 v131, v131
v_exp_f32 v132, v132
v_exp_f32 v133, v133
v_exp_f32 v134, v134
v_exp_f32 v135, v135
v_exp_f32 v136, v136
v_exp_f32 v137, v137
v_cvt_pk_bf16_f32 v160, v128, v129
v_exp_f32 v138, v138
v_exp_f32 v139, v139
v_cvt_pk_bf16_f32 v161, v130, v131
v_exp_f32 v140, v140
v_exp_f32 v141, v141
v_cvt_pk_bf16_f32 v162, v132, v133
v_exp_f32 v142, v142
v_exp_f32 v143, v143
v_cvt_pk_bf16_f32 v163, v134, v135
v_cvt_pk_bf16_f32 v164, v136, v137
v_cvt_pk_bf16_f32 v165, v138, v139
s_waitcnt lgkmcnt(0)
v_cvt_pk_bf16_f32 v166, v140, v141
v_cvt_pk_bf16_f32 v167, v142, v143
v_mfma_f32_16x16x32_bf16 v[128:131], a[64:67], a[0:3], v[176:179]
ds_read_b128 a[76:79], v219 offset:4608
ds_read_b64_tr_b16 a[96:97], v220 offset:8192
v_mfma_f32_16x16x32_bf16 v[136:139], a[64:67], a[16:19], v[180:183]
ds_read_b64_tr_b16 a[98:99], v220 offset:12288
s_add_u32 m0, s24, 66560
v_mfma_f32_16x16x32_bf16 v[140:143], a[68:71], a[16:19], v[180:183]
s_add_u32 s91, s91, 0
buffer_load_dwordx4 v252, s[52:55], s91 offen lds
v_mfma_f32_16x16x32_bf16 v[132:135], a[68:71], a[0:3], v[176:179]
ds_read_b128 a[80:83], v219 offset:1024
ds_read_b64_tr_b16 a[100:101], v221 offset:8192
v_mfma_f32_16x16x32_bf16 v[128:131], a[72:75], a[4:7], v[128:131]
ds_read_b64_tr_b16 a[102:103], v221 offset:12288
ds_read_b64_tr_b16 a[104:105], v220 offset:8704
v_mfma_f32_16x16x32_bf16 v[136:139], a[72:75], a[20:23], v[136:139]
ds_read_b128 a[84:87], v219 offset:5120
ds_read_b64_tr_b16 a[106:107], v220 offset:12800
s_waitcnt lgkmcnt(8)
v_mfma_f32_16x16x32_bf16 v[140:143], a[76:79], a[20:23], v[140:143]
ds_read_b64_tr_b16 a[108:109], v221 offset:8704
ds_read_b64_tr_b16 a[110:111], v221 offset:12800
v_mfma_f32_16x16x32_bf16 v[132:135], a[76:79], a[4:7], v[132:135]
ds_read_b128 a[88:91], v219 offset:1536
s_waitcnt lgkmcnt(0)
v_mfma_f32_16x16x32_bf16 v[128:131], a[80:83], a[8:11], v[128:131]
s_add_u32 m0, s24, 67584
s_add_u32 s74, s74, s25
buffer_load_dwordx4 v252, s[48:51], s74 offen lds
v_mfma_f32_16x16x32_bf16 v[136:139], a[80:83], a[24:27], v[136:139]
ds_read_b128 a[92:95], v219 offset:5632
ds_read_b64_tr_b16 a[112:113], v220 offset:9216
v_mfma_f32_16x16x32_bf16 v[140:143], a[84:87], a[24:27], v[140:143]
ds_read_b64_tr_b16 a[114:115], v220 offset:13312
ds_read_b64_tr_b16 a[116:117], v221 offset:9216
v_mfma_f32_16x16x32_bf16 v[132:135], a[84:87], a[8:11], v[132:135]
ds_read_b64_tr_b16 a[118:119], v221 offset:13312
ds_read_b64_tr_b16 a[120:121], v220 offset:9728
v_mfma_f32_16x16x32_bf16 v[128:131], a[88:91], a[12:15], v[128:131]
ds_read_b64_tr_b16 a[122:123], v220 offset:13824
ds_read_b64_tr_b16 a[124:125], v221 offset:9728
v_mfma_f32_16x16x32_bf16 v[136:139], a[88:91], a[28:31], v[136:139]
ds_read_b64_tr_b16 a[126:127], v221 offset:13824
s_add_u32 m0, s24, 68608
s_waitcnt lgkmcnt(8)
v_mfma_f32_16x16x32_bf16 v[140:143], a[92:95], a[28:31], v[140:143]
s_add_u32 s91, s91, s25
buffer_load_dwordx4 v252, s[52:55], s91 offen lds
v_mfma_f32_16x16x32_bf16 v[132:135], a[92:95], a[12:15], v[132:135]
v_max3_f32 v214, v144, v145, v146
v_max3_f32 v214, v214, v147, v148
v_mfma_f32_16x16x32_bf16 v[192:195], v[232:235], v[160:163], 0
v_max3_f32 v214, v214, v149, v150
v_max_f32 v214, v214, v151
v_mfma_f32_16x16x32_bf16 v[196:199], v[232:235], v[164:167], 0
v_mov_b32 v216, v214
v_mfma_f32_16x16x32_bf16 v[32:35], a[96:99], v[164:167], 0
v_max3_f32 v215, v152, v153, v154
v_max3_f32 v215, v215, v155, v156
v_mfma_f32_16x16x32_bf16 v[0:3], a[96:99], v[160:163], 0
s_add_u32 m0, s24, 69632
s_add_u32 s74, s74, s25
buffer_load_dwordx4 v252, s[48:51], s74 offen lds
v_mfma_f32_16x16x32_bf16 v[4:7], a[100:103], v[160:163], 0
v_max3_f32 v215, v215, v157, v158
v_max_f32 v215, v215, v159
v_mfma_f32_16x16x32_bf16 v[36:39], a[100:103], v[164:167], 0
v_mov_b32 v217, v215
v_permlane16_swap_b32 v214, v216
v_mfma_f32_16x16x32_bf16 v[40:43], a[104:107], v[164:167], 0
v_permlane16_swap_b32 v215, v217
v_max_f32 v214, v214, v216
v_mfma_f32_16x16x32_bf16 v[8:11], a[104:107], v[160:163], 0
v_max_f32 v215, v215, v217
v_mov_b32 v216, v214
v_mfma_f32_16x16x32_bf16 v[12:15], a[108:111], v[160:163], 0
v_mov_b32 v217, v215
v_permlane32_swap_b32 v214, v216
v_mfma_f32_16x16x32_bf16 v[44:47], a[108:111], v[164:167], 0
s_add_u32 m0, s24, 70656
s_add_u32 s91, s91, s25
buffer_load_dwordx4 v252, s[52:55], s91 offen lds
s_waitcnt lgkmcnt(0)
v_mfma_f32_16x16x32_bf16 v[48:51], a[112:115], v[164:167], 0
v_permlane32_swap_b32 v215, v217
v_max_f32 v214, v214, v216
v_mfma_f32_16x16x32_bf16 v[16:19], a[112:115], v[160:163], 0
v_max_f32 v215, v215, v217
v_sub_f32 v184, 0, v214
v_mfma_f32_16x16x32_bf16 v[20:23], a[116:119], v[160:163], 0
v_sub_f32 v185, 0, v214
v_sub_f32 v186, 0, v214
v_mfma_f32_16x16x32_bf16 v[52:55], a[116:119], v[164:167], 0
v_sub_f32 v187, 0, v214
v_sub_f32 v188, 0, v215
v_mfma_f32_16x16x32_bf16 v[56:59], a[120:123], v[164:167], 0
v_sub_f32 v189, 0, v215
v_sub_f32 v190, 0, v215
v_mfma_f32_16x16x32_bf16 v[24:27], a[120:123], v[160:163], 0
s_add_u32 m0, s24, 71680
s_add_u32 s74, s74, s25
buffer_load_dwordx4 v252, s[48:51], s74 offen lds
v_mfma_f32_16x16x32_bf16 v[28:31], a[124:127], v[160:163], 0
v_sub_f32 v191, 0, v215
v_sub_f32 v144, v144, v214
v_mfma_f32_16x16x32_bf16 v[60:63], a[124:127], v[164:167], 0
v_sub_f32 v145, v145, v214
v_sub_f32 v146, v146, v214
v_sub_f32 v147, v147, v214
v_sub_f32 v148, v148, v214
v_sub_f32 v149, v149, v214
v_sub_f32 v150, v150, v214
v_sub_f32 v151, v151, v214
v_sub_f32 v152, v152, v215
v_sub_f32 v153, v153, v215
v_sub_f32 v154, v154, v215
v_sub_f32 v155, v155, v215
v_sub_f32 v156, v156, v215
v_sub_f32 v157, v157, v215
v_sub_f32 v158, v158, v215
v_sub_f32 v159, v159, v215
v_exp_f32 v144, v144
v_exp_f32 v145, v145
v_exp_f32 v146, v146
v_exp_f32 v147, v147
v_exp_f32 v148, v148
v_cvt_pk_bf16_f32 v168, v144, v145
v_exp_f32 v149, v149
v_exp_f32 v150, v150
v_cvt_pk_bf16_f32 v169, v146, v147
v_exp_f32 v151, v151
v_cvt_pk_bf16_f32 v170, v148, v149
v_exp_f32 v152, v152
v_exp_f32 v153, v153
v_exp_f32 v154, v154
v_cvt_pk_bf16_f32 v171, v150, v151
v_exp_f32 v155, v155
v_exp_f32 v156, v156
v_cvt_pk_bf16_f32 v172, v152, v153
v_exp_f32 v157, v157
v_exp_f32 v158, v158
v_cvt_pk_bf16_f32 v173, v154, v155
v_exp_f32 v159, v159
v_cvt_pk_bf16_f32 v174, v156, v157
v_cvt_pk_bf16_f32 v175, v158, v159
s_waitcnt vmcnt(7)
v_cmp_ne_u64 vcc, s[80:81], v[228:229]
s_nop 0
s_cbranch_vccz .Lk_rt_ok
s_mov_b32 s89, 0
.Lk_rt:
buffer_load_dwordx2 v[228:229], v226, s[36:39], s33 offen sc1
s_waitcnt vmcnt(0)
v_cmp_ne_u64 vcc, s[80:81], v[228:229]
s_nop 0
s_cbranch_vccz .Lk_rt_ok
s_add_u32 s89, s89, 1
s_cmp_lt_u32 s89, 0x20000
s_cbranch_scc1 .Lk_rt
.Lk_rt_ok:
s_barrier
v_mfma_f32_16x16x32_bf16 v[144:147], a[64:67], a[32:35], v[184:187]
v_exp_f32 v128, v128
v_mfma_f32_16x16x32_bf16 v[152:155], a[64:67], a[48:51], v[188:191]
v_exp_f32 v129, v129
v_mfma_f32_16x16x32_bf16 v[156:159], a[68:71], a[48:51], v[188:191]
s_add_u32 m0, s71, 49152
ds_read_b128 a[64:67], v219 offset:16384
v_mfma_f32_16x16x32_bf16 v[148:151], a[68:71], a[32:35], v[184:187]
v_exp_f32 v130, v130
v_mfma_f32_16x16x32_bf16 v[144:147], a[72:75], a[36:39], v[144:147]
v_exp_f32 v131, v131
v_mfma_f32_16x16x32_bf16 v[152:155], a[72:75], a[52:55], v[152:155]
ds_read_b128 a[68:71], v219 offset:20480
v_cvt_pk_bf16_f32 v160, v128, v129
v_mfma_f32_16x16x32_bf16 v[156:159], a[76:79], a[52:55], v[156:159]
buffer_load_dwordx4 v222, s[44:47], s72 offen offset:0 lds
v_mfma_f32_16x16x32_bf16 v[148:151], a[76:79], a[36:39], v[148:151]
v_exp_f32 v132, v132
v_mfma_f32_16x16x32_bf16 v[144:147], a[80:83], a[40:43], v[144:147]
ds_read_b128 a[72:75], v219 offset:16896
v_cvt_pk_bf16_f32 v161, v130, v131
v_mfma_f32_16x16x32_bf16 v[152:155], a[80:83], a[56:59], v[152:155]
v_exp_f32 v133, v133
v_mfma_f32_16x16x32_bf16 v[156:159], a[84:87], a[56:59], v[156:159]
buffer_load_dwordx4 v223, s[44:47], s72 offen offset:1024 lds
v_mfma_f32_16x16x32_bf16 v[148:151], a[84:87], a[40:43], v[148:151]
ds_read_b128 a[76:79], v219 offset:20992
ds_read_b128 a[80:83], v219 offset:17408
v_mfma_f32_16x16x32_bf16 v[144:147], a[88:91], a[44:47], v[144:147]
v_exp_f32 v134, v134
v_mfma_f32_16x16x32_bf16 v[152:155], a[88:91], a[60:63], v[152:155]
v_exp_f32 v135, v135
v_mfma_f32_16x16x32_bf16 v[156:159], a[92:95], a[60:63], v[156:159]
ds_read_b128 a[84:87], v219 offset:21504
v_cvt_pk_bf16_f32 v162, v132, v133
v_mfma_f32_16x16x32_bf16 v[148:151], a[92:95], a[44:47], v[148:151]
v_exp_f32 v136, v136
v_mfma_f32_16x16x32_bf16 v[200:203], v[232:235], v[168:171], 0
buffer_load_dwordx4 v224, s[44:47], s72 offen offset:2048 lds
v_mfma_f32_16x16x32_bf16 v[204:207], v[232:235], v[172:175], 0
ds_read_b128 a[88:91], v219 offset:17920
ds_read_b128 a[92:95], v219 offset:22016
v_mfma_f32_16x16x32_bf16 v[96:99], a[96:99], v[172:175], 0
v_exp_f32 v137, v137
v_mfma_f32_16x16x32_bf16 v[64:67], a[96:99], v[168:171], 0
ds_read_b64_tr_b16 a[96:97], v220 offset:24576
v_cvt_pk_bf16_f32 v163, v134, v135
v_mfma_f32_16x16x32_bf16 v[68:71], a[100:103], v[168:171], 0
v_exp_f32 v138, v138
v_mfma_f32_16x16x32_bf16 v[100:103], a[100:103], v[172:175], 0
buffer_load_dwordx4 v225, s[44:47], s72 offen offset:3072 lds
v_mfma_f32_16x16x32_bf16 v[104:107], a[104:107], v[172:175], 0
s_add_u32 s72, s72, 8192
ds_read_b64_tr_b16 a[98:99], v220 offset:28672
v_mfma_f32_16x16x32_bf16 v[72:75], a[104:107], v[168:171], 0
v_exp_f32 v139, v139
v_mfma_f32_16x16x32_bf16 v[76:79], a[108:111], v[168:171], 0
v_cvt_pk_bf16_f32 v164, v136, v137
s_add_u32 m0, s71, 0
v_mfma_f32_16x16x32_bf16 v[108:111], a[108:111], v[172:175], 0
v_exp_f32 v140, v140
v_mfma_f32_16x16x32_bf16 v[112:115], a[112:115], v[172:175], 0
ds_read_b64_tr_b16 a[100:101], v221 offset:24576
ds_read_b64_tr_b16 a[102:103], v221 offset:28672
v_mfma_f32_16x16x32_bf16 v[80:83], a[112:115], v[168:171], 0
v_exp_f32 v141, v141
v_mfma_f32_16x16x32_bf16 v[84:87], a[116:119], v[168:171], 0
ds_read_b64_tr_b16 a[104:105], v220 offset:25088
v_cvt_pk_bf16_f32 v165, v138, v139
v_mfma_f32_16x16x32_bf16 v[116:119], a[116:119], v[172:175], 0
buffer_load_dwordx4 v222, s[44:47], s72 offen offset:0 lds
v_mfma_f32_16x16x32_bf16 v[120:123], a[120:123], v[172:175], 0
v_exp_f32 v142, v142
v_mfma_f32_16x16x32_bf16 v[88:91], a[120:123], v[168:171], 0
ds_read_b64_tr_b16 a[106:107], v220 offset:29184
v_cvt_pk_bf16_f32 v166, v140, v141
v_mfma_f32_16x16x32_bf16 v[92:95], a[124:127], v[168:171], 0
v_exp_f32 v143, v143
v_mfma_f32_16x16x32_bf16 v[124:127], a[124:127], v[172:175], 0
v_cvt_pk_bf16_f32 v167, v142, v143
s_waitcnt lgkmcnt(10)
v_mfma_f32_16x16x32_bf16 v[128:131], a[64:67], a[0:3], v[176:179]
v_exp_f32 v144, v144
v_mfma_f32_16x16x32_bf16 v[136:139], a[64:67], a[16:19], v[180:183]
v_exp_f32 v145, v145
v_mfma_f32_16x16x32_bf16 v[140:143], a[68:71], a[16:19], v[180:183]
buffer_load_dwordx4 v223, s[44:47], s72 offen offset:1024 lds
v_mfma_f32_16x16x32_bf16 v[132:135], a[68:71], a[0:3], v[176:179]
ds_read_b64_tr_b16 a[108:109], v221 offset:25088
ds_read_b64_tr_b16 a[110:111], v221 offset:29184
v_mfma_f32_16x16x32_bf16 v[128:131], a[72:75], a[4:7], v[128:131]
v_exp_f32 v146, v146
v_mfma_f32_16x16x32_bf16 v[136:139], a[72:75], a[20:23], v[136:139]
v_exp_f32 v147, v147
v_mfma_f32_16x16x32_bf16 v[140:143], a[76:79], a[20:23], v[140:143]
ds_read_b64_tr_b16 a[112:113], v220 offset:25600
v_cvt_pk_bf16_f32 v168, v144, v145
v_mfma_f32_16x16x32_bf16 v[132:135], a[76:79], a[4:7], v[132:135]
buffer_load_dwordx4 v224, s[44:47], s72 offen offset:2048 lds
s_waitcnt lgkmcnt(9)
v_mfma_f32_16x16x32_bf16 v[128:131], a[80:83], a[8:11], v[128:131]
ds_read_b64_tr_b16 a[114:115], v220 offset:29696
ds_read_b64_tr_b16 a[116:117], v221 offset:25600
v_mfma_f32_16x16x32_bf16 v[136:139], a[80:83], a[24:27], v[136:139]
v_exp_f32 v148, v148
v_mfma_f32_16x16x32_bf16 v[140:143], a[84:87], a[24:27], v[140:143]
v_exp_f32 v149, v149
v_mfma_f32_16x16x32_bf16 v[132:135], a[84:87], a[8:11], v[132:135]
ds_read_b64_tr_b16 a[118:119], v221 offset:29696
v_cvt_pk_bf16_f32 v169, v146, v147
v_mfma_f32_16x16x32_bf16 v[128:131], a[88:91], a[12:15], v[128:131]
v_exp_f32 v150, v150
v_mfma_f32_16x16x32_bf16 v[136:139], a[88:91], a[28:31], v[136:139]
buffer_load_dwordx4 v225, s[44:47], s72 offen offset:3072 lds
s_add_u32 s72, s72, 8192
v_mfma_f32_16x16x32_bf16 v[140:143], a[92:95], a[28:31], v[140:143]
s_add_u32 m0, s24, 72704
s_add_u32 s91, s91, s25
v_mfma_f32_16x16x32_bf16 v[132:135], a[92:95], a[12:15], v[132:135]
ds_read_b64_tr_b16 a[120:121], v220 offset:26112
ds_read_b64_tr_b16 a[122:123], v220 offset:30208
v_mfma_f32_16x16x32_bf16 v[192:195], v[232:235], v[160:163], v[192:195]
v_exp_f32 v151, v151
v_mfma_f32_16x16x32_bf16 v[196:199], v[232:235], v[164:167], v[196:199]
s_waitcnt lgkmcnt(6)
v_cvt_pk_bf16_f32 v170, v148, v149
v_mfma_f32_16x16x32_bf16 v[32:35], a[96:99], v[164:167], v[32:35]
v_exp_f32 v152, v152
v_mfma_f32_16x16x32_bf16 v[0:3], a[96:99], v[160:163], v[0:3]
buffer_load_dwordx4 v252, s[52:55], s91 offen lds
v_mfma_f32_16x16x32_bf16 v[4:7], a[100:103], v[160:163], v[4:7]
ds_read_b64_tr_b16 a[124:125], v221 offset:26112
ds_read_b64_tr_b16 a[126:127], v221 offset:30208
v_mfma_f32_16x16x32_bf16 v[36:39], a[100:103], v[164:167], v[36:39]
v_exp_f32 v153, v153
v_mfma_f32_16x16x32_bf16 v[40:43], a[104:107], v[164:167], v[40:43]
v_cvt_pk_bf16_f32 v171, v150, v151
s_add_u32 m0, s24, 73728
v_mfma_f32_16x16x32_bf16 v[8:11], a[104:107], v[160:163], v[8:11]
s_add_u32 s74, s74, s25
v_mfma_f32_16x16x32_bf16 v[12:15], a[108:111], v[160:163], v[12:15]
v_exp_f32 v154, v154
v_mfma_f32_16x16x32_bf16 v[44:47], a[108:111], v[164:167], v[44:47]
buffer_load_dwordx4 v252, s[48:51], s74 offen lds
s_waitcnt lgkmcnt(0)
v_mfma_f32_16x16x32_bf16 v[48:51], a[112:115], v[164:167], v[48:51]
v_exp_f32 v155, v155
v_mfma_f32_16x16x32_bf16 v[16:19], a[112:115], v[160:163], v[16:19]
v_cvt_pk_bf16_f32 v172, v152, v153
s_add_u32 m0, s24, 74752
v_mfma_f32_16x16x32_bf16 v[20:23], a[116:119], v[160:163], v[20:23]
v_exp_f32 v156, v156
v_mfma_f32_16x16x32_bf16 v[52:55], a[116:119], v[164:167], v[52:55]
s_add_u32 s91, s91, s25
v_cvt_pk_bf16_f32 v173, v154, v155
v_mfma_f32_16x16x32_bf16 v[56:59], a[120:123], v[164:167], v[56:59]
v_exp_f32 v157, v157
v_mfma_f32_16x16x32_bf16 v[24:27], a[120:123], v[160:163], v[24:27]
buffer_load_dwordx4 v252, s[52:55], s91 offen lds
v_mfma_f32_16x16x32_bf16 v[28:31], a[124:127], v[160:163], v[28:31]
v_exp_f32 v158, v158
v_mfma_f32_16x16x32_bf16 v[60:63], a[124:127], v[164:167], v[60:63]
v_exp_f32 v159, v159
v_cvt_pk_bf16_f32 v174, v156, v157
v_cvt_pk_bf16_f32 v175, v158, v159
v_mfma_f32_16x16x32_bf16 v[144:147], a[64:67], a[32:35], v[184:187]
v_exp_f32 v128, v128
v_mfma_f32_16x16x32_bf16 v[152:155], a[64:67], a[48:51], v[188:191]
v_exp_f32 v129, v129
v_mfma_f32_16x16x32_bf16 v[156:159], a[68:71], a[48:51], v[188:191]
s_add_u32 s15, s85, 0
ds_read_b128 a[64:67], v219 offset:32768
v_mfma_f32_16x16x32_bf16 v[148:151], a[68:71], a[32:35], v[184:187]
v_exp_f32 v130, v130
v_mfma_f32_16x16x32_bf16 v[144:147], a[72:75], a[36:39], v[144:147]
v_exp_f32 v131, v131
v_mfma_f32_16x16x32_bf16 v[152:155], a[72:75], a[52:55], v[152:155]
ds_read_b128 a[68:71], v219 offset:36864
v_cvt_pk_bf16_f32 v160, v128, v129
v_mfma_f32_16x16x32_bf16 v[156:159], a[76:79], a[52:55], v[156:159]
buffer_load_dwordx4 a[128:131], v208, s[36:39], s15 offen offset:0 sc1
v_mfma_f32_16x16x32_bf16 v[148:151], a[76:79], a[36:39], v[148:151]
v_exp_f32 v132, v132
v_mfma_f32_16x16x32_bf16 v[144:147], a[80:83], a[40:43], v[144:147]
ds_read_b128 a[72:75], v219 offset:33280
v_cvt_pk_bf16_f32 v161, v130, v131
v_mfma_f32_16x16x32_bf16 v[152:155], a[80:83], a[56:59], v[152:155]
v_exp_f32 v133, v133
v_mfma_f32_16x16x32_bf16 v[156:159], a[84:87], a[56:59], v[156:159]
ds_read_b128 a[76:79], v219 offset:37376
ds_read_b128 a[80:83], v219 offset:33792
v_mfma_f32_16x16x32_bf16 v[148:151], a[84:87], a[40:43], v[148:151]
v_exp_f32 v134, v134
v_mfma_f32_16x16x32_bf16 v[144:147], a[88:91], a[44:47], v[144:147]
buffer_load_dwordx4 a[132:135], v208, s[36:39], s15 offen offset:1024 sc1
v_mfma_f32_16x16x32_bf16 v[152:155], a[88:91], a[60:63], v[152:155]
v_exp_f32 v135, v135
v_mfma_f32_16x16x32_bf16 v[156:159], a[92:95], a[60:63], v[156:159]
ds_read_b128 a[84:87], v219 offset:37888
v_cvt_pk_bf16_f32 v162, v132, v133
v_mfma_f32_16x16x32_bf16 v[148:151], a[92:95], a[44:47], v[148:151]
v_exp_f32 v136, v136
v_mfma_f32_16x16x32_bf16 v[200:203], v[232:235], v[168:171], v[200:203]
ds_read_b128 a[88:91], v219 offset:34304
ds_read_b128 a[92:95], v219 offset:38400
v_mfma_f32_16x16x32_bf16 v[204:207], v[232:235], v[172:175], v[204:207]
v_exp_f32 v137, v137
v_mfma_f32_16x16x32_bf16 v[96:99], a[96:99], v[172:175], v[96:99]
v_cvt_pk_bf16_f32 v163, v134, v135
v_mfma_f32_16x16x32_bf16 v[64:67], a[96:99], v[168:171], v[64:67]
v_exp_f32 v138, v138
v_mfma_f32_16x16x32_bf16 v[68:71], a[100:103], v[168:171], v[68:71]
buffer_load_dwordx4 a[136:139], v208, s[36:39], s15 offen offset:2048 sc1
v_mfma_f32_16x16x32_bf16 v[100:103], a[100:103], v[172:175], v[100:103]
v_exp_f32 v139, v139
v_mfma_f32_16x16x32_bf16 v[104:107], a[104:107], v[172:175], v[104:107]
ds_read_b64_tr_b16 a[96:97], v220 offset:40960
ds_read_b64_tr_b16 a[98:99], v220 offset:45056
v_mfma_f32_16x16x32_bf16 v[72:75], a[104:107], v[168:171], v[72:75]
ds_read_b64_tr_b16 a[100:101], v221 offset:40960
v_cvt_pk_bf16_f32 v164, v136, v137
v_mfma_f32_16x16x32_bf16 v[76:79], a[108:111], v[168:171], v[76:79]
v_exp_f32 v140, v140
v_mfma_f32_16x16x32_bf16 v[108:111], a[108:111], v[172:175], v[108:111]
ds_read_b64_tr_b16 a[102:103], v221 offset:45056
v_cvt_pk_bf16_f32 v165, v138, v139
v_mfma_f32_16x16x32_bf16 v[112:115], a[112:115], v[172:175], v[112:115]
v_exp_f32 v141, v141
v_mfma_f32_16x16x32_bf16 v[80:83], a[112:115], v[168:171], v[80:83]
buffer_load_dwordx4 a[140:143], v208, s[36:39], s15 offen offset:3072 sc1
v_mfma_f32_16x16x32_bf16 v[84:87], a[116:119], v[168:171], v[84:87]
v_exp_f32 v142, v142
v_mfma_f32_16x16x32_bf16 v[116:119], a[116:119], v[172:175], v[116:119]
ds_read_b64_tr_b16 a[104:105], v220 offset:41472
ds_read_b64_tr_b16 a[106:107], v220 offset:45568
v_mfma_f32_16x16x32_bf16 v[120:123], a[120:123], v[172:175], v[120:123]
v_exp_f32 v143, v143
v_mfma_f32_16x16x32_bf16 v[88:91], a[120:123], v[168:171], v[88:91]
v_exp_f32 v144, v144
v_mfma_f32_16x16x32_bf16 v[92:95], a[124:127], v[168:171], v[92:95]
v_cvt_pk_bf16_f32 v166, v140, v141
s_add_u32 s15, s85, 4096
v_mfma_f32_16x16x32_bf16 v[124:127], a[124:127], v[172:175], v[124:127]
v_cvt_pk_bf16_f32 v167, v142, v143
s_waitcnt lgkmcnt(10)
v_mfma_f32_16x16x32_bf16 v[128:131], a[64:67], a[0:3], v[176:179]
v_exp_f32 v145, v145
v_mfma_f32_16x16x32_bf16 v[136:139], a[64:67], a[16:19], v[180:183]
ds_read_b64_tr_b16 a[108:109], v221 offset:41472
ds_read_b64_tr_b16 a[110:111], v221 offset:45568
v_mfma_f32_16x16x32_bf16 v[140:143], a[68:71], a[16:19], v[180:183]
v_exp_f32 v146, v146
v_mfma_f32_16x16x32_bf16 v[132:135], a[68:71], a[0:3], v[176:179]
buffer_load_dwordx4 a[144:147], v208, s[36:39], s15 offen offset:0 sc1
v_mfma_f32_16x16x32_bf16 v[128:131], a[72:75], a[4:7], v[128:131]
v_exp_f32 v147, v147
v_mfma_f32_16x16x32_bf16 v[136:139], a[72:75], a[20:23], v[136:139]
ds_read_b64_tr_b16 a[112:113], v220 offset:41984
v_cvt_pk_bf16_f32 v168, v144, v145
v_mfma_f32_16x16x32_bf16 v[140:143], a[76:79], a[20:23], v[140:143]
ds_read_b64_tr_b16 a[114:115], v220 offset:46080
ds_read_b64_tr_b16 a[116:117], v221 offset:41984
v_mfma_f32_16x16x32_bf16 v[132:135], a[76:79], a[4:7], v[132:135]
s_waitcnt lgkmcnt(11)
ds_read_b64_tr_b16 a[118:119], v221 offset:46080
v_mfma_f32_16x16x32_bf16 v[128:131], a[80:83], a[8:11], v[128:131]
v_exp_f32 v148, v148
v_mfma_f32_16x16x32_bf16 v[136:139], a[80:83], a[24:27], v[136:139]
v_exp_f32 v149, v149
v_mfma_f32_16x16x32_bf16 v[140:143], a[84:87], a[24:27], v[140:143]
ds_read_b64_tr_b16 a[120:121], v220 offset:42496
v_cvt_pk_bf16_f32 v169, v146, v147
v_mfma_f32_16x16x32_bf16 v[132:135], a[84:87], a[8:11], v[132:135]
v_exp_f32 v150, v150
v_mfma_f32_16x16x32_bf16 v[128:131], a[88:91], a[12:15], v[128:131]
buffer_load_dwordx4 a[148:151], v208, s[36:39], s15 offen offset:1024 sc1
v_mfma_f32_16x16x32_bf16 v[136:139], a[88:91], a[28:31], v[136:139]
ds_read_b64_tr_b16 a[122:123], v220 offset:46592
v_cvt_pk_bf16_f32 v170, v148, v149
v_mfma_f32_16x16x32_bf16 v[140:143], a[92:95], a[28:31], v[140:143]
v_exp_f32 v151, v151
v_mfma_f32_16x16x32_bf16 v[132:135], a[92:95], a[12:15], v[132:135]
v_exp_f32 v152, v152
v_mfma_f32_16x16x32_bf16 v[192:195], v[232:235], v[160:163], v[192:195]
ds_read_b64_tr_b16 a[124:125], v221 offset:42496
ds_read_b64_tr_b16 a[126:127], v221 offset:46592
v_mfma_f32_16x16x32_bf16 v[196:199], v[232:235], v[164:167], v[196:199]
s_waitcnt lgkmcnt(8)
v_cvt_pk_bf16_f32 v171, v150, v151
v_mfma_f32_16x16x32_bf16 v[32:35], a[96:99], v[164:167], v[32:35]
v_exp_f32 v153, v153
v_mfma_f32_16x16x32_bf16 v[0:3], a[96:99], v[160:163], v[0:3]
buffer_load_dwordx4 a[152:155], v208, s[36:39], s15 offen offset:2048 sc1
v_mfma_f32_16x16x32_bf16 v[4:7], a[100:103], v[160:163], v[4:7]
v_exp_f32 v154, v154
v_mfma_f32_16x16x32_bf16 v[36:39], a[100:103], v[164:167], v[36:39]
v_exp_f32 v155, v155
v_mfma_f32_16x16x32_bf16 v[40:43], a[104:107], v[164:167], v[40:43]
v_cvt_pk_bf16_f32 v172, v152, v153
v_mfma_f32_16x16x32_bf16 v[8:11], a[104:107], v[160:163], v[8:11]
v_exp_f32 v156, v156
v_mfma_f32_16x16x32_bf16 v[12:15], a[108:111], v[160:163], v[12:15]
buffer_load_dwordx4 a[156:159], v208, s[36:39], s15 offen offset:3072 sc1
v_mfma_f32_16x16x32_bf16 v[44:47], a[108:111], v[164:167], v[44:47]
s_waitcnt lgkmcnt(0)
v_cvt_pk_bf16_f32 v173, v154, v155
v_mfma_f32_16x16x32_bf16 v[48:51], a[112:115], v[164:167], v[48:51]
v_exp_f32 v157, v157
v_mfma_f32_16x16x32_bf16 v[16:19], a[112:115], v[160:163], v[16:19]
v_exp_f32 v158, v158
v_mfma_f32_16x16x32_bf16 v[20:23], a[116:119], v[160:163], v[20:23]
v_exp_f32 v159, v159
v_mfma_f32_16x16x32_bf16 v[52:55], a[116:119], v[164:167], v[52:55]
v_exp_f32 v128, v128
v_mfma_f32_16x16x32_bf16 v[56:59], a[120:123], v[164:167], v[56:59]
v_cvt_pk_bf16_f32 v174, v156, v157
v_cvt_pk_bf16_f32 v175, v158, v159
v_mfma_f32_16x16x32_bf16 v[24:27], a[120:123], v[160:163], v[24:27]
v_exp_f32 v129, v129
v_mfma_f32_16x16x32_bf16 v[28:31], a[124:127], v[160:163], v[28:31]
v_exp_f32 v130, v130
v_mfma_f32_16x16x32_bf16 v[60:63], a[124:127], v[164:167], v[60:63]
s_waitcnt vmcnt(8)
s_barrier
v_mfma_f32_16x16x32_bf16 v[144:147], a[64:67], a[32:35], v[184:187]
s_add_u32 m0, s71, 16384
ds_read_b64_tr_b16 a[212:213], v245 offset:0
v_mfma_f32_16x16x32_bf16 v[152:155], a[64:67], a[48:51], v[188:191]
v_exp_f32 v131, v131
v_mfma_f32_16x16x32_bf16 v[156:159], a[68:71], a[48:51], v[188:191]
ds_read_b128 a[64:67], v219 offset:49152
v_cvt_pk_bf16_f32 v160, v128, v129
v_mfma_f32_16x16x32_bf16 v[148:151], a[68:71], a[32:35], v[184:187]
buffer_load_dwordx4 v222, s[44:47], s72 offen offset:0 lds
v_mfma_f32_16x16x32_bf16 v[144:147], a[72:75], a[36:39], v[144:147]
s_add_u32 s15, s85, 8192
ds_read_b128 a[68:71], v219 offset:53248
v_mfma_f32_16x16x32_bf16 v[152:155], a[72:75], a[52:55], v[152:155]
v_exp_f32 v132, v132
v_mfma_f32_16x16x32_bf16 v[156:159], a[76:79], a[52:55], v[156:159]
ds_read_b128 a[72:75], v219 offset:49664
ds_read_b64_tr_b16 a[214:215], v245 offset:512
v_mfma_f32_16x16x32_bf16 v[148:151], a[76:79], a[36:39], v[148:151]
v_exp_f32 v133, v133
v_mfma_f32_16x16x32_bf16 v[144:147], a[80:83], a[40:43], v[144:147]
ds_read_b128 a[76:79], v219 offset:53760
v_cvt_pk_bf16_f32 v161, v130, v131
v_mfma_f32_16x16x32_bf16 v[152:155], a[80:83], a[56:59], v[152:155]
buffer_load_dwordx4 a[160:163], v208, s[36:39], s15 offen offset:0 sc1
v_mfma_f32_16x16x32_bf16 v[156:159], a[84:87], a[56:59], v[156:159]
v_exp_f32 v134, v134
v_mfma_f32_16x16x32_bf16 v[148:151], a[84:87], a[40:43], v[148:151]
ds_read_b128 a[80:83], v219 offset:50176
v_add_u32 v245, s34, v245
v_mfma_f32_16x16x32_bf16 v[144:147], a[88:91], a[44:47], v[144:147]
v_exp_f32 v135, v135
v_mfma_f32_16x16x32_bf16 v[152:155], a[88:91], a[60:63], v[152:155]
ds_read_b128 a[84:87], v219 offset:54272
v_cvt_pk_bf16_f32 v162, v132, v133
v_mfma_f32_16x16x32_bf16 v[156:159], a[92:95], a[60:63], v[156:159]
v_exp_f32 v136, v136
v_mfma_f32_16x16x32_bf16 v[148:151], a[92:95], a[44:47], v[148:151]
buffer_load_dwordx4 v223, s[44:47], s72 offen offset:1024 lds
v_mfma_f32_16x16x32_bf16 v[200:203], v[232:235], v[168:171], v[200:203]
ds_read_b128 a[88:91], v219 offset:50688
ds_read_b128 a[92:95], v219 offset:54784
v_mfma_f32_16x16x32_bf16 v[204:207], v[232:235], v[172:175], v[204:207]
v_exp_f32 v137, v137
v_mfma_f32_16x16x32_bf16 v[96:99], a[96:99], v[172:175], v[96:99]
ds_read_b64_tr_b16 a[216:217], v246 offset:0
v_cvt_pk_bf16_f32 v163, v134, v135
v_mfma_f32_16x16x32_bf16 v[64:67], a[96:99], v[168:171], v[64:67]
v_exp_f32 v138, v138
v_mfma_f32_16x16x32_bf16 v[68:71], a[100:103], v[168:171], v[68:71]
buffer_load_dwordx4 a[164:167], v208, s[36:39], s15 offen offset:1024 sc1
v_mfma_f32_16x16x32_bf16 v[100:103], a[100:103], v[172:175], v[100:103]
v_exp_f32 v139, v139
v_mfma_f32_16x16x32_bf16 v[104:107], a[104:107], v[172:175], v[104:107]
ds_read_b64_tr_b16 a[96:97], v220 offset:57344
ds_read_b64_tr_b16 a[98:99], v220 offset:61440
v_mfma_f32_16x16x32_bf16 v[72:75], a[104:107], v[168:171], v[72:75]
ds_read_b64_tr_b16 a[218:219], v246 offset:512
v_cvt_pk_bf16_f32 v164, v136, v137
v_mfma_f32_16x16x32_bf16 v[76:79], a[108:111], v[168:171], v[76:79]
v_exp_f32 v140, v140
v_mfma_f32_16x16x32_bf16 v[108:111], a[108:111], v[172:175], v[108:111]
ds_read_b64_tr_b16 a[100:101], v221 offset:57344
ds_read_b64_tr_b16 a[102:103], v221 offset:61440
v_mfma_f32_16x16x32_bf16 v[112:115], a[112:115], v[172:175], v[112:115]
v_exp_f32 v141, v141
v_mfma_f32_16x16x32_bf16 v[80:83], a[112:115], v[168:171], v[80:83]
v_cvt_pk_bf16_f32 v165, v138, v139
v_add_u32 v246, s34, v246
v_mfma_f32_16x16x32_bf16 v[84:87], a[116:119], v[168:171], v[84:87]
buffer_load_dwordx4 v224, s[44:47], s72 offen offset:2048 lds
v_mfma_f32_16x16x32_bf16 v[116:119], a[116:119], v[172:175], v[116:119]
v_exp_f32 v142, v142
v_mfma_f32_16x16x32_bf16 v[120:123], a[120:123], v[172:175], v[120:123]
ds_read_b64_tr_b16 a[104:105], v220 offset:57856
ds_read_b64_tr_b16 a[106:107], v220 offset:61952
v_mfma_f32_16x16x32_bf16 v[88:91], a[120:123], v[168:171], v[88:91]
v_exp_f32 v143, v143
v_mfma_f32_16x16x32_bf16 v[92:95], a[124:127], v[168:171], v[92:95]
v_exp_f32 v144, v144
v_mfma_f32_16x16x32_bf16 v[124:127], a[124:127], v[172:175], v[124:127]
v_cvt_pk_bf16_f32 v166, v140, v141
v_cvt_pk_bf16_f32 v167, v142, v143
s_waitcnt lgkmcnt(12)
v_mfma_f32_16x16x32_bf16 v[128:131], a[64:67], a[0:3], v[176:179]
ds_read_b64_tr_b16 a[220:221], v247 offset:0
ds_read_b64_tr_b16 a[108:109], v221 offset:57856
v_mfma_f32_16x16x32_bf16 v[136:139], a[64:67], a[16:19], v[180:183]
v_exp_f32 v145, v145
v_mfma_f32_16x16x32_bf16 v[140:143], a[68:71], a[16:19], v[180:183]
buffer_load_dwordx4 v225, s[44:47], s72 offen offset:3072 lds
s_add_u32 s72, s72, 8192
v_mfma_f32_16x16x32_bf16 v[132:135], a[68:71], a[0:3], v[176:179]
s_add_u32 m0, s71, 32768
ds_read_b64_tr_b16 a[110:111], v221 offset:61952
v_mfma_f32_16x16x32_bf16 v[128:131], a[72:75], a[4:7], v[128:131]
v_exp_f32 v146, v146
v_mfma_f32_16x16x32_bf16 v[136:139], a[72:75], a[20:23], v[136:139]
v_exp_f32 v147, v147
v_mfma_f32_16x16x32_bf16 v[140:143], a[76:79], a[20:23], v[140:143]
ds_read_b64_tr_b16 a[222:223], v247 offset:512
v_cvt_pk_bf16_f32 v168, v144, v145
v_mfma_f32_16x16x32_bf16 v[132:135], a[76:79], a[4:7], v[132:135]
buffer_load_dwordx4 v222, s[44:47], s72 offen offset:0 lds
s_waitcnt lgkmcnt(12)
v_mfma_f32_16x16x32_bf16 v[128:131], a[80:83], a[8:11], v[128:131]
ds_read_b64_tr_b16 a[112:113], v220 offset:58368
ds_read_b64_tr_b16 a[114:115], v220 offset:62464
v_mfma_f32_16x16x32_bf16 v[136:139], a[80:83], a[24:27], v[136:139]
v_exp_f32 v148, v148
v_mfma_f32_16x16x32_bf16 v[140:143], a[84:87], a[24:27], v[140:143]
v_exp_f32 v149, v149
v_mfma_f32_16x16x32_bf16 v[132:135], a[84:87], a[8:11], v[132:135]
ds_read_b64_tr_b16 a[116:117], v221 offset:58368
ds_read_b64_tr_b16 a[118:119], v221 offset:62464
v_mfma_f32_16x16x32_bf16 v[128:131], a[88:91], a[12:15], v[128:131]
v_add_u32 v247, s34, v247
v_cvt_pk_bf16_f32 v169, v146, v147
v_mfma_f32_16x16x32_bf16 v[136:139], a[88:91], a[28:31], v[136:139]
buffer_load_dwordx4 v223, s[44:47], s72 offen offset:1024 lds
v_mfma_f32_16x16x32_bf16 v[140:143], a[92:95], a[28:31], v[140:143]
v_exp_f32 v150, v150
v_mfma_f32_16x16x32_bf16 v[132:135], a[92:95], a[12:15], v[132:135]
ds_read_b64_tr_b16 a[120:121], v220 offset:58880
ds_read_b64_tr_b16 a[122:123], v220 offset:62976
v_mfma_f32_16x16x32_bf16 v[192:195], v[232:235], v[160:163], v[192:195]
v_exp_f32 v151, v151
v_mfma_f32_16x16x32_bf16 v[196:199], v[232:235], v[164:167], v[196:199]
s_waitcnt lgkmcnt(7)
v_cvt_pk_bf16_f32 v170, v148, v149
v_mfma_f32_16x16x32_bf16 v[32:35], a[96:99], v[164:167], v[32:35]
ds_read_b64_tr_b16 a[224:225], v248 offset:0
ds_read_b64_tr_b16 a[124:125], v221 offset:58880
v_mfma_f32_16x16x32_bf16 v[0:3], a[96:99], v[160:163], v[0:3]
buffer_load_dwordx4 v224, s[44:47], s72 offen offset:2048 lds
v_mfma_f32_16x16x32_bf16 v[4:7], a[100:103], v[160:163], v[4:7]
v_exp_f32 v152, v152
v_mfma_f32_16x16x32_bf16 v[36:39], a[100:103], v[164:167], v[36:39]
ds_read_b64_tr_b16 a[126:127], v221 offset:62976
v_cvt_pk_bf16_f32 v171, v150, v151
v_mfma_f32_16x16x32_bf16 v[40:43], a[104:107], v[164:167], v[40:43]
v_exp_f32 v153, v153
v_mfma_f32_16x16x32_bf16 v[8:11], a[104:107], v[160:163], v[8:11]
ds_read_b64_tr_b16 a[226:227], v248 offset:512
v_add_u32 v248, s34, v248
v_mfma_f32_16x16x32_bf16 v[12:15], a[108:111], v[160:163], v[12:15]
v_exp_f32 v154, v154
v_mfma_f32_16x16x32_bf16 v[44:47], a[108:111], v[164:167], v[44:47]
buffer_load_dwordx4 v225, s[44:47], s72 offen offset:3072 lds
s_add_u32 s72, s72, 8192
s_waitcnt lgkmcnt(1)
v_mfma_f32_16x16x32_bf16 v[48:51], a[112:115], v[164:167], v[48:51]
v_exp_f32 v155, v155
v_mfma_f32_16x16x32_bf16 v[16:19], a[112:115], v[160:163], v[16:19]
v_cvt_pk_bf16_f32 v172, v152, v153
s_lshr_b32 s77, s65, 3
v_mfma_f32_16x16x32_bf16 v[20:23], a[116:119], v[160:163], v[20:23]
v_exp_f32 v156, v156
v_mfma_f32_16x16x32_bf16 v[52:55], a[116:119], v[164:167], v[52:55]
s_mul_i32 s77, s77, 256
s_add_u32 s77, s77, .Lk_fbret-.Lk_start-17408
v_mfma_f32_16x16x32_bf16 v[56:59], a[120:123], v[164:167], v[56:59]
v_and_b32 v227, 1, v209
v_lshlrev_b32 v227, 7, v227
v_mfma_f32_16x16x32_bf16 v[24:27], a[120:123], v[160:163], v[24:27]
buffer_load_dword v227, v227, s[60:63], s77 offen
v_mfma_f32_16x16x32_bf16 v[28:31], a[124:127], v[160:163], v[28:31]
v_exp_f32 v157, v157
v_mfma_f32_16x16x32_bf16 v[60:63], a[124:127], v[164:167], v[60:63]
v_cvt_pk_bf16_f32 v173, v154, v155
v_exp_f32 v158, v158
v_exp_f32 v159, v159
v_cvt_pk_bf16_f32 v174, v156, v157
v_cvt_pk_bf16_f32 v175, v158, v159
v_mfma_f32_16x16x32_bf16 v[144:147], a[64:67], a[32:35], v[184:187]
ds_read_b64_tr_b16 a[228:229], v249 offset:0
ds_read_b64_tr_b16 a[230:231], v249 offset:512
v_mfma_f32_16x16x32_bf16 v[152:155], a[64:67], a[48:51], v[188:191]
v_exp_f32 v128, v128
v_mfma_f32_16x16x32_bf16 v[156:159], a[68:71], a[48:51], v[188:191]
v_exp_f32 v129, v129
v_mfma_f32_16x16x32_bf16 v[148:151], a[68:71], a[32:35], v[184:187]
v_exp_f32 v130, v130
v_mfma_f32_16x16x32_bf16 v[144:147], a[72:75], a[36:39], v[144:147]
v_exp_f32 v131, v131
v_mfma_f32_16x16x32_bf16 v[152:155], a[72:75], a[52:55], v[152:155]
ds_read_b128 a[64:67], v219 offset:0
v_cvt_pk_bf16_f32 v160, v128, v129
v_mfma_f32_16x16x32_bf16 v[156:159], a[76:79], a[52:55], v[156:159]
buffer_load_dwordx4 a[168:171], v208, s[36:39], s15 offen offset:2048 sc1
v_mfma_f32_16x16x32_bf16 v[148:151], a[76:79], a[36:39], v[148:151]
ds_read_b128 a[68:71], v219 offset:4096
v_add_u32 v249, s34, v249
v_mfma_f32_16x16x32_bf16 v[144:147], a[80:83], a[40:43], v[144:147]
v_exp_f32 v132, v132
v_mfma_f32_16x16x32_bf16 v[152:155], a[80:83], a[56:59], v[152:155]
ds_read_b128 a[72:75], v219 offset:512
v_cvt_pk_bf16_f32 v161, v130, v131
v_mfma_f32_16x16x32_bf16 v[156:159], a[84:87], a[56:59], v[156:159]
v_exp_f32 v133, v133
v_mfma_f32_16x16x32_bf16 v[148:151], a[84:87], a[40:43], v[148:151]
ds_read_b128 a[76:79], v219 offset:4608
ds_read_b64_tr_b16 a[232:233], v245 offset:0
v_mfma_f32_16x16x32_bf16 v[144:147], a[88:91], a[44:47], v[144:147]
v_exp_f32 v134, v134
v_mfma_f32_16x16x32_bf16 v[152:155], a[88:91], a[60:63], v[152:155]
ds_read_b128 a[80:83], v219 offset:1024
v_cvt_pk_bf16_f32 v162, v132, v133
v_mfma_f32_16x16x32_bf16 v[156:159], a[92:95], a[60:63], v[156:159]
v_exp_f32 v135, v135
v_mfma_f32_16x16x32_bf16 v[148:151], a[92:95], a[44:47], v[148:151]
ds_read_b128 a[84:87], v219 offset:5120
ds_read_b128 a[88:91], v219 offset:1536
v_mfma_f32_16x16x32_bf16 v[200:203], v[232:235], v[168:171], v[200:203]
buffer_load_dwordx4 a[172:175], v208, s[36:39], s15 offen offset:3072 sc1
v_mfma_f32_16x16x32_bf16 v[204:207], v[232:235], v[172:175], v[204:207]
v_exp_f32 v136, v136
v_mfma_f32_16x16x32_bf16 v[96:99], a[96:99], v[172:175], v[96:99]
ds_read_b64_tr_b16 a[234:235], v245 offset:512
ds_read_b128 a[92:95], v219 offset:5632
v_mfma_f32_16x16x32_bf16 v[64:67], a[96:99], v[168:171], v[64:67]
v_exp_f32 v137, v137
v_mfma_f32_16x16x32_bf16 v[68:71], a[100:103], v[168:171], v[68:71]
v_cvt_pk_bf16_f32 v163, v134, v135
s_add_u32 s15, s85, 12288
v_mfma_f32_16x16x32_bf16 v[100:103], a[100:103], v[172:175], v[100:103]
v_exp_f32 v138, v138
v_mfma_f32_16x16x32_bf16 v[104:107], a[104:107], v[172:175], v[104:107]
ds_read_b64_tr_b16 a[236:237], v246 offset:0
ds_read_b64_tr_b16 a[96:97], v220 offset:8192
v_mfma_f32_16x16x32_bf16 v[72:75], a[104:107], v[168:171], v[72:75]
v_exp_f32 v139, v139
v_mfma_f32_16x16x32_bf16 v[76:79], a[108:111], v[168:171], v[76:79]
ds_read_b64_tr_b16 a[98:99], v220 offset:12288
v_cvt_pk_bf16_f32 v164, v136, v137
v_mfma_f32_16x16x32_bf16 v[108:111], a[108:111], v[172:175], v[108:111]
v_exp_f32 v140, v140
v_mfma_f32_16x16x32_bf16 v[112:115], a[112:115], v[172:175], v[112:115]
buffer_load_dwordx4 a[176:179], v208, s[36:39], s15 offen offset:0 sc1
v_mfma_f32_16x16x32_bf16 v[80:83], a[112:115], v[168:171], v[80:83]
ds_read_b64_tr_b16 a[100:101], v221 offset:8192
ds_read_b64_tr_b16 a[102:103], v221 offset:12288
v_mfma_f32_16x16x32_bf16 v[84:87], a[116:119], v[168:171], v[84:87]
ds_read_b64_tr_b16 a[238:239], v246 offset:512
v_cvt_pk_bf16_f32 v165, v138, v139
v_mfma_f32_16x16x32_bf16 v[116:119], a[116:119], v[172:175], v[116:119]
v_exp_f32 v141, v141
v_mfma_f32_16x16x32_bf16 v[120:123], a[120:123], v[172:175], v[120:123]
v_exp_f32 v142, v142
v_mfma_f32_16x16x32_bf16 v[88:91], a[120:123], v[168:171], v[88:91]
ds_read_b64_tr_b16 a[104:105], v220 offset:8704
ds_read_b64_tr_b16 a[106:107], v220 offset:12800
v_mfma_f32_16x16x32_bf16 v[92:95], a[124:127], v[168:171], v[92:95]
v_exp_f32 v143, v143
v_mfma_f32_16x16x32_bf16 v[124:127], a[124:127], v[172:175], v[124:127]
v_cvt_pk_bf16_f32 v166, v140, v141
v_cvt_pk_bf16_f32 v167, v142, v143
s_waitcnt lgkmcnt(14)
v_mfma_f32_16x16x32_bf16 v[128:131], a[64:67], a[0:3], v[176:179]
v_exp_f32 v144, v144
v_mfma_f32_16x16x32_bf16 v[136:139], a[64:67], a[16:19], v[180:183]
ds_read_b64_tr_b16 a[240:241], v247 offset:0
ds_read_b64_tr_b16 a[108:109], v221 offset:8704
v_mfma_f32_16x16x32_bf16 v[140:143], a[68:71], a[16:19], v[180:183]
v_exp_f32 v145, v145
v_mfma_f32_16x16x32_bf16 v[132:135], a[68:71], a[0:3], v[176:179]
ds_read_b64_tr_b16 a[110:111], v221 offset:12800
ds_read_b64_tr_b16 a[242:243], v247 offset:512
v_mfma_f32_16x16x32_bf16 v[128:131], a[72:75], a[4:7], v[128:131]
v_exp_f32 v146, v146
v_mfma_f32_16x16x32_bf16 v[136:139], a[72:75], a[20:23], v[136:139]
buffer_load_dwordx4 a[180:183], v208, s[36:39], s15 offen offset:1024 sc1
v_mfma_f32_16x16x32_bf16 v[140:143], a[76:79], a[20:23], v[140:143]
v_exp_f32 v147, v147
v_mfma_f32_16x16x32_bf16 v[132:135], a[76:79], a[4:7], v[132:135]
s_waitcnt lgkmcnt(12)
v_cvt_pk_bf16_f32 v168, v144, v145
v_mfma_f32_16x16x32_bf16 v[128:131], a[80:83], a[8:11], v[128:131]
ds_read_b64_tr_b16 a[112:113], v220 offset:9216
ds_read_b64_tr_b16 a[114:115], v220 offset:13312
v_mfma_f32_16x16x32_bf16 v[136:139], a[80:83], a[24:27], v[136:139]
v_exp_f32 v148, v148
v_mfma_f32_16x16x32_bf16 v[140:143], a[84:87], a[24:27], v[140:143]
ds_read_b64_tr_b16 a[244:245], v248 offset:0
ds_read_b64_tr_b16 a[116:117], v221 offset:9216
v_mfma_f32_16x16x32_bf16 v[132:135], a[84:87], a[8:11], v[132:135]
v_exp_f32 v149, v149
v_mfma_f32_16x16x32_bf16 v[128:131], a[88:91], a[12:15], v[128:131]
ds_read_b64_tr_b16 a[118:119], v221 offset:13312
v_cvt_pk_bf16_f32 v169, v146, v147
v_mfma_f32_16x16x32_bf16 v[136:139], a[88:91], a[28:31], v[136:139]
v_exp_f32 v150, v150
v_mfma_f32_16x16x32_bf16 v[140:143], a[92:95], a[28:31], v[140:143]
buffer_load_dwordx4 a[184:187], v208, s[36:39], s15 offen offset:2048 sc1
v_mfma_f32_16x16x32_bf16 v[132:135], a[92:95], a[12:15], v[132:135]
ds_read_b64_tr_b16 a[120:121], v220 offset:9728
ds_read_b64_tr_b16 a[122:123], v220 offset:13824
v_mfma_f32_16x16x32_bf16 v[192:195], v[232:235], v[160:163], v[192:195]
ds_read_b64_tr_b16 a[246:247], v248 offset:512
v_cvt_pk_bf16_f32 v170, v148, v149
v_mfma_f32_16x16x32_bf16 v[196:199], v[232:235], v[164:167], v[196:199]
s_waitcnt lgkmcnt(9)
ds_read_b64_tr_b16 a[124:125], v221 offset:9728
v_mfma_f32_16x16x32_bf16 v[32:35], a[96:99], v[164:167], v[32:35]
v_exp_f32 v151, v151
v_mfma_f32_16x16x32_bf16 v[0:3], a[96:99], v[160:163], v[0:3]
v_exp_f32 v152, v152
v_mfma_f32_16x16x32_bf16 v[4:7], a[100:103], v[160:163], v[4:7]
ds_read_b64_tr_b16 a[126:127], v221 offset:13824
ds_read_b64_tr_b16 a[248:249], v249 offset:0
v_mfma_f32_16x16x32_bf16 v[36:39], a[100:103], v[164:167], v[36:39]
v_exp_f32 v153, v153
v_mfma_f32_16x16x32_bf16 v[40:43], a[104:107], v[164:167], v[40:43]
ds_read_b64_tr_b16 a[250:251], v249 offset:512
v_cvt_pk_bf16_f32 v171, v150, v151
v_mfma_f32_16x16x32_bf16 v[8:11], a[104:107], v[160:163], v[8:11]
v_exp_f32 v154, v154
v_mfma_f32_16x16x32_bf16 v[12:15], a[108:111], v[160:163], v[12:15]
v_exp_f32 v155, v155
v_mfma_f32_16x16x32_bf16 v[44:47], a[108:111], v[164:167], v[44:47]
s_waitcnt lgkmcnt(2)
v_cvt_pk_bf16_f32 v172, v152, v153
v_mfma_f32_16x16x32_bf16 v[48:51], a[112:115], v[164:167], v[48:51]
buffer_load_dwordx4 a[188:191], v208, s[36:39], s15 offen offset:3072 sc1
v_mfma_f32_16x16x32_bf16 v[16:19], a[112:115], v[160:163], v[16:19]
v_exp_f32 v156, v156
v_mfma_f32_16x16x32_bf16 v[20:23], a[116:119], v[160:163], v[20:23]
v_exp_f32 v157, v157
v_mfma_f32_16x16x32_bf16 v[52:55], a[116:119], v[164:167], v[52:55]
v_cvt_pk_bf16_f32 v173, v154, v155
v_mfma_f32_16x16x32_bf16 v[56:59], a[120:123], v[164:167], v[56:59]
v_exp_f32 v158, v158
v_mfma_f32_16x16x32_bf16 v[24:27], a[120:123], v[160:163], v[24:27]
v_exp_f32 v159, v159
v_mfma_f32_16x16x32_bf16 v[28:31], a[124:127], v[160:163], v[28:31]
v_exp_f32 v128, v128
v_mfma_f32_16x16x32_bf16 v[60:63], a[124:127], v[164:167], v[60:63]
v_cvt_pk_bf16_f32 v174, v156, v157
v_cvt_pk_bf16_f32 v175, v158, v159
s_waitcnt lgkmcnt(0)
s_waitcnt vmcnt(7)
s_barrier
v_mfma_f32_16x16x32_bf16 v[144:147], a[64:67], a[32:35], v[184:187]
v_exp_f32 v129, v129
v_mfma_f32_16x16x32_bf16 v[152:155], a[64:67], a[48:51], v[188:191]
s_add_u32 m0, s71, 49152
ds_read_b128 a[64:67], v219 offset:16384
v_mfma_f32_16x16x32_bf16 v[156:159], a[68:71], a[48:51], v[188:191]
v_mfma_f32_16x16x32_bf16 v[236:239], a[192:195], a[212:215], 0
v_mfma_f32_16x16x32_bf16 v[236:239], a[196:199], a[216:219], v[236:239]
v_mfma_f32_16x16x32_bf16 v[236:239], a[200:203], a[220:223], v[236:239]
v_mfma_f32_16x16x32_bf16 v[236:239], a[204:207], a[224:227], v[236:239]
v_mfma_f32_16x16x32_bf16 v[236:239], a[208:211], a[228:231], v[236:239]
v_mfma_f32_16x16x32_bf16 v[240:243], a[192:195], a[232:235], 0
v_mfma_f32_16x16x32_bf16 v[240:243], a[196:199], a[236:239], v[240:243]
v_mfma_f32_16x16x32_bf16 v[240:243], a[200:203], a[240:243], v[240:243]
v_mfma_f32_16x16x32_bf16 v[240:243], a[204:207], a[244:247], v[240:243]
v_mfma_f32_16x16x32_bf16 v[240:243], a[208:211], a[248:251], v[240:243]
v_mfma_f32_16x16x32_bf16 v[148:151], a[68:71], a[32:35], v[184:187]
v_exp_f32 v130, v130
v_mfma_f32_16x16x32_bf16 v[144:147], a[72:75], a[36:39], v[144:147]
v_exp_f32 v131, v131
v_mfma_f32_16x16x32_bf16 v[152:155], a[72:75], a[52:55], v[152:155]
ds_read_b128 a[68:71], v219 offset:20480
v_cvt_pk_bf16_f32 v160, v128, v129
v_mfma_f32_16x16x32_bf16 v[156:159], a[76:79], a[52:55], v[156:159]
buffer_load_dwordx4 v222, s[44:47], s72 offen offset:0 lds
v_mfma_f32_16x16x32_bf16 v[148:151], a[76:79], a[36:39], v[148:151]
v_exp_f32 v132, v132
v_mfma_f32_16x16x32_bf16 v[144:147], a[80:83], a[40:43], v[144:147]
ds_read_b128 a[72:75], v219 offset:16896
v_cvt_pk_bf16_f32 v161, v130, v131
v_mfma_f32_16x16x32_bf16 v[152:155], a[80:83], a[56:59], v[152:155]
v_exp_f32 v133, v133
v_mfma_f32_16x16x32_bf16 v[156:159], a[84:87], a[56:59], v[156:159]
ds_read_b128 a[76:79], v219 offset:20992
ds_read_b128 a[80:83], v219 offset:17408
v_mfma_f32_16x16x32_bf16 v[148:151], a[84:87], a[40:43], v[148:151]
v_exp_f32 v134, v134
v_mfma_f32_16x16x32_bf16 v[144:147], a[88:91], a[44:47], v[144:147]
buffer_store_dwordx4 a[212:215], v208, s[36:39], s86 offen offset:0 sc1
v_mfma_f32_16x16x32_bf16 v[152:155], a[88:91], a[60:63], v[152:155]
v_cvt_pk_bf16_f32 v239, v236, v237
v_cvt_pk_bf16_f32 v162, v132, v133
v_mfma_f32_16x16x32_bf16 v[156:159], a[92:95], a[60:63], v[156:159]
v_exp_f32 v135, v135
v_mfma_f32_16x16x32_bf16 v[148:151], a[92:95], a[44:47], v[148:151]
ds_read_b128 a[84:87], v219 offset:21504
ds_read_b128 a[88:91], v219 offset:17920
v_mfma_f32_16x16x32_bf16 v[200:203], v[232:235], v[168:171], v[200:203]
v_exp_f32 v136, v136
v_mfma_f32_16x16x32_bf16 v[204:207], v[232:235], v[172:175], v[204:207]
buffer_load_dwordx4 v223, s[44:47], s72 offen offset:1024 lds
v_mfma_f32_16x16x32_bf16 v[96:99], a[96:99], v[172:175], v[96:99]
v_exp_f32 v137, v137
v_mfma_f32_16x16x32_bf16 v[64:67], a[96:99], v[168:171], v[64:67]
ds_read_b128 a[92:95], v219 offset:22016
v_cvt_pk_bf16_f32 v163, v134, v135
v_mfma_f32_16x16x32_bf16 v[68:71], a[100:103], v[168:171], v[68:71]
ds_read_b64_tr_b16 a[96:97], v220 offset:24576
v_lshlrev_b32 v250, 16, v239
v_mfma_f32_16x16x32_bf16 v[100:103], a[100:103], v[172:175], v[100:103]
v_exp_f32 v138, v138
v_mfma_f32_16x16x32_bf16 v[104:107], a[104:107], v[172:175], v[104:107]
v_exp_f32 v139, v139
v_mfma_f32_16x16x32_bf16 v[72:75], a[104:107], v[168:171], v[72:75]
ds_read_b64_tr_b16 a[98:99], v220 offset:28672
v_cvt_pk_bf16_f32 v164, v136, v137
v_mfma_f32_16x16x32_bf16 v[76:79], a[108:111], v[168:171], v[76:79]
buffer_store_dwordx4 a[216:219], v208, s[36:39], s86 offen offset:1024 sc1
v_mfma_f32_16x16x32_bf16 v[108:111], a[108:111], v[172:175], v[108:111]
v_exp_f32 v140, v140
v_mfma_f32_16x16x32_bf16 v[112:115], a[112:115], v[172:175], v[112:115]
ds_read_b64_tr_b16 a[100:101], v221 offset:24576
ds_read_b64_tr_b16 a[102:103], v221 offset:28672
v_mfma_f32_16x16x32_bf16 v[80:83], a[112:115], v[168:171], v[80:83]
v_and_b32 v251, 0xffff0000, v239
v_cvt_pk_bf16_f32 v165, v138, v139
v_mfma_f32_16x16x32_bf16 v[84:87], a[116:119], v[168:171], v[84:87]
v_exp_f32 v141, v141
v_mfma_f32_16x16x32_bf16 v[116:119], a[116:119], v[172:175], v[116:119]
v_exp_f32 v142, v142
v_mfma_f32_16x16x32_bf16 v[120:123], a[120:123], v[172:175], v[120:123]
buffer_load_dwordx4 v224, s[44:47], s72 offen offset:2048 lds
v_mfma_f32_16x16x32_bf16 v[88:91], a[120:123], v[168:171], v[88:91]
ds_read_b64_tr_b16 a[104:105], v220 offset:25088
ds_read_b64_tr_b16 a[106:107], v220 offset:29184
v_mfma_f32_16x16x32_bf16 v[92:95], a[124:127], v[168:171], v[92:95]
v_exp_f32 v143, v143
v_mfma_f32_16x16x32_bf16 v[124:127], a[124:127], v[172:175], v[124:127]
v_cvt_pk_bf16_f32 v166, v140, v141
v_cvt_pk_bf16_f32 v167, v142, v143
s_waitcnt lgkmcnt(10)
v_mfma_f32_16x16x32_bf16 v[128:131], a[64:67], a[0:3], v[176:179]
v_exp_f32 v144, v144
v_mfma_f32_16x16x32_bf16 v[136:139], a[64:67], a[16:19], v[180:183]
v_exp_f32 v145, v145
v_mfma_f32_16x16x32_bf16 v[140:143], a[68:71], a[16:19], v[180:183]
ds_read_b64_tr_b16 a[108:109], v221 offset:25088
v_sub_f32 v250, v236, v250
v_mfma_f32_16x16x32_bf16 v[132:135], a[68:71], a[0:3], v[176:179]
ds_read_b64_tr_b16 a[110:111], v221 offset:29184
ds_read_b64_tr_b16 a[112:113], v220 offset:25600
v_mfma_f32_16x16x32_bf16 v[128:131], a[72:75], a[4:7], v[128:131]
v_exp_f32 v146, v146
v_mfma_f32_16x16x32_bf16 v[136:139], a[72:75], a[20:23], v[136:139]
buffer_load_dwordx4 v225, s[44:47], s72 offen offset:3072 lds
s_add_u32 s72, s72, 8192
v_mfma_f32_16x16x32_bf16 v[140:143], a[76:79], a[20:23], v[140:143]
v_exp_f32 v147, v147
v_mfma_f32_16x16x32_bf16 v[132:135], a[76:79], a[4:7], v[132:135]
s_waitcnt lgkmcnt(9)
v_cvt_pk_bf16_f32 v168, v144, v145
v_mfma_f32_16x16x32_bf16 v[128:131], a[80:83], a[8:11], v[128:131]
s_add_u32 m0, s71, 0
ds_read_b64_tr_b16 a[114:115], v220 offset:29696
v_mfma_f32_16x16x32_bf16 v[136:139], a[80:83], a[24:27], v[136:139]
v_exp_f32 v148, v148
v_mfma_f32_16x16x32_bf16 v[140:143], a[84:87], a[24:27], v[140:143]
ds_read_b64_tr_b16 a[116:117], v221 offset:25600
v_sub_f32 v251, v237, v251
v_mfma_f32_16x16x32_bf16 v[132:135], a[84:87], a[8:11], v[132:135]
buffer_load_dwordx4 v222, s[44:47], s72 offen offset:0 lds
v_mfma_f32_16x16x32_bf16 v[128:131], a[88:91], a[12:15], v[128:131]
v_exp_f32 v149, v149
v_mfma_f32_16x16x32_bf16 v[136:139], a[88:91], a[28:31], v[136:139]
ds_read_b64_tr_b16 a[118:119], v221 offset:29696
v_cvt_pk_bf16_f32 v169, v146, v147
v_mfma_f32_16x16x32_bf16 v[140:143], a[92:95], a[28:31], v[140:143]
v_exp_f32 v150, v150
v_mfma_f32_16x16x32_bf16 v[132:135], a[92:95], a[12:15], v[132:135]
ds_read_b64_tr_b16 a[120:121], v220 offset:26112
ds_read_b64_tr_b16 a[122:123], v220 offset:30208
v_mfma_f32_16x16x32_bf16 v[192:195], v[232:235], v[160:163], v[192:195]
v_exp_f32 v151, v151
v_mfma_f32_16x16x32_bf16 v[196:199], v[232:235], v[164:167], v[196:199]
buffer_load_dwordx4 v223, s[44:47], s72 offen offset:1024 lds
s_waitcnt lgkmcnt(6)
v_mfma_f32_16x16x32_bf16 v[32:35], a[96:99], v[164:167], v[32:35]
v_cvt_pk_bf16_f32 v170, v148, v149
v_cvt_pk_bf16_f32 v236, v236, v250
v_mfma_f32_16x16x32_bf16 v[0:3], a[96:99], v[160:163], v[0:3]
v_exp_f32 v152, v152
v_mfma_f32_16x16x32_bf16 v[4:7], a[100:103], v[160:163], v[4:7]
ds_read_b64_tr_b16 a[124:125], v221 offset:26112
ds_read_b64_tr_b16 a[126:127], v221 offset:30208
v_mfma_f32_16x16x32_bf16 v[36:39], a[100:103], v[164:167], v[36:39]
v_exp_f32 v153, v153
v_mfma_f32_16x16x32_bf16 v[40:43], a[104:107], v[164:167], v[40:43]
v_cvt_pk_bf16_f32 v171, v150, v151
v_cvt_pk_bf16_f32 v238, v251, v237
v_mfma_f32_16x16x32_bf16 v[8:11], a[104:107], v[160:163], v[8:11]
v_exp_f32 v154, v154
v_mfma_f32_16x16x32_bf16 v[12:15], a[108:111], v[160:163], v[12:15]
buffer_load_dwordx4 v224, s[44:47], s72 offen offset:2048 lds
v_mfma_f32_16x16x32_bf16 v[44:47], a[108:111], v[164:167], v[44:47]
s_waitcnt lgkmcnt(0)
v_cvt_pk_bf16_f32 v172, v152, v153
v_mfma_f32_16x16x32_bf16 v[48:51], a[112:115], v[164:167], v[48:51]
v_exp_f32 v155, v155
v_mfma_f32_16x16x32_bf16 v[16:19], a[112:115], v[160:163], v[16:19]
v_exp_f32 v156, v156
v_mfma_f32_16x16x32_bf16 v[20:23], a[116:119], v[160:163], v[20:23]
v_exp_f32 v157, v157
v_mfma_f32_16x16x32_bf16 v[52:55], a[116:119], v[164:167], v[52:55]
v_cvt_pk_bf16_f32 v173, v154, v155
v_mov_b32 v237, v239
v_mfma_f32_16x16x32_bf16 v[56:59], a[120:123], v[164:167], v[56:59]
buffer_load_dwordx4 v225, s[44:47], s72 offen offset:3072 lds
s_add_u32 s72, s72, 8192
v_mfma_f32_16x16x32_bf16 v[24:27], a[120:123], v[160:163], v[24:27]
v_exp_f32 v158, v158
v_mfma_f32_16x16x32_bf16 v[28:31], a[124:127], v[160:163], v[28:31]
v_exp_f32 v159, v159
v_mfma_f32_16x16x32_bf16 v[60:63], a[124:127], v[164:167], v[60:63]
v_cvt_pk_bf16_f32 v174, v156, v157
v_cvt_pk_bf16_f32 v175, v158, v159
v_mfma_f32_16x16x32_bf16 v[144:147], a[64:67], a[32:35], v[184:187]
v_exp_f32 v128, v128
v_mfma_f32_16x16x32_bf16 v[152:155], a[64:67], a[48:51], v[188:191]
v_exp_f32 v129, v129
v_mfma_f32_16x16x32_bf16 v[156:159], a[68:71], a[48:51], v[188:191]
ds_read_b128 a[64:67], v219 offset:32768
v_mov_b32 v239, 0
v_mfma_f32_16x16x32_bf16 v[148:151], a[68:71], a[32:35], v[184:187]
v_exp_f32 v130, v130
v_mfma_f32_16x16x32_bf16 v[144:147], a[72:75], a[36:39], v[144:147]
v_exp_f32 v131, v131
v_mfma_f32_16x16x32_bf16 v[152:155], a[72:75], a[52:55], v[152:155]
ds_read_b128 a[68:71], v219 offset:36864
v_cvt_pk_bf16_f32 v160, v128, v129
v_mfma_f32_16x16x32_bf16 v[156:159], a[76:79], a[52:55], v[156:159]
buffer_store_dwordx4 a[220:223], v208, s[36:39], s86 offen offset:2048 sc1
v_mfma_f32_16x16x32_bf16 v[148:151], a[76:79], a[36:39], v[148:151]
v_exp_f32 v132, v132
v_mfma_f32_16x16x32_bf16 v[144:147], a[80:83], a[40:43], v[144:147]
ds_read_b128 a[72:75], v219 offset:33280
v_cvt_pk_bf16_f32 v243, v240, v241
v_mfma_f32_16x16x32_bf16 v[152:155], a[80:83], a[56:59], v[152:155]
v_exp_f32 v133, v133
v_mfma_f32_16x16x32_bf16 v[156:159], a[84:87], a[56:59], v[156:159]
ds_read_b128 a[76:79], v219 offset:37376
v_cvt_pk_bf16_f32 v161, v130, v131
v_mfma_f32_16x16x32_bf16 v[148:151], a[84:87], a[40:43], v[148:151]
v_exp_f32 v134, v134
v_mfma_f32_16x16x32_bf16 v[144:147], a[88:91], a[44:47], v[144:147]
buffer_store_dwordx4 a[224:227], v208, s[36:39], s86 offen offset:3072 sc1
v_mfma_f32_16x16x32_bf16 v[152:155], a[88:91], a[60:63], v[152:155]
ds_read_b128 a[80:83], v219 offset:33792
s_add_u32 s86, s86, 4096
v_mfma_f32_16x16x32_bf16 v[156:159], a[92:95], a[60:63], v[156:159]
v_exp_f32 v135, v135
v_mfma_f32_16x16x32_bf16 v[148:151], a[92:95], a[44:47], v[148:151]
ds_read_b128 a[84:87], v219 offset:37888
v_cvt_pk_bf16_f32 v162, v132, v133
v_mfma_f32_16x16x32_bf16 v[200:203], v[232:235], v[168:171], v[200:203]
ds_read_b128 a[88:91], v219 offset:34304
v_lshlrev_b32 v250, 16, v243
v_mfma_f32_16x16x32_bf16 v[204:207], v[232:235], v[172:175], v[204:207]
v_exp_f32 v136, v136
v_mfma_f32_16x16x32_bf16 v[96:99], a[96:99], v[172:175], v[96:99]
buffer_store_dwordx4 a[228:231], v208, s[36:39], s86 offen offset:0 sc1
v_mfma_f32_16x16x32_bf16 v[64:67], a[96:99], v[168:171], v[64:67]
v_exp_f32 v137, v137
v_mfma_f32_16x16x32_bf16 v[68:71], a[100:103], v[168:171], v[68:71]
ds_read_b128 a[92:95], v219 offset:38400
v_cvt_pk_bf16_f32 v163, v134, v135
v_mfma_f32_16x16x32_bf16 v[100:103], a[100:103], v[172:175], v[100:103]
v_exp_f32 v138, v138
v_mfma_f32_16x16x32_bf16 v[104:107], a[104:107], v[172:175], v[104:107]
ds_read_b64_tr_b16 a[96:97], v220 offset:40960
v_and_b32 v251, 0xffff0000, v243
v_mfma_f32_16x16x32_bf16 v[72:75], a[104:107], v[168:171], v[72:75]
v_exp_f32 v139, v139
v_mfma_f32_16x16x32_bf16 v[76:79], a[108:111], v[168:171], v[76:79]
ds_read_b64_tr_b16 a[98:99], v220 offset:45056
v_cvt_pk_bf16_f32 v164, v136, v137
v_mfma_f32_16x16x32_bf16 v[108:111], a[108:111], v[172:175], v[108:111]
buffer_store_dwordx4 a[232:235], v208, s[36:39], s87 offen offset:0 sc1
v_mfma_f32_16x16x32_bf16 v[112:115], a[112:115], v[172:175], v[112:115]
v_exp_f32 v140, v140
v_mfma_f32_16x16x32_bf16 v[80:83], a[112:115], v[168:171], v[80:83]
s_mov_b32 m0, s79
s_add_u32 s79, s79, 3072
v_mfma_f32_16x16x32_bf16 v[84:87], a[116:119], v[168:171], v[84:87]
ds_read_b64_tr_b16 a[100:101], v221 offset:40960
ds_read_b64_tr_b16 a[102:103], v221 offset:45056
v_mfma_f32_16x16x32_bf16 v[116:119], a[116:119], v[172:175], v[116:119]
v_exp_f32 v141, v141
v_mfma_f32_16x16x32_bf16 v[120:123], a[120:123], v[172:175], v[120:123]
v_cvt_pk_bf16_f32 v165, v138, v139
v_sub_f32 v250, v240, v250
v_mfma_f32_16x16x32_bf16 v[88:91], a[120:123], v[168:171], v[88:91]
v_exp_f32 v142, v142
v_mfma_f32_16x16x32_bf16 v[92:95], a[124:127], v[168:171], v[92:95]
buffer_load_dwordx4 v208, s[36:39], s78 offen offset:0 sc1 lds
v_mfma_f32_16x16x32_bf16 v[124:127], a[124:127], v[172:175], v[124:127]
v_exp_f32 v143, v143
v_cvt_pk_bf16_f32 v166, v140, v141
v_cvt_pk_bf16_f32 v167, v142, v143
s_waitcnt lgkmcnt(8)
v_mfma_f32_16x16x32_bf16 v[128:131], a[64:67], a[0:3], v[176:179]
ds_read_b64_tr_b16 a[104:105], v220 offset:41472
ds_read_b64_tr_b16 a[106:107], v220 offset:45568
v_mfma_f32_16x16x32_bf16 v[136:139], a[64:67], a[16:19], v[180:183]
v_exp_f32 v144, v144
v_mfma_f32_16x16x32_bf16 v[140:143], a[68:71], a[16:19], v[180:183]
v_exp_f32 v145, v145
v_mfma_f32_16x16x32_bf16 v[132:135], a[68:71], a[0:3], v[176:179]
ds_read_b64_tr_b16 a[108:109], v221 offset:41472
ds_read_b64_tr_b16 a[110:111], v221 offset:45568
v_mfma_f32_16x16x32_bf16 v[128:131], a[72:75], a[4:7], v[128:131]
v_exp_f32 v146, v146
v_mfma_f32_16x16x32_bf16 v[136:139], a[72:75], a[20:23], v[136:139]
buffer_load_dwordx4 v208, s[36:39], s78 offen offset:1024 sc1 lds
v_mfma_f32_16x16x32_bf16 v[140:143], a[76:79], a[20:23], v[140:143]
v_sub_f32 v251, v241, v251
v_cvt_pk_bf16_f32 v168, v144, v145
v_mfma_f32_16x16x32_bf16 v[132:135], a[76:79], a[4:7], v[132:135]
s_waitcnt lgkmcnt(8)
ds_read_b64_tr_b16 a[112:113], v220 offset:41984
v_mfma_f32_16x16x32_bf16 v[128:131], a[80:83], a[8:11], v[128:131]
v_exp_f32 v147, v147
v_mfma_f32_16x16x32_bf16 v[136:139], a[80:83], a[24:27], v[136:139]
ds_read_b64_tr_b16 a[114:115], v220 offset:46080
ds_read_b64_tr_b16 a[116:117], v221 offset:41984
v_mfma_f32_16x16x32_bf16 v[140:143], a[84:87], a[24:27], v[140:143]
v_exp_f32 v148, v148
v_mfma_f32_16x16x32_bf16 v[132:135], a[84:87], a[8:11], v[132:135]
buffer_load_dwordx4 v208, s[36:39], s78 offen offset:2048 sc1 lds
s_add_u32 s78, s78, 3072
v_mfma_f32_16x16x32_bf16 v[128:131], a[88:91], a[12:15], v[128:131]
v_exp_f32 v149, v149
v_mfma_f32_16x16x32_bf16 v[136:139], a[88:91], a[28:31], v[136:139]
ds_read_b64_tr_b16 a[118:119], v221 offset:46080
v_cvt_pk_bf16_f32 v240, v240, v250
v_mfma_f32_16x16x32_bf16 v[140:143], a[92:95], a[28:31], v[140:143]
v_cvt_pk_bf16_f32 v169, v146, v147
s_mov_b32 m0, s79
v_mfma_f32_16x16x32_bf16 v[132:135], a[92:95], a[12:15], v[132:135]
v_exp_f32 v150, v150
v_mfma_f32_16x16x32_bf16 v[192:195], v[232:235], v[160:163], v[192:195]
s_add_u32 s79, s79, 3072
ds_read_b64_tr_b16 a[120:121], v220 offset:42496
v_mfma_f32_16x16x32_bf16 v[196:199], v[232:235], v[164:167], v[196:199]
buffer_load_dwordx4 v208, s[36:39], s78 offen offset:0 sc1 lds
s_waitcnt lgkmcnt(5)
v_mfma_f32_16x16x32_bf16 v[32:35], a[96:99], v[164:167], v[32:35]
ds_read_b64_tr_b16 a[122:123], v220 offset:46592
v_cvt_pk_bf16_f32 v170, v148, v149
v_mfma_f32_16x16x32_bf16 v[0:3], a[96:99], v[160:163], v[0:3]
v_exp_f32 v151, v151
v_mfma_f32_16x16x32_bf16 v[4:7], a[100:103], v[160:163], v[4:7]
v_exp_f32 v152, v152
v_mfma_f32_16x16x32_bf16 v[36:39], a[100:103], v[164:167], v[36:39]
ds_read_b64_tr_b16 a[124:125], v221 offset:42496
v_cvt_pk_bf16_f32 v242, v251, v241
v_mfma_f32_16x16x32_bf16 v[40:43], a[104:107], v[164:167], v[40:43]
ds_read_b64_tr_b16 a[126:127], v221 offset:46592
v_cvt_pk_bf16_f32 v171, v150, v151
v_mfma_f32_16x16x32_bf16 v[8:11], a[104:107], v[160:163], v[8:11]
v_exp_f32 v153, v153
v_mfma_f32_16x16x32_bf16 v[12:15], a[108:111], v[160:163], v[12:15]
buffer_load_dwordx4 v208, s[36:39], s78 offen offset:1024 sc1 lds
v_mfma_f32_16x16x32_bf16 v[44:47], a[108:111], v[164:167], v[44:47]
s_waitcnt lgkmcnt(0)
v_mov_b32 v241, v243
v_mfma_f32_16x16x32_bf16 v[48:51], a[112:115], v[164:167], v[48:51]
v_exp_f32 v154, v154
v_mfma_f32_16x16x32_bf16 v[16:19], a[112:115], v[160:163], v[16:19]
v_exp_f32 v155, v155
v_mfma_f32_16x16x32_bf16 v[20:23], a[116:119], v[160:163], v[20:23]
v_cvt_pk_bf16_f32 v172, v152, v153
v_mov_b32 v243, 0
v_mfma_f32_16x16x32_bf16 v[52:55], a[116:119], v[164:167], v[52:55]
v_exp_f32 v156, v156
v_mfma_f32_16x16x32_bf16 v[56:59], a[120:123], v[164:167], v[56:59]
buffer_load_dwordx4 v208, s[36:39], s78 offen offset:2048 sc1 lds
s_add_u32 s78, s78, 3072
v_mfma_f32_16x16x32_bf16 v[24:27], a[120:123], v[160:163], v[24:27]
v_exp_f32 v157, v157
v_mfma_f32_16x16x32_bf16 v[28:31], a[124:127], v[160:163], v[28:31]
v_cvt_pk_bf16_f32 v173, v154, v155
s_add_u32 m0, s71, 16384
v_mfma_f32_16x16x32_bf16 v[60:63], a[124:127], v[164:167], v[60:63]
v_exp_f32 v158, v158
v_exp_f32 v159, v159
v_cvt_pk_bf16_f32 v174, v156, v157
v_cvt_pk_bf16_f32 v175, v158, v159
s_waitcnt vmcnt(10)
s_barrier
v_mfma_f32_16x16x32_bf16 v[144:147], a[64:67], a[32:35], v[184:187]
v_exp_f32 v128, v128
v_mfma_f32_16x16x32_bf16 v[152:155], a[64:67], a[48:51], v[188:191]
v_exp_f32 v129, v129
v_mfma_f32_16x16x32_bf16 v[156:159], a[68:71], a[48:51], v[188:191]
v_exp_f32 v130, v130
v_mfma_f32_16x16x32_bf16 v[148:151], a[68:71], a[32:35], v[184:187]
v_exp_f32 v131, v131
v_mfma_f32_16x16x32_bf16 v[144:147], a[72:75], a[36:39], v[144:147]
ds_read_b128 a[64:67], v219 offset:49152
v_cvt_pk_bf16_f32 v160, v128, v129
v_mfma_f32_16x16x32_bf16 v[152:155], a[72:75], a[52:55], v[152:155]
buffer_load_dwordx4 v222, s[44:47], s72 offen offset:0 lds
v_mfma_f32_16x16x32_bf16 v[156:159], a[76:79], a[52:55], v[156:159]
ds_read_b128 a[68:71], v219 offset:53248
ds_read_b128 a[72:75], v219 offset:49664
v_mfma_f32_16x16x32_bf16 v[148:151], a[76:79], a[36:39], v[148:151]
v_exp_f32 v132, v132
v_mfma_f32_16x16x32_bf16 v[144:147], a[80:83], a[40:43], v[144:147]
v_exp_f32 v133, v133
v_mfma_f32_16x16x32_bf16 v[152:155], a[80:83], a[56:59], v[152:155]
ds_read_b128 a[76:79], v219 offset:53760
v_cvt_pk_bf16_f32 v161, v130, v131
v_mfma_f32_16x16x32_bf16 v[156:159], a[84:87], a[56:59], v[156:159]
buffer_store_dwordx4 a[236:239], v208, s[36:39], s87 offen offset:1024 sc1
v_mfma_f32_16x16x32_bf16 v[148:151], a[84:87], a[40:43], v[148:151]
v_exp_f32 v134, v134
v_mfma_f32_16x16x32_bf16 v[144:147], a[88:91], a[44:47], v[144:147]
ds_read_b128 a[80:83], v219 offset:50176
v_cvt_pk_bf16_f32 v162, v132, v133
v_mfma_f32_16x16x32_bf16 v[152:155], a[88:91], a[60:63], v[152:155]
v_exp_f32 v135, v135
v_mfma_f32_16x16x32_bf16 v[156:159], a[92:95], a[60:63], v[156:159]
ds_read_b128 a[84:87], v219 offset:54272
ds_read_b128 a[88:91], v219 offset:50688
v_mfma_f32_16x16x32_bf16 v[148:151], a[92:95], a[44:47], v[148:151]
v_exp_f32 v136, v136
v_mfma_f32_16x16x32_bf16 v[200:203], v[232:235], v[168:171], v[200:203]
buffer_load_dwordx4 v223, s[44:47], s72 offen offset:1024 lds
v_mfma_f32_16x16x32_bf16 v[204:207], v[232:235], v[172:175], v[204:207]
v_exp_f32 v137, v137
v_mfma_f32_16x16x32_bf16 v[96:99], a[96:99], v[172:175], v[96:99]
ds_read_b128 a[92:95], v219 offset:54784
v_cvt_pk_bf16_f32 v163, v134, v135
v_mfma_f32_16x16x32_bf16 v[64:67], a[96:99], v[168:171], v[64:67]
v_exp_f32 v138, v138
v_mfma_f32_16x16x32_bf16 v[68:71], a[100:103], v[168:171], v[68:71]
buffer_store_dwordx4 a[240:243], v208, s[36:39], s87 offen offset:2048 sc1
v_mfma_f32_16x16x32_bf16 v[100:103], a[100:103], v[172:175], v[100:103]
v_exp_f32 v139, v139
v_mfma_f32_16x16x32_bf16 v[104:107], a[104:107], v[172:175], v[104:107]
ds_read_b64_tr_b16 a[96:97], v220 offset:57344
ds_read_b64_tr_b16 a[98:99], v220 offset:61440
v_mfma_f32_16x16x32_bf16 v[72:75], a[104:107], v[168:171], v[72:75]
ds_read_b64_tr_b16 a[100:101], v221 offset:57344
v_cvt_pk_bf16_f32 v164, v136, v137
v_mfma_f32_16x16x32_bf16 v[76:79], a[108:111], v[168:171], v[76:79]
v_exp_f32 v140, v140
v_mfma_f32_16x16x32_bf16 v[108:111], a[108:111], v[172:175], v[108:111]
ds_read_b64_tr_b16 a[102:103], v221 offset:61440
v_cvt_pk_bf16_f32 v165, v138, v139
v_mfma_f32_16x16x32_bf16 v[112:115], a[112:115], v[172:175], v[112:115]
v_exp_f32 v141, v141
v_mfma_f32_16x16x32_bf16 v[80:83], a[112:115], v[168:171], v[80:83]
buffer_load_dwordx4 v224, s[44:47], s72 offen offset:2048 lds
v_mfma_f32_16x16x32_bf16 v[84:87], a[116:119], v[168:171], v[84:87]
v_exp_f32 v142, v142
v_mfma_f32_16x16x32_bf16 v[116:119], a[116:119], v[172:175], v[116:119]
ds_read_b64_tr_b16 a[104:105], v220 offset:57856
ds_read_b64_tr_b16 a[106:107], v220 offset:61952
v_mfma_f32_16x16x32_bf16 v[120:123], a[120:123], v[172:175], v[120:123]
v_exp_f32 v143, v143
v_mfma_f32_16x16x32_bf16 v[88:91], a[120:123], v[168:171], v[88:91]
v_exp_f32 v144, v144
v_mfma_f32_16x16x32_bf16 v[92:95], a[124:127], v[168:171], v[92:95]
v_cvt_pk_bf16_f32 v166, v140, v141
v_cvt_pk_bf16_f32 v167, v142, v143
v_mfma_f32_16x16x32_bf16 v[124:127], a[124:127], v[172:175], v[124:127]
s_waitcnt lgkmcnt(10)
ds_read_b64_tr_b16 a[108:109], v221 offset:57856
v_mfma_f32_16x16x32_bf16 v[128:131], a[64:67], a[0:3], v[176:179]
v_exp_f32 v145, v145
v_mfma_f32_16x16x32_bf16 v[136:139], a[64:67], a[16:19], v[180:183]
buffer_load_dwordx4 v225, s[44:47], s72 offen offset:3072 lds
v_mfma_f32_16x16x32_bf16 v[140:143], a[68:71], a[16:19], v[180:183]
s_add_u32 s72, s72, 8192
s_add_u32 m0, s71, 32768
v_mfma_f32_16x16x32_bf16 v[132:135], a[68:71], a[0:3], v[176:179]
ds_read_b64_tr_b16 a[110:111], v221 offset:61952
ds_read_b64_tr_b16 a[112:113], v220 offset:58368
v_mfma_f32_16x16x32_bf16 v[128:131], a[72:75], a[4:7], v[128:131]
v_exp_f32 v146, v146
v_mfma_f32_16x16x32_bf16 v[136:139], a[72:75], a[20:23], v[136:139]
v_exp_f32 v147, v147
v_mfma_f32_16x16x32_bf16 v[140:143], a[76:79], a[20:23], v[140:143]
ds_read_b64_tr_b16 a[114:115], v220 offset:62464
v_cvt_pk_bf16_f32 v168, v144, v145
v_mfma_f32_16x16x32_bf16 v[132:135], a[76:79], a[4:7], v[132:135]
buffer_load_dwordx4 v222, s[44:47], s72 offen offset:0 lds
s_waitcnt lgkmcnt(10)
v_mfma_f32_16x16x32_bf16 v[128:131], a[80:83], a[8:11], v[128:131]
v_exp_f32 v148, v148
v_mfma_f32_16x16x32_bf16 v[136:139], a[80:83], a[24:27], v[136:139]
v_exp_f32 v149, v149
v_mfma_f32_16x16x32_bf16 v[140:143], a[84:87], a[24:27], v[140:143]
ds_read_b64_tr_b16 a[116:117], v221 offset:58368
ds_read_b64_tr_b16 a[118:119], v221 offset:62464
v_mfma_f32_16x16x32_bf16 v[132:135], a[84:87], a[8:11], v[132:135]
ds_read_b64_tr_b16 a[120:121], v220 offset:58880
v_cvt_pk_bf16_f32 v169, v146, v147
v_mfma_f32_16x16x32_bf16 v[128:131], a[88:91], a[12:15], v[128:131]
v_exp_f32 v150, v150
v_mfma_f32_16x16x32_bf16 v[136:139], a[88:91], a[28:31], v[136:139]
buffer_load_dwordx4 v223, s[44:47], s72 offen offset:1024 lds
v_mfma_f32_16x16x32_bf16 v[140:143], a[92:95], a[28:31], v[140:143]
ds_read_b64_tr_b16 a[122:123], v220 offset:62976
v_cvt_pk_bf16_f32 v170, v148, v149
v_mfma_f32_16x16x32_bf16 v[132:135], a[92:95], a[12:15], v[132:135]
v_exp_f32 v151, v151
v_mfma_f32_16x16x32_bf16 v[192:195], v[232:235], v[160:163], v[192:195]
v_exp_f32 v152, v152
v_mfma_f32_16x16x32_bf16 v[196:199], v[232:235], v[164:167], v[196:199]
s_waitcnt lgkmcnt(6)
ds_read_b64_tr_b16 a[124:125], v221 offset:58880
v_mfma_f32_16x16x32_bf16 v[32:35], a[96:99], v[164:167], v[32:35]
buffer_load_dwordx4 v224, s[44:47], s72 offen offset:2048 lds
v_mfma_f32_16x16x32_bf16 v[0:3], a[96:99], v[160:163], v[0:3]
ds_read_b64_tr_b16 a[126:127], v221 offset:62976
v_cvt_pk_bf16_f32 v171, v150, v151
v_mfma_f32_16x16x32_bf16 v[4:7], a[100:103], v[160:163], v[4:7]
v_exp_f32 v153, v153
v_mfma_f32_16x16x32_bf16 v[36:39], a[100:103], v[164:167], v[36:39]
v_exp_f32 v154, v154
v_mfma_f32_16x16x32_bf16 v[40:43], a[104:107], v[164:167], v[40:43]
v_exp_f32 v155, v155
v_mfma_f32_16x16x32_bf16 v[8:11], a[104:107], v[160:163], v[8:11]
buffer_load_dwordx4 v225, s[44:47], s72 offen offset:3072 lds
v_mfma_f32_16x16x32_bf16 v[12:15], a[108:111], v[160:163], v[12:15]
s_add_u32 s72, s72, 8192
v_cvt_pk_bf16_f32 v172, v152, v153
v_mfma_f32_16x16x32_bf16 v[44:47], a[108:111], v[164:167], v[44:47]
s_waitcnt lgkmcnt(0)
s_lshr_b32 s77, s65, 3
v_mfma_f32_16x16x32_bf16 v[48:51], a[112:115], v[164:167], v[48:51]
v_exp_f32 v156, v156
v_mfma_f32_16x16x32_bf16 v[16:19], a[112:115], v[160:163], v[16:19]
s_mul_i32 s77, s77, 512
s_add_u32 s77, s77, .Lk_fbret-.Lk_start-11264
v_mfma_f32_16x16x32_bf16 v[20:23], a[116:119], v[160:163], v[20:23]
v_and_b32 v230, 3, v209
v_lshlrev_b32 v230, 7, v230
v_mfma_f32_16x16x32_bf16 v[52:55], a[116:119], v[164:167], v[52:55]
v_exp_f32 v157, v157
v_mfma_f32_16x16x32_bf16 v[56:59], a[120:123], v[164:167], v[56:59]
v_cvt_pk_bf16_f32 v173, v154, v155
v_mfma_f32_16x16x32_bf16 v[24:27], a[120:123], v[160:163], v[24:27]
buffer_load_dword v230, v230, s[60:63], s77 offen
v_mfma_f32_16x16x32_bf16 v[28:31], a[124:127], v[160:163], v[28:31]
v_exp_f32 v158, v158
v_mfma_f32_16x16x32_bf16 v[60:63], a[124:127], v[164:167], v[60:63]
v_exp_f32 v159, v159
v_cvt_pk_bf16_f32 v174, v156, v157
v_cvt_pk_bf16_f32 v175, v158, v159
v_mfma_f32_16x16x32_bf16 v[144:147], a[64:67], a[32:35], v[184:187]
v_exp_f32 v128, v128
v_mfma_f32_16x16x32_bf16 v[152:155], a[64:67], a[48:51], v[188:191]
v_exp_f32 v129, v129
v_mfma_f32_16x16x32_bf16 v[156:159], a[68:71], a[48:51], v[188:191]
v_exp_f32 v130, v130
v_mfma_f32_16x16x32_bf16 v[148:151], a[68:71], a[32:35], v[184:187]
v_exp_f32 v131, v131
v_mfma_f32_16x16x32_bf16 v[144:147], a[72:75], a[36:39], v[144:147]
ds_read_b128 a[64:67], v219 offset:0
v_cvt_pk_bf16_f32 v160, v128, v129
v_mfma_f32_16x16x32_bf16 v[152:155], a[72:75], a[52:55], v[152:155]
buffer_store_dwordx4 a[244:247], v208, s[36:39], s87 offen offset:3072 sc1
v_mfma_f32_16x16x32_bf16 v[156:159], a[76:79], a[52:55], v[156:159]
ds_read_b128 a[68:71], v219 offset:4096
s_add_u32 s87, s87, 4096
v_mfma_f32_16x16x32_bf16 v[148:151], a[76:79], a[36:39], v[148:151]
v_exp_f32 v132, v132
v_mfma_f32_16x16x32_bf16 v[144:147], a[80:83], a[40:43], v[144:147]
ds_read_b128 a[72:75], v219 offset:512
v_cvt_pk_bf16_f32 v161, v130, v131
v_mfma_f32_16x16x32_bf16 v[152:155], a[80:83], a[56:59], v[152:155]
v_exp_f32 v133, v133
v_mfma_f32_16x16x32_bf16 v[156:159], a[84:87], a[56:59], v[156:159]
ds_read_b128 a[76:79], v219 offset:4608
ds_read_b128 a[80:83], v219 offset:1024
v_mfma_f32_16x16x32_bf16 v[148:151], a[84:87], a[40:43], v[148:151]
v_exp_f32 v134, v134
v_mfma_f32_16x16x32_bf16 v[144:147], a[88:91], a[44:47], v[144:147]
buffer_store_dwordx4 a[248:251], v208, s[36:39], s87 offen offset:0 sc1
v_mfma_f32_16x16x32_bf16 v[152:155], a[88:91], a[60:63], v[152:155]
v_exp_f32 v135, v135
v_mfma_f32_16x16x32_bf16 v[156:159], a[92:95], a[60:63], v[156:159]
ds_read_b128 a[84:87], v219 offset:5120
v_cvt_pk_bf16_f32 v162, v132, v133
v_mfma_f32_16x16x32_bf16 v[148:151], a[92:95], a[44:47], v[148:151]
v_exp_f32 v136, v136
v_mfma_f32_16x16x32_bf16 v[200:203], v[232:235], v[168:171], v[200:203]
ds_read_b128 a[88:91], v219 offset:1536
ds_read_b128 a[92:95], v219 offset:5632
v_mfma_f32_16x16x32_bf16 v[204:207], v[232:235], v[172:175], v[204:207]
buffer_store_dwordx4 v[236:239], v244, s[36:39], s95 offen offset:0 sc1
v_mfma_f32_16x16x32_bf16 v[96:99], a[96:99], v[172:175], v[96:99]
v_exp_f32 v137, v137
v_mfma_f32_16x16x32_bf16 v[64:67], a[96:99], v[168:171], v[64:67]
ds_read_b64_tr_b16 a[96:97], v220 offset:8192
v_cvt_pk_bf16_f32 v163, v134, v135
v_mfma_f32_16x16x32_bf16 v[68:71], a[100:103], v[168:171], v[68:71]
v_exp_f32 v138, v138
v_mfma_f32_16x16x32_bf16 v[100:103], a[100:103], v[172:175], v[100:103]
v_exp_f32 v139, v139
v_mfma_f32_16x16x32_bf16 v[104:107], a[104:107], v[172:175], v[104:107]
ds_read_b64_tr_b16 a[98:99], v220 offset:12288
v_cvt_pk_bf16_f32 v164, v136, v137
v_mfma_f32_16x16x32_bf16 v[72:75], a[104:107], v[168:171], v[72:75]
buffer_store_dwordx4 v[240:243], v244, s[36:39], s96 offen offset:0 sc1
v_mfma_f32_16x16x32_bf16 v[76:79], a[108:111], v[168:171], v[76:79]
v_exp_f32 v140, v140
v_mfma_f32_16x16x32_bf16 v[108:111], a[108:111], v[172:175], v[108:111]
s_mov_b32 m0, s79
s_add_u32 s79, s79, 3072
v_mfma_f32_16x16x32_bf16 v[112:115], a[112:115], v[172:175], v[112:115]
ds_read_b64_tr_b16 a[100:101], v221 offset:8192
ds_read_b64_tr_b16 a[102:103], v221 offset:12288
v_mfma_f32_16x16x32_bf16 v[80:83], a[112:115], v[168:171], v[80:83]
v_exp_f32 v141, v141
v_mfma_f32_16x16x32_bf16 v[84:87], a[116:119], v[168:171], v[84:87]
ds_read_b64_tr_b16 a[104:105], v220 offset:8704
v_cvt_pk_bf16_f32 v165, v138, v139
v_mfma_f32_16x16x32_bf16 v[116:119], a[116:119], v[172:175], v[116:119]
v_exp_f32 v142, v142
v_mfma_f32_16x16x32_bf16 v[120:123], a[120:123], v[172:175], v[120:123]
buffer_load_dwordx4 v208, s[36:39], s78 offen offset:0 sc1 lds
v_mfma_f32_16x16x32_bf16 v[88:91], a[120:123], v[168:171], v[88:91]
ds_read_b64_tr_b16 a[106:107], v220 offset:12800
v_cvt_pk_bf16_f32 v166, v140, v141
v_mfma_f32_16x16x32_bf16 v[92:95], a[124:127], v[168:171], v[92:95]
v_exp_f32 v143, v143
v_mfma_f32_16x16x32_bf16 v[124:127], a[124:127], v[172:175], v[124:127]
v_cvt_pk_bf16_f32 v167, v142, v143
s_waitcnt lgkmcnt(10)
v_mfma_f32_16x16x32_bf16 v[128:131], a[64:67], a[0:3], v[176:179]
v_exp_f32 v144, v144
v_mfma_f32_16x16x32_bf16 v[136:139], a[64:67], a[16:19], v[180:183]
v_exp_f32 v145, v145
v_mfma_f32_16x16x32_bf16 v[140:143], a[68:71], a[16:19], v[180:183]
ds_read_b64_tr_b16 a[108:109], v221 offset:8704
ds_read_b64_tr_b16 a[110:111], v221 offset:12800
v_mfma_f32_16x16x32_bf16 v[132:135], a[68:71], a[0:3], v[176:179]
v_exp_f32 v146, v146
v_mfma_f32_16x16x32_bf16 v[128:131], a[72:75], a[4:7], v[128:131]
buffer_load_dwordx4 v208, s[36:39], s78 offen offset:1024 sc1 lds
v_mfma_f32_16x16x32_bf16 v[136:139], a[72:75], a[20:23], v[136:139]
v_exp_f32 v147, v147
v_mfma_f32_16x16x32_bf16 v[140:143], a[76:79], a[20:23], v[140:143]
ds_read_b64_tr_b16 a[112:113], v220 offset:9216
v_cvt_pk_bf16_f32 v168, v144, v145
v_mfma_f32_16x16x32_bf16 v[132:135], a[76:79], a[4:7], v[132:135]
s_waitcnt lgkmcnt(9)
ds_read_b64_tr_b16 a[114:115], v220 offset:13312
v_mfma_f32_16x16x32_bf16 v[128:131], a[80:83], a[8:11], v[128:131]
v_exp_f32 v148, v148
v_mfma_f32_16x16x32_bf16 v[136:139], a[80:83], a[24:27], v[136:139]
v_exp_f32 v149, v149
v_mfma_f32_16x16x32_bf16 v[140:143], a[84:87], a[24:27], v[140:143]
buffer_load_dwordx4 v208, s[36:39], s78 offen offset:2048 sc1 lds
v_mfma_f32_16x16x32_bf16 v[132:135], a[84:87], a[8:11], v[132:135]
s_add_u32 s78, s78, 3072
ds_read_b64_tr_b16 a[116:117], v221 offset:9216
v_mfma_f32_16x16x32_bf16 v[128:131], a[88:91], a[12:15], v[128:131]
ds_read_b64_tr_b16 a[118:119], v221 offset:13312
v_cvt_pk_bf16_f32 v169, v146, v147
v_mfma_f32_16x16x32_bf16 v[136:139], a[88:91], a[28:31], v[136:139]
v_exp_f32 v150, v150
v_mfma_f32_16x16x32_bf16 v[140:143], a[92:95], a[28:31], v[140:143]
s_mov_b32 m0, s79
s_add_u32 s79, s79, 3072
v_mfma_f32_16x16x32_bf16 v[132:135], a[92:95], a[12:15], v[132:135]
ds_read_b64_tr_b16 a[120:121], v220 offset:9728
ds_read_b64_tr_b16 a[122:123], v220 offset:13824
v_mfma_f32_16x16x32_bf16 v[192:195], v[232:235], v[160:163], v[192:195]
v_exp_f32 v151, v151
v_mfma_f32_16x16x32_bf16 v[196:199], v[232:235], v[164:167], v[196:199]
buffer_load_dwordx4 v208, s[36:39], s78 offen offset:0 sc1 lds
s_waitcnt lgkmcnt(6)
v_mfma_f32_16x16x32_bf16 v[32:35], a[96:99], v[164:167], v[32:35]
ds_read_b64_tr_b16 a[124:125], v221 offset:9728
v_cvt_pk_bf16_f32 v170, v148, v149
v_mfma_f32_16x16x32_bf16 v[0:3], a[96:99], v[160:163], v[0:3]
v_exp_f32 v152, v152
v_mfma_f32_16x16x32_bf16 v[4:7], a[100:103], v[160:163], v[4:7]
ds_read_b64_tr_b16 a[126:127], v221 offset:13824
v_cvt_pk_bf16_f32 v171, v150, v151
v_mfma_f32_16x16x32_bf16 v[36:39], a[100:103], v[164:167], v[36:39]
v_exp_f32 v153, v153
v_mfma_f32_16x16x32_bf16 v[40:43], a[104:107], v[164:167], v[40:43]
v_exp_f32 v154, v154
v_mfma_f32_16x16x32_bf16 v[8:11], a[104:107], v[160:163], v[8:11]
buffer_load_dwordx4 v208, s[36:39], s78 offen offset:1024 sc1 lds
v_mfma_f32_16x16x32_bf16 v[12:15], a[108:111], v[160:163], v[12:15]
v_exp_f32 v155, v155
v_mfma_f32_16x16x32_bf16 v[44:47], a[108:111], v[164:167], v[44:47]
s_waitcnt lgkmcnt(0)
v_cvt_pk_bf16_f32 v172, v152, v153
v_mfma_f32_16x16x32_bf16 v[48:51], a[112:115], v[164:167], v[48:51]
v_exp_f32 v156, v156
v_mfma_f32_16x16x32_bf16 v[16:19], a[112:115], v[160:163], v[16:19]
v_exp_f32 v157, v157
v_mfma_f32_16x16x32_bf16 v[20:23], a[116:119], v[160:163], v[20:23]
v_cvt_pk_bf16_f32 v173, v154, v155
v_mfma_f32_16x16x32_bf16 v[52:55], a[116:119], v[164:167], v[52:55]
buffer_load_dwordx4 v208, s[36:39], s78 offen offset:2048 sc1 lds
v_mfma_f32_16x16x32_bf16 v[56:59], a[120:123], v[164:167], v[56:59]
s_add_u32 s78, s78, 3072
v_mfma_f32_16x16x32_bf16 v[24:27], a[120:123], v[160:163], v[24:27]
v_exp_f32 v158, v158
v_mfma_f32_16x16x32_bf16 v[28:31], a[124:127], v[160:163], v[28:31]
v_exp_f32 v159, v159
v_mfma_f32_16x16x32_bf16 v[60:63], a[124:127], v[164:167], v[60:63]
v_cvt_pk_bf16_f32 v174, v156, v157
v_cvt_pk_bf16_f32 v175, v158, v159
s_waitcnt vmcnt(11)
s_barrier
v_mfma_f32_16x16x32_bf16 v[144:147], a[64:67], a[32:35], v[184:187]
v_exp_f32 v128, v128
v_mfma_f32_16x16x32_bf16 v[152:155], a[64:67], a[48:51], v[188:191]
v_exp_f32 v129, v129
v_mfma_f32_16x16x32_bf16 v[156:159], a[68:71], a[48:51], v[188:191]
s_add_u32 m0, s71, 49152
ds_read_b128 a[64:67], v219 offset:16384
v_mfma_f32_16x16x32_bf16 v[148:151], a[68:71], a[32:35], v[184:187]
v_exp_f32 v130, v130
v_mfma_f32_16x16x32_bf16 v[144:147], a[72:75], a[36:39], v[144:147]
v_exp_f32 v131, v131
v_mfma_f32_16x16x32_bf16 v[152:155], a[72:75], a[52:55], v[152:155]
ds_read_b128 a[68:71], v219 offset:20480
v_cvt_pk_bf16_f32 v160, v128, v129
v_mfma_f32_16x16x32_bf16 v[156:159], a[76:79], a[52:55], v[156:159]
buffer_load_dwordx4 v222, s[44:47], s72 offen offset:0 lds
v_mfma_f32_16x16x32_bf16 v[148:151], a[76:79], a[36:39], v[148:151]
s_add_u32 s15, s85, 16384
ds_read_b128 a[72:75], v219 offset:16896
v_mfma_f32_16x16x32_bf16 v[144:147], a[80:83], a[40:43], v[144:147]
v_exp_f32 v132, v132
v_mfma_f32_16x16x32_bf16 v[152:155], a[80:83], a[56:59], v[152:155]
v_exp_f32 v133, v133
v_mfma_f32_16x16x32_bf16 v[156:159], a[84:87], a[56:59], v[156:159]
ds_read_b128 a[76:79], v219 offset:20992
v_cvt_pk_bf16_f32 v161, v130, v131
v_mfma_f32_16x16x32_bf16 v[148:151], a[84:87], a[40:43], v[148:151]
buffer_load_dwordx4 v[236:239], v208, s[36:39], s15 offen offset:0 sc1
v_mfma_f32_16x16x32_bf16 v[144:147], a[88:91], a[44:47], v[144:147]
v_exp_f32 v134, v134
v_mfma_f32_16x16x32_bf16 v[152:155], a[88:91], a[60:63], v[152:155]
ds_read_b128 a[80:83], v219 offset:17408
v_cvt_pk_bf16_f32 v162, v132, v133
v_mfma_f32_16x16x32_bf16 v[156:159], a[92:95], a[60:63], v[156:159]
v_exp_f32 v135, v135
v_mfma_f32_16x16x32_bf16 v[148:151], a[92:95], a[44:47], v[148:151]
ds_read_b128 a[84:87], v219 offset:21504
ds_read_b128 a[88:91], v219 offset:17920
v_mfma_f32_16x16x32_bf16 v[200:203], v[232:235], v[168:171], v[200:203]
buffer_load_dwordx4 v223, s[44:47], s72 offen offset:1024 lds
v_mfma_f32_16x16x32_bf16 v[204:207], v[232:235], v[172:175], v[204:207]
v_exp_f32 v136, v136
v_mfma_f32_16x16x32_bf16 v[96:99], a[96:99], v[172:175], v[96:99]
v_exp_f32 v137, v137
v_mfma_f32_16x16x32_bf16 v[64:67], a[96:99], v[168:171], v[64:67]
ds_read_b128 a[92:95], v219 offset:22016
v_cvt_pk_bf16_f32 v163, v134, v135
v_mfma_f32_16x16x32_bf16 v[68:71], a[100:103], v[168:171], v[68:71]
v_exp_f32 v138, v138
v_mfma_f32_16x16x32_bf16 v[100:103], a[100:103], v[172:175], v[100:103]
buffer_load_dwordx4 v[240:243], v208, s[36:39], s15 offen offset:1024 sc1
v_mfma_f32_16x16x32_bf16 v[104:107], a[104:107], v[172:175], v[104:107]
v_exp_f32 v139, v139
v_mfma_f32_16x16x32_bf16 v[72:75], a[104:107], v[168:171], v[72:75]
ds_read_b64_tr_b16 a[96:97], v220 offset:24576
ds_read_b64_tr_b16 a[98:99], v220 offset:28672
v_mfma_f32_16x16x32_bf16 v[76:79], a[108:111], v[168:171], v[76:79]
ds_read_b64_tr_b16 a[100:101], v221 offset:24576
v_cvt_pk_bf16_f32 v164, v136, v137
v_mfma_f32_16x16x32_bf16 v[108:111], a[108:111], v[172:175], v[108:111]
v_exp_f32 v140, v140
v_mfma_f32_16x16x32_bf16 v[112:115], a[112:115], v[172:175], v[112:115]
buffer_load_dwordx4 v224, s[44:47], s72 offen offset:2048 lds
v_mfma_f32_16x16x32_bf16 v[80:83], a[112:115], v[168:171], v[80:83]
ds_read_b64_tr_b16 a[102:103], v221 offset:28672
v_cvt_pk_bf16_f32 v165, v138, v139
v_mfma_f32_16x16x32_bf16 v[84:87], a[116:119], v[168:171], v[84:87]
v_exp_f32 v141, v141
v_mfma_f32_16x16x32_bf16 v[116:119], a[116:119], v[172:175], v[116:119]
v_exp_f32 v142, v142
v_mfma_f32_16x16x32_bf16 v[120:123], a[120:123], v[172:175], v[120:123]
buffer_load_dwordx4 v[244:247], v208, s[36:39], s15 offen offset:2048 sc1
v_mfma_f32_16x16x32_bf16 v[88:91], a[120:123], v[168:171], v[88:91]
ds_read_b64_tr_b16 a[104:105], v220 offset:25088
ds_read_b64_tr_b16 a[106:107], v220 offset:29184
v_mfma_f32_16x16x32_bf16 v[92:95], a[124:127], v[168:171], v[92:95]
v_exp_f32 v143, v143
v_mfma_f32_16x16x32_bf16 v[124:127], a[124:127], v[172:175], v[124:127]
v_cvt_pk_bf16_f32 v166, v140, v141
v_cvt_pk_bf16_f32 v167, v142, v143
s_waitcnt lgkmcnt(10)
v_mfma_f32_16x16x32_bf16 v[128:131], a[64:67], a[0:3], v[176:179]
v_exp_f32 v144, v144
v_mfma_f32_16x16x32_bf16 v[136:139], a[64:67], a[16:19], v[180:183]
v_exp_f32 v145, v145
v_mfma_f32_16x16x32_bf16 v[140:143], a[68:71], a[16:19], v[180:183]
ds_read_b64_tr_b16 a[108:109], v221 offset:25088
ds_read_b64_tr_b16 a[110:111], v221 offset:29184
v_mfma_f32_16x16x32_bf16 v[132:135], a[68:71], a[0:3], v[176:179]
v_exp_f32 v146, v146
v_mfma_f32_16x16x32_bf16 v[128:131], a[72:75], a[4:7], v[128:131]
buffer_load_dwordx4 v225, s[44:47], s72 offen offset:3072 lds
v_mfma_f32_16x16x32_bf16 v[136:139], a[72:75], a[20:23], v[136:139]
s_add_u32 s72, s72, 8192
s_add_u32 m0, s71, 0
v_mfma_f32_16x16x32_bf16 v[140:143], a[76:79], a[20:23], v[140:143]
v_exp_f32 v147, v147
v_mfma_f32_16x16x32_bf16 v[132:135], a[76:79], a[4:7], v[132:135]
s_waitcnt lgkmcnt(8)
v_cvt_pk_bf16_f32 v168, v144, v145
v_mfma_f32_16x16x32_bf16 v[128:131], a[80:83], a[8:11], v[128:131]
ds_read_b64_tr_b16 a[112:113], v220 offset:25600
ds_read_b64_tr_b16 a[114:115], v220 offset:29696
v_mfma_f32_16x16x32_bf16 v[136:139], a[80:83], a[24:27], v[136:139]
v_exp_f32 v148, v148
v_mfma_f32_16x16x32_bf16 v[140:143], a[84:87], a[24:27], v[140:143]
buffer_load_dwordx4 v222, s[44:47], s72 offen offset:0 lds
v_mfma_f32_16x16x32_bf16 v[132:135], a[84:87], a[8:11], v[132:135]
v_exp_f32 v149, v149
v_mfma_f32_16x16x32_bf16 v[128:131], a[88:91], a[12:15], v[128:131]
ds_read_b64_tr_b16 a[116:117], v221 offset:25600
ds_read_b64_tr_b16 a[118:119], v221 offset:29696
v_mfma_f32_16x16x32_bf16 v[136:139], a[88:91], a[28:31], v[136:139]
ds_read_b64_tr_b16 a[120:121], v220 offset:26112
v_cvt_pk_bf16_f32 v169, v146, v147
v_mfma_f32_16x16x32_bf16 v[140:143], a[92:95], a[28:31], v[140:143]
v_exp_f32 v150, v150
v_mfma_f32_16x16x32_bf16 v[132:135], a[92:95], a[12:15], v[132:135]
buffer_load_dwordx4 v223, s[44:47], s72 offen offset:1024 lds
v_mfma_f32_16x16x32_bf16 v[192:195], v[232:235], v[160:163], v[192:195]
ds_read_b64_tr_b16 a[122:123], v220 offset:30208
v_cvt_pk_bf16_f32 v170, v148, v149
v_mfma_f32_16x16x32_bf16 v[196:199], v[232:235], v[164:167], v[196:199]
s_waitcnt lgkmcnt(6)
ds_read_b64_tr_b16 a[124:125], v221 offset:26112
v_mfma_f32_16x16x32_bf16 v[32:35], a[96:99], v[164:167], v[32:35]
v_exp_f32 v151, v151
v_mfma_f32_16x16x32_bf16 v[0:3], a[96:99], v[160:163], v[0:3]
v_exp_f32 v152, v152
v_mfma_f32_16x16x32_bf16 v[4:7], a[100:103], v[160:163], v[4:7]
ds_read_b64_tr_b16 a[126:127], v221 offset:30208
v_cvt_pk_bf16_f32 v171, v150, v151
v_mfma_f32_16x16x32_bf16 v[36:39], a[100:103], v[164:167], v[36:39]
buffer_load_dwordx4 v224, s[44:47], s72 offen offset:2048 lds
v_mfma_f32_16x16x32_bf16 v[40:43], a[104:107], v[164:167], v[40:43]
v_exp_f32 v153, v153
v_mfma_f32_16x16x32_bf16 v[8:11], a[104:107], v[160:163], v[8:11]
v_exp_f32 v154, v154
v_mfma_f32_16x16x32_bf16 v[12:15], a[108:111], v[160:163], v[12:15]
v_exp_f32 v155, v155
v_mfma_f32_16x16x32_bf16 v[44:47], a[108:111], v[164:167], v[44:47]
s_waitcnt lgkmcnt(0)
v_cvt_pk_bf16_f32 v172, v152, v153
v_mfma_f32_16x16x32_bf16 v[48:51], a[112:115], v[164:167], v[48:51]
buffer_load_dwordx4 v225, s[44:47], s72 offen offset:3072 lds
s_add_u32 s72, s72, 8192
v_mfma_f32_16x16x32_bf16 v[16:19], a[112:115], v[160:163], v[16:19]
v_exp_f32 v156, v156
v_mfma_f32_16x16x32_bf16 v[20:23], a[116:119], v[160:163], v[20:23]
s_or_b32 s76, s65, s64
s_cselect_b32 s76, 47923200, 0
v_mfma_f32_16x16x32_bf16 v[52:55], a[116:119], v[164:167], v[52:55]
s_add_u32 s76, s76, 16777216
v_cvt_pk_bf16_f32 v173, v154, v155
v_mfma_f32_16x16x32_bf16 v[56:59], a[120:123], v[164:167], v[56:59]
v_exp_f32 v157, v157
v_mfma_f32_16x16x32_bf16 v[24:27], a[120:123], v[160:163], v[24:27]
buffer_store_dwordx2 v[210:211], v212, s[36:39], s76 offen sc1
v_mfma_f32_16x16x32_bf16 v[28:31], a[124:127], v[160:163], v[28:31]
v_exp_f32 v158, v158
v_mfma_f32_16x16x32_bf16 v[60:63], a[124:127], v[164:167], v[60:63]
v_exp_f32 v159, v159
v_cvt_pk_bf16_f32 v174, v156, v157
v_cvt_pk_bf16_f32 v175, v158, v159
v_mfma_f32_16x16x32_bf16 v[144:147], a[64:67], a[32:35], v[184:187]
v_exp_f32 v128, v128
v_mfma_f32_16x16x32_bf16 v[152:155], a[64:67], a[48:51], v[188:191]
v_exp_f32 v129, v129
v_mfma_f32_16x16x32_bf16 v[156:159], a[68:71], a[48:51], v[188:191]
v_exp_f32 v130, v130
v_mfma_f32_16x16x32_bf16 v[148:151], a[68:71], a[32:35], v[184:187]
v_exp_f32 v131, v131
v_mfma_f32_16x16x32_bf16 v[144:147], a[72:75], a[36:39], v[144:147]
ds_read_b128 a[64:67], v219 offset:32768
v_cvt_pk_bf16_f32 v160, v128, v129
v_mfma_f32_16x16x32_bf16 v[152:155], a[72:75], a[52:55], v[152:155]
buffer_load_dwordx4 v[248:251], v208, s[36:39], s15 offen offset:3072 sc1
v_mfma_f32_16x16x32_bf16 v[156:159], a[76:79], a[52:55], v[156:159]
s_add_u32 s15, s85, 20480
ds_read_b128 a[68:71], v219 offset:36864
v_mfma_f32_16x16x32_bf16 v[148:151], a[76:79], a[36:39], v[148:151]
v_exp_f32 v132, v132
v_mfma_f32_16x16x32_bf16 v[144:147], a[80:83], a[40:43], v[144:147]
ds_read_b128 a[72:75], v219 offset:33280
v_cvt_pk_bf16_f32 v161, v130, v131
v_mfma_f32_16x16x32_bf16 v[152:155], a[80:83], a[56:59], v[152:155]
v_exp_f32 v133, v133
v_mfma_f32_16x16x32_bf16 v[156:159], a[84:87], a[56:59], v[156:159]
buffer_load_dwordx4 v[252:255], v208, s[36:39], s15 offen offset:0 sc1
v_mfma_f32_16x16x32_bf16 v[148:151], a[84:87], a[40:43], v[148:151]
ds_read_b128 a[76:79], v219 offset:37376
ds_read_b128 a[80:83], v219 offset:33792
v_mfma_f32_16x16x32_bf16 v[144:147], a[88:91], a[44:47], v[144:147]
v_exp_f32 v134, v134
v_mfma_f32_16x16x32_bf16 v[152:155], a[88:91], a[60:63], v[152:155]
v_exp_f32 v135, v135
v_mfma_f32_16x16x32_bf16 v[156:159], a[92:95], a[60:63], v[156:159]
ds_read_b128 a[84:87], v219 offset:37888
v_cvt_pk_bf16_f32 v162, v132, v133
v_mfma_f32_16x16x32_bf16 v[148:151], a[92:95], a[44:47], v[148:151]
v_exp_f32 v136, v136
v_mfma_f32_16x16x32_bf16 v[200:203], v[232:235], v[168:171], v[200:203]
buffer_load_dwordx4 a[192:195], v208, s[36:39], s15 offen offset:1024 sc1
v_mfma_f32_16x16x32_bf16 v[204:207], v[232:235], v[172:175], v[204:207]
ds_read_b128 a[88:91], v219 offset:34304
ds_read_b128 a[92:95], v219 offset:38400
v_mfma_f32_16x16x32_bf16 v[96:99], a[96:99], v[172:175], v[96:99]
v_exp_f32 v137, v137
v_mfma_f32_16x16x32_bf16 v[64:67], a[96:99], v[168:171], v[64:67]
ds_read_b64_tr_b16 a[96:97], v220 offset:40960
v_cvt_pk_bf16_f32 v163, v134, v135
v_mfma_f32_16x16x32_bf16 v[68:71], a[100:103], v[168:171], v[68:71]
v_exp_f32 v138, v138
v_mfma_f32_16x16x32_bf16 v[100:103], a[100:103], v[172:175], v[100:103]
buffer_load_dwordx4 a[196:199], v208, s[36:39], s15 offen offset:2048 sc1
v_mfma_f32_16x16x32_bf16 v[104:107], a[104:107], v[172:175], v[104:107]
v_exp_f32 v139, v139
v_mfma_f32_16x16x32_bf16 v[72:75], a[104:107], v[168:171], v[72:75]
ds_read_b64_tr_b16 a[98:99], v220 offset:45056
v_cvt_pk_bf16_f32 v164, v136, v137
v_mfma_f32_16x16x32_bf16 v[76:79], a[108:111], v[168:171], v[76:79]
s_mov_b32 m0, s79
s_add_u32 s79, s79, 3072
v_mfma_f32_16x16x32_bf16 v[108:111], a[108:111], v[172:175], v[108:111]
v_exp_f32 v140, v140
v_mfma_f32_16x16x32_bf16 v[112:115], a[112:115], v[172:175], v[112:115]
ds_read_b64_tr_b16 a[100:101], v221 offset:40960
ds_read_b64_tr_b16 a[102:103], v221 offset:45056
v_mfma_f32_16x16x32_bf16 v[80:83], a[112:115], v[168:171], v[80:83]
v_exp_f32 v141, v141
v_mfma_f32_16x16x32_bf16 v[84:87], a[116:119], v[168:171], v[84:87]
ds_read_b64_tr_b16 a[104:105], v220 offset:41472
v_cvt_pk_bf16_f32 v165, v138, v139
v_mfma_f32_16x16x32_bf16 v[116:119], a[116:119], v[172:175], v[116:119]
buffer_load_dwordx4 v208, s[36:39], s78 offen offset:0 sc1 lds
v_mfma_f32_16x16x32_bf16 v[120:123], a[120:123], v[172:175], v[120:123]
v_exp_f32 v142, v142
v_mfma_f32_16x16x32_bf16 v[88:91], a[120:123], v[168:171], v[88:91]
ds_read_b64_tr_b16 a[106:107], v220 offset:45568
v_cvt_pk_bf16_f32 v166, v140, v141
v_mfma_f32_16x16x32_bf16 v[92:95], a[124:127], v[168:171], v[92:95]
v_exp_f32 v143, v143
v_mfma_f32_16x16x32_bf16 v[124:127], a[124:127], v[172:175], v[124:127]
v_cvt_pk_bf16_f32 v167, v142, v143
s_waitcnt lgkmcnt(10)
v_mfma_f32_16x16x32_bf16 v[128:131], a[64:67], a[0:3], v[176:179]
v_exp_f32 v144, v144
v_mfma_f32_16x16x32_bf16 v[136:139], a[64:67], a[16:19], v[180:183]
v_exp_f32 v145, v145
v_mfma_f32_16x16x32_bf16 v[140:143], a[68:71], a[16:19], v[180:183]
buffer_load_dwordx4 v208, s[36:39], s78 offen offset:1024 sc1 lds
v_mfma_f32_16x16x32_bf16 v[132:135], a[68:71], a[0:3], v[176:179]
ds_read_b64_tr_b16 a[108:109], v221 offset:41472
ds_read_b64_tr_b16 a[110:111], v221 offset:45568
v_mfma_f32_16x16x32_bf16 v[128:131], a[72:75], a[4:7], v[128:131]
v_exp_f32 v146, v146
v_mfma_f32_16x16x32_bf16 v[136:139], a[72:75], a[20:23], v[136:139]
v_exp_f32 v147, v147
v_mfma_f32_16x16x32_bf16 v[140:143], a[76:79], a[20:23], v[140:143]
ds_read_b64_tr_b16 a[112:113], v220 offset:41984
v_cvt_pk_bf16_f32 v168, v144, v145
v_mfma_f32_16x16x32_bf16 v[132:135], a[76:79], a[4:7], v[132:135]
buffer_load_dwordx4 v208, s[36:39], s78 offen offset:2048 sc1 lds
s_add_u32 s78, s78, 3072
s_waitcnt lgkmcnt(9)
v_mfma_f32_16x16x32_bf16 v[128:131], a[80:83], a[8:11], v[128:131]
ds_read_b64_tr_b16 a[114:115], v220 offset:46080
s_mov_b32 m0, s79
v_mfma_f32_16x16x32_bf16 v[136:139], a[80:83], a[24:27], v[136:139]
v_exp_f32 v148, v148
v_mfma_f32_16x16x32_bf16 v[140:143], a[84:87], a[24:27], v[140:143]
s_add_u32 s79, s79, 3072
ds_read_b64_tr_b16 a[116:117], v221 offset:41984
v_mfma_f32_16x16x32_bf16 v[132:135], a[84:87], a[8:11], v[132:135]
v_exp_f32 v149, v149
v_mfma_f32_16x16x32_bf16 v[128:131], a[88:91], a[12:15], v[128:131]
ds_read_b64_tr_b16 a[118:119], v221 offset:46080
v_cvt_pk_bf16_f32 v169, v146, v147
v_mfma_f32_16x16x32_bf16 v[136:139], a[88:91], a[28:31], v[136:139]
buffer_load_dwordx4 v208, s[36:39], s78 offen offset:0 sc1 lds
v_mfma_f32_16x16x32_bf16 v[140:143], a[92:95], a[28:31], v[140:143]
v_exp_f32 v150, v150
v_mfma_f32_16x16x32_bf16 v[132:135], a[92:95], a[12:15], v[132:135]
ds_read_b64_tr_b16 a[120:121], v220 offset:42496
ds_read_b64_tr_b16 a[122:123], v220 offset:46592
v_mfma_f32_16x16x32_bf16 v[192:195], v[232:235], v[160:163], v[192:195]
v_exp_f32 v151, v151
v_mfma_f32_16x16x32_bf16 v[196:199], v[232:235], v[164:167], v[196:199]
s_waitcnt lgkmcnt(6)
v_cvt_pk_bf16_f32 v170, v148, v149
v_mfma_f32_16x16x32_bf16 v[32:35], a[96:99], v[164:167], v[32:35]
v_exp_f32 v152, v152
v_mfma_f32_16x16x32_bf16 v[0:3], a[96:99], v[160:163], v[0:3]
buffer_load_dwordx4 v208, s[36:39], s78 offen offset:1024 sc1 lds
v_mfma_f32_16x16x32_bf16 v[4:7], a[100:103], v[160:163], v[4:7]
ds_read_b64_tr_b16 a[124:125], v221 offset:42496
ds_read_b64_tr_b16 a[126:127], v221 offset:46592
v_mfma_f32_16x16x32_bf16 v[36:39], a[100:103], v[164:167], v[36:39]
v_exp_f32 v153, v153
v_mfma_f32_16x16x32_bf16 v[40:43], a[104:107], v[164:167], v[40:43]
v_cvt_pk_bf16_f32 v171, v150, v151
v_mfma_f32_16x16x32_bf16 v[8:11], a[104:107], v[160:163], v[8:11]
v_exp_f32 v154, v154
v_mfma_f32_16x16x32_bf16 v[12:15], a[108:111], v[160:163], v[12:15]
v_exp_f32 v155, v155
v_mfma_f32_16x16x32_bf16 v[44:47], a[108:111], v[164:167], v[44:47]
buffer_load_dwordx4 v208, s[36:39], s78 offen offset:2048 sc1 lds
s_add_u32 s78, s78, 3072
s_waitcnt lgkmcnt(0)
v_mfma_f32_16x16x32_bf16 v[48:51], a[112:115], v[164:167], v[48:51]
v_cvt_pk_bf16_f32 v172, v152, v153
s_lshr_b32 s77, s65, 3
v_mfma_f32_16x16x32_bf16 v[16:19], a[112:115], v[160:163], v[16:19]
v_exp_f32 v156, v156
v_mfma_f32_16x16x32_bf16 v[20:23], a[116:119], v[160:163], v[20:23]
s_mul_i32 s77, s77, 1024
s_add_u32 s77, s77, .Lk_fbret-.Lk_start
v_mfma_f32_16x16x32_bf16 v[52:55], a[116:119], v[164:167], v[52:55]
v_and_b32 v218, 7, v209
v_lshlrev_b32 v218, 7, v218
v_mfma_f32_16x16x32_bf16 v[56:59], a[120:123], v[164:167], v[56:59]
v_exp_f32 v157, v157
v_mfma_f32_16x16x32_bf16 v[24:27], a[120:123], v[160:163], v[24:27]
buffer_load_dword v218, v218, s[60:63], s77 offen
v_mfma_f32_16x16x32_bf16 v[28:31], a[124:127], v[160:163], v[28:31]
v_cvt_pk_bf16_f32 v173, v154, v155
s_add_u32 m0, s71, 16384
v_mfma_f32_16x16x32_bf16 v[60:63], a[124:127], v[164:167], v[60:63]
v_exp_f32 v158, v158
v_exp_f32 v159, v159
v_cvt_pk_bf16_f32 v174, v156, v157
v_cvt_pk_bf16_f32 v175, v158, v159
s_waitcnt vmcnt(29)
s_waitcnt vmcnt(12)
s_barrier
v_mfma_f32_16x16x32_bf16 v[144:147], a[64:67], a[32:35], v[184:187]
v_exp_f32 v128, v128
v_mfma_f32_16x16x32_bf16 v[152:155], a[64:67], a[48:51], v[188:191]
v_exp_f32 v129, v129
v_mfma_f32_16x16x32_bf16 v[156:159], a[68:71], a[48:51], v[188:191]
v_exp_f32 v130, v130
v_mfma_f32_16x16x32_bf16 v[148:151], a[68:71], a[32:35], v[184:187]
v_exp_f32 v131, v131
v_mfma_f32_16x16x32_bf16 v[144:147], a[72:75], a[36:39], v[144:147]
ds_read_b128 a[64:67], v219 offset:49152
v_cvt_pk_bf16_f32 v160, v128, v129
v_mfma_f32_16x16x32_bf16 v[152:155], a[72:75], a[52:55], v[152:155]
buffer_load_dwordx4 v222, s[44:47], s72 offen offset:0 lds
v_mfma_f32_16x16x32_bf16 v[156:159], a[76:79], a[52:55], v[156:159]
ds_read_b128 a[68:71], v219 offset:53248
ds_read_b128 a[72:75], v219 offset:49664
v_mfma_f32_16x16x32_bf16 v[148:151], a[76:79], a[36:39], v[148:151]
v_exp_f32 v132, v132
v_mfma_f32_16x16x32_bf16 v[144:147], a[80:83], a[40:43], v[144:147]
v_exp_f32 v133, v133
v_mfma_f32_16x16x32_bf16 v[152:155], a[80:83], a[56:59], v[152:155]
ds_read_b128 a[76:79], v219 offset:53760
v_cvt_pk_bf16_f32 v161, v130, v131
v_mfma_f32_16x16x32_bf16 v[156:159], a[84:87], a[56:59], v[156:159]
buffer_load_dwordx4 a[200:203], v208, s[36:39], s15 offen offset:3072 sc1
v_mfma_f32_16x16x32_bf16 v[148:151], a[84:87], a[40:43], v[148:151]
v_exp_f32 v134, v134
v_mfma_f32_16x16x32_bf16 v[144:147], a[88:91], a[44:47], v[144:147]
ds_read_b128 a[80:83], v219 offset:50176
v_cvt_pk_bf16_f32 v162, v132, v133
v_mfma_f32_16x16x32_bf16 v[152:155], a[88:91], a[60:63], v[152:155]
v_exp_f32 v135, v135
v_mfma_f32_16x16x32_bf16 v[156:159], a[92:95], a[60:63], v[156:159]
ds_read_b128 a[84:87], v219 offset:54272
ds_read_b128 a[88:91], v219 offset:50688
v_mfma_f32_16x16x32_bf16 v[148:151], a[92:95], a[44:47], v[148:151]
buffer_load_dwordx4 v223, s[44:47], s72 offen offset:1024 lds
v_mfma_f32_16x16x32_bf16 v[200:203], v[232:235], v[168:171], v[200:203]
v_exp_f32 v136, v136
v_mfma_f32_16x16x32_bf16 v[204:207], v[232:235], v[172:175], v[204:207]
s_add_u32 s15, s85, 24576
ds_read_b128 a[92:95], v219 offset:54784
v_mfma_f32_16x16x32_bf16 v[96:99], a[96:99], v[172:175], v[96:99]
v_exp_f32 v137, v137
v_mfma_f32_16x16x32_bf16 v[64:67], a[96:99], v[168:171], v[64:67]
ds_read_b64_tr_b16 a[96:97], v220 offset:57344
v_cvt_pk_bf16_f32 v163, v134, v135
v_mfma_f32_16x16x32_bf16 v[68:71], a[100:103], v[168:171], v[68:71]
v_exp_f32 v138, v138
v_mfma_f32_16x16x32_bf16 v[100:103], a[100:103], v[172:175], v[100:103]
buffer_load_dwordx4 a[204:207], v208, s[36:39], s15 offen offset:0 sc1
v_mfma_f32_16x16x32_bf16 v[104:107], a[104:107], v[172:175], v[104:107]
v_exp_f32 v139, v139
v_mfma_f32_16x16x32_bf16 v[72:75], a[104:107], v[168:171], v[72:75]
ds_read_b64_tr_b16 a[98:99], v220 offset:61440
v_cvt_pk_bf16_f32 v164, v136, v137
v_mfma_f32_16x16x32_bf16 v[76:79], a[108:111], v[168:171], v[76:79]
v_exp_f32 v140, v140
v_mfma_f32_16x16x32_bf16 v[108:111], a[108:111], v[172:175], v[108:111]
buffer_load_dwordx4 v224, s[44:47], s72 offen offset:2048 lds
v_mfma_f32_16x16x32_bf16 v[112:115], a[112:115], v[172:175], v[112:115]
ds_read_b64_tr_b16 a[100:101], v221 offset:57344
ds_read_b64_tr_b16 a[102:103], v221 offset:61440
v_mfma_f32_16x16x32_bf16 v[80:83], a[112:115], v[168:171], v[80:83]
v_exp_f32 v141, v141
v_mfma_f32_16x16x32_bf16 v[84:87], a[116:119], v[168:171], v[84:87]
ds_read_b64_tr_b16 a[104:105], v220 offset:57856
v_cvt_pk_bf16_f32 v165, v138, v139
v_mfma_f32_16x16x32_bf16 v[116:119], a[116:119], v[172:175], v[116:119]
v_exp_f32 v142, v142
v_mfma_f32_16x16x32_bf16 v[120:123], a[120:123], v[172:175], v[120:123]
buffer_load_dwordx4 a[208:211], v208, s[36:39], s15 offen offset:1024 sc1
v_mfma_f32_16x16x32_bf16 v[88:91], a[120:123], v[168:171], v[88:91]
ds_read_b64_tr_b16 a[106:107], v220 offset:61952
v_cvt_pk_bf16_f32 v166, v140, v141
v_mfma_f32_16x16x32_bf16 v[92:95], a[124:127], v[168:171], v[92:95]
v_exp_f32 v143, v143
v_mfma_f32_16x16x32_bf16 v[124:127], a[124:127], v[172:175], v[124:127]
v_cvt_pk_bf16_f32 v167, v142, v143
s_waitcnt lgkmcnt(10)
v_mfma_f32_16x16x32_bf16 v[128:131], a[64:67], a[0:3], v[176:179]
v_exp_f32 v144, v144
v_mfma_f32_16x16x32_bf16 v[136:139], a[64:67], a[16:19], v[180:183]
v_exp_f32 v145, v145
v_mfma_f32_16x16x32_bf16 v[140:143], a[68:71], a[16:19], v[180:183]
ds_read_b64_tr_b16 a[108:109], v221 offset:57856
ds_read_b64_tr_b16 a[110:111], v221 offset:61952
v_mfma_f32_16x16x32_bf16 v[132:135], a[68:71], a[0:3], v[176:179]
v_exp_f32 v146, v146
v_mfma_f32_16x16x32_bf16 v[128:131], a[72:75], a[4:7], v[128:131]
buffer_load_dwordx4 v225, s[44:47], s72 offen offset:3072 lds
v_mfma_f32_16x16x32_bf16 v[136:139], a[72:75], a[20:23], v[136:139]
s_add_u32 s72, s72, 8192
s_add_u32 m0, s71, 32768
v_mfma_f32_16x16x32_bf16 v[140:143], a[76:79], a[20:23], v[140:143]
v_exp_f32 v147, v147
v_mfma_f32_16x16x32_bf16 v[132:135], a[76:79], a[4:7], v[132:135]
s_waitcnt lgkmcnt(8)
v_cvt_pk_bf16_f32 v168, v144, v145
v_mfma_f32_16x16x32_bf16 v[128:131], a[80:83], a[8:11], v[128:131]
ds_read_b64_tr_b16 a[112:113], v220 offset:58368
ds_read_b64_tr_b16 a[114:115], v220 offset:62464
v_mfma_f32_16x16x32_bf16 v[136:139], a[80:83], a[24:27], v[136:139]
v_exp_f32 v148, v148
v_mfma_f32_16x16x32_bf16 v[140:143], a[84:87], a[24:27], v[140:143]
buffer_load_dwordx4 v222, s[44:47], s72 offen offset:0 lds
v_mfma_f32_16x16x32_bf16 v[132:135], a[84:87], a[8:11], v[132:135]
v_exp_f32 v149, v149
v_mfma_f32_16x16x32_bf16 v[128:131], a[88:91], a[12:15], v[128:131]
ds_read_b64_tr_b16 a[116:117], v221 offset:58368
ds_read_b64_tr_b16 a[118:119], v221 offset:62464
v_mfma_f32_16x16x32_bf16 v[136:139], a[88:91], a[28:31], v[136:139]
ds_read_b64_tr_b16 a[120:121], v220 offset:58880
v_cvt_pk_bf16_f32 v169, v146, v147
v_mfma_f32_16x16x32_bf16 v[140:143], a[92:95], a[28:31], v[140:143]
v_exp_f32 v150, v150
v_mfma_f32_16x16x32_bf16 v[132:135], a[92:95], a[12:15], v[132:135]
buffer_load_dwordx4 v223, s[44:47], s72 offen offset:1024 lds
v_mfma_f32_16x16x32_bf16 v[192:195], v[232:235], v[160:163], v[192:195]
ds_read_b64_tr_b16 a[122:123], v220 offset:62976
v_cvt_pk_bf16_f32 v170, v148, v149
v_mfma_f32_16x16x32_bf16 v[196:199], v[232:235], v[164:167], v[196:199]
s_waitcnt lgkmcnt(6)
ds_read_b64_tr_b16 a[124:125], v221 offset:58880
v_mfma_f32_16x16x32_bf16 v[32:35], a[96:99], v[164:167], v[32:35]
v_exp_f32 v151, v151
v_mfma_f32_16x16x32_bf16 v[0:3], a[96:99], v[160:163], v[0:3]
v_exp_f32 v152, v152
v_mfma_f32_16x16x32_bf16 v[4:7], a[100:103], v[160:163], v[4:7]
ds_read_b64_tr_b16 a[126:127], v221 offset:62976
v_cvt_pk_bf16_f32 v171, v150, v151
v_mfma_f32_16x16x32_bf16 v[36:39], a[100:103], v[164:167], v[36:39]
buffer_load_dwordx4 v224, s[44:47], s72 offen offset:2048 lds
v_mfma_f32_16x16x32_bf16 v[40:43], a[104:107], v[164:167], v[40:43]
v_exp_f32 v153, v153
v_mfma_f32_16x16x32_bf16 v[8:11], a[104:107], v[160:163], v[8:11]
v_exp_f32 v154, v154
v_mfma_f32_16x16x32_bf16 v[12:15], a[108:111], v[160:163], v[12:15]
v_exp_f32 v155, v155
v_mfma_f32_16x16x32_bf16 v[44:47], a[108:111], v[164:167], v[44:47]
s_waitcnt lgkmcnt(0)
v_cvt_pk_bf16_f32 v172, v152, v153
v_mfma_f32_16x16x32_bf16 v[48:51], a[112:115], v[164:167], v[48:51]
buffer_load_dwordx4 v225, s[44:47], s72 offen offset:3072 lds
s_add_u32 s72, s72, 8192
v_mfma_f32_16x16x32_bf16 v[16:19], a[112:115], v[160:163], v[16:19]
v_exp_f32 v156, v156
v_mfma_f32_16x16x32_bf16 v[20:23], a[116:119], v[160:163], v[20:23]
s_lshl_b32 s76, s65, 3
s_add_u32 s76, s76, 16785408
v_mfma_f32_16x16x32_bf16 v[52:55], a[116:119], v[164:167], v[52:55]
s_cmp_eq_u32 s64, 0
s_cselect_b32 s76, s76, 47923200
v_mfma_f32_16x16x32_bf16 v[56:59], a[120:123], v[164:167], v[56:59]
v_exp_f32 v157, v157
v_mfma_f32_16x16x32_bf16 v[24:27], a[120:123], v[160:163], v[24:27]
buffer_store_dwordx2 v[210:211], v212, s[36:39], s76 offen sc1
v_mfma_f32_16x16x32_bf16 v[28:31], a[124:127], v[160:163], v[28:31]
v_cvt_pk_bf16_f32 v173, v154, v155
v_mfma_f32_16x16x32_bf16 v[60:63], a[124:127], v[164:167], v[60:63]
v_exp_f32 v158, v158
v_exp_f32 v159, v159
v_cvt_pk_bf16_f32 v174, v156, v157
v_cvt_pk_bf16_f32 v175, v158, v159
v_mfma_f32_16x16x32_bf16 v[144:147], a[64:67], a[32:35], v[184:187]
v_exp_f32 v128, v128
v_mfma_f32_16x16x32_bf16 v[152:155], a[64:67], a[48:51], v[188:191]
v_exp_f32 v129, v129
v_mfma_f32_16x16x32_bf16 v[156:159], a[68:71], a[48:51], v[188:191]
v_exp_f32 v130, v130
v_mfma_f32_16x16x32_bf16 v[148:151], a[68:71], a[32:35], v[184:187]
v_exp_f32 v131, v131
v_mfma_f32_16x16x32_bf16 v[144:147], a[72:75], a[36:39], v[144:147]
ds_read_b128 a[64:67], v219 offset:0
v_cvt_pk_bf16_f32 v160, v128, v129
v_mfma_f32_16x16x32_bf16 v[152:155], a[72:75], a[52:55], v[152:155]
buffer_load_dwordx4 a[212:215], v208, s[36:39], s15 offen offset:2048 sc1
v_mfma_f32_16x16x32_bf16 v[156:159], a[76:79], a[52:55], v[156:159]
ds_read_b128 a[68:71], v219 offset:4096
ds_read_b128 a[72:75], v219 offset:512
v_mfma_f32_16x16x32_bf16 v[148:151], a[76:79], a[36:39], v[148:151]
v_exp_f32 v132, v132
v_mfma_f32_16x16x32_bf16 v[144:147], a[80:83], a[40:43], v[144:147]
v_exp_f32 v133, v133
v_mfma_f32_16x16x32_bf16 v[152:155], a[80:83], a[56:59], v[152:155]
ds_read_b128 a[76:79], v219 offset:4608
v_cvt_pk_bf16_f32 v161, v130, v131
v_mfma_f32_16x16x32_bf16 v[156:159], a[84:87], a[56:59], v[156:159]
v_exp_f32 v134, v134
v_mfma_f32_16x16x32_bf16 v[148:151], a[84:87], a[40:43], v[148:151]
buffer_load_dwordx4 a[216:219], v208, s[36:39], s15 offen offset:3072 sc1
v_mfma_f32_16x16x32_bf16 v[144:147], a[88:91], a[44:47], v[144:147]
ds_read_b128 a[80:83], v219 offset:1024
s_add_u32 s15, s85, 28672
v_mfma_f32_16x16x32_bf16 v[152:155], a[88:91], a[60:63], v[152:155]
v_exp_f32 v135, v135
v_mfma_f32_16x16x32_bf16 v[156:159], a[92:95], a[60:63], v[156:159]
ds_read_b128 a[84:87], v219 offset:5120
v_cvt_pk_bf16_f32 v162, v132, v133
v_mfma_f32_16x16x32_bf16 v[148:151], a[92:95], a[44:47], v[148:151]
v_exp_f32 v136, v136
v_mfma_f32_16x16x32_bf16 v[200:203], v[232:235], v[168:171], v[200:203]
ds_read_b128 a[88:91], v219 offset:1536
ds_read_b128 a[92:95], v219 offset:5632
v_mfma_f32_16x16x32_bf16 v[204:207], v[232:235], v[172:175], v[204:207]
buffer_load_dwordx4 a[220:223], v208, s[36:39], s15 offen offset:0 sc1
v_mfma_f32_16x16x32_bf16 v[96:99], a[96:99], v[172:175], v[96:99]
v_exp_f32 v137, v137
v_mfma_f32_16x16x32_bf16 v[64:67], a[96:99], v[168:171], v[64:67]
ds_read_b64_tr_b16 a[96:97], v220 offset:8192
v_cvt_pk_bf16_f32 v163, v134, v135
v_mfma_f32_16x16x32_bf16 v[68:71], a[100:103], v[168:171], v[68:71]
v_exp_f32 v138, v138
v_mfma_f32_16x16x32_bf16 v[100:103], a[100:103], v[172:175], v[100:103]
v_exp_f32 v139, v139
v_mfma_f32_16x16x32_bf16 v[104:107], a[104:107], v[172:175], v[104:107]
ds_read_b64_tr_b16 a[98:99], v220 offset:12288
v_cvt_pk_bf16_f32 v164, v136, v137
v_mfma_f32_16x16x32_bf16 v[72:75], a[104:107], v[168:171], v[72:75]
buffer_load_dwordx4 a[224:227], v208, s[36:39], s15 offen offset:1024 sc1
v_mfma_f32_16x16x32_bf16 v[76:79], a[108:111], v[168:171], v[76:79]
v_exp_f32 v140, v140
v_mfma_f32_16x16x32_bf16 v[108:111], a[108:111], v[172:175], v[108:111]
s_mov_b32 m0, s79
s_add_u32 s79, s79, 3072
v_mfma_f32_16x16x32_bf16 v[112:115], a[112:115], v[172:175], v[112:115]
ds_read_b64_tr_b16 a[100:101], v221 offset:8192
ds_read_b64_tr_b16 a[102:103], v221 offset:12288
v_mfma_f32_16x16x32_bf16 v[80:83], a[112:115], v[168:171], v[80:83]
v_exp_f32 v141, v141
v_mfma_f32_16x16x32_bf16 v[84:87], a[116:119], v[168:171], v[84:87]
ds_read_b64_tr_b16 a[104:105], v220 offset:8704
v_cvt_pk_bf16_f32 v165, v138, v139
v_mfma_f32_16x16x32_bf16 v[116:119], a[116:119], v[172:175], v[116:119]
v_exp_f32 v142, v142
v_mfma_f32_16x16x32_bf16 v[120:123], a[120:123], v[172:175], v[120:123]
buffer_load_dwordx4 v208, s[36:39], s78 offen offset:0 sc1 lds
v_mfma_f32_16x16x32_bf16 v[88:91], a[120:123], v[168:171], v[88:91]
ds_read_b64_tr_b16 a[106:107], v220 offset:12800
v_cvt_pk_bf16_f32 v166, v140, v141
v_mfma_f32_16x16x32_bf16 v[92:95], a[124:127], v[168:171], v[92:95]
v_exp_f32 v143, v143
v_mfma_f32_16x16x32_bf16 v[124:127], a[124:127], v[172:175], v[124:127]
v_cvt_pk_bf16_f32 v167, v142, v143
s_waitcnt lgkmcnt(10)
v_mfma_f32_16x16x32_bf16 v[128:131], a[64:67], a[0:3], v[176:179]
v_exp_f32 v144, v144
v_mfma_f32_16x16x32_bf16 v[136:139], a[64:67], a[16:19], v[180:183]
v_exp_f32 v145, v145
v_mfma_f32_16x16x32_bf16 v[140:143], a[68:71], a[16:19], v[180:183]
ds_read_b64_tr_b16 a[108:109], v221 offset:8704
ds_read_b64_tr_b16 a[110:111], v221 offset:12800
v_mfma_f32_16x16x32_bf16 v[132:135], a[68:71], a[0:3], v[176:179]
v_exp_f32 v146, v146
v_mfma_f32_16x16x32_bf16 v[128:131], a[72:75], a[4:7], v[128:131]
buffer_load_dwordx4 v208, s[36:39], s78 offen offset:1024 sc1 lds
v_mfma_f32_16x16x32_bf16 v[136:139], a[72:75], a[20:23], v[136:139]
v_exp_f32 v147, v147
v_mfma_f32_16x16x32_bf16 v[140:143], a[76:79], a[20:23], v[140:143]
ds_read_b64_tr_b16 a[112:113], v220 offset:9216
v_cvt_pk_bf16_f32 v168, v144, v145
v_mfma_f32_16x16x32_bf16 v[132:135], a[76:79], a[4:7], v[132:135]
s_waitcnt lgkmcnt(9)
ds_read_b64_tr_b16 a[114:115], v220 offset:13312
v_mfma_f32_16x16x32_bf16 v[128:131], a[80:83], a[8:11], v[128:131]
v_exp_f32 v148, v148
v_mfma_f32_16x16x32_bf16 v[136:139], a[80:83], a[24:27], v[136:139]
v_exp_f32 v149, v149
v_mfma_f32_16x16x32_bf16 v[140:143], a[84:87], a[24:27], v[140:143]
buffer_load_dwordx4 v208, s[36:39], s78 offen offset:2048 sc1 lds
v_mfma_f32_16x16x32_bf16 v[132:135], a[84:87], a[8:11], v[132:135]
s_add_u32 s78, s78, 3072
ds_read_b64_tr_b16 a[116:117], v221 offset:9216
v_mfma_f32_16x16x32_bf16 v[128:131], a[88:91], a[12:15], v[128:131]
ds_read_b64_tr_b16 a[118:119], v221 offset:13312
v_cvt_pk_bf16_f32 v169, v146, v147
v_mfma_f32_16x16x32_bf16 v[136:139], a[88:91], a[28:31], v[136:139]
v_exp_f32 v150, v150
v_mfma_f32_16x16x32_bf16 v[140:143], a[92:95], a[28:31], v[140:143]
s_mov_b32 m0, s79
s_add_u32 s79, s79, 3072
v_mfma_f32_16x16x32_bf16 v[132:135], a[92:95], a[12:15], v[132:135]
ds_read_b64_tr_b16 a[120:121], v220 offset:9728
ds_read_b64_tr_b16 a[122:123], v220 offset:13824
v_mfma_f32_16x16x32_bf16 v[192:195], v[232:235], v[160:163], v[192:195]
v_exp_f32 v151, v151
v_mfma_f32_16x16x32_bf16 v[196:199], v[232:235], v[164:167], v[196:199]
buffer_load_dwordx4 v208, s[36:39], s78 offen offset:0 sc1 lds
s_waitcnt lgkmcnt(6)
v_mfma_f32_16x16x32_bf16 v[32:35], a[96:99], v[164:167], v[32:35]
ds_read_b64_tr_b16 a[124:125], v221 offset:9728
v_cvt_pk_bf16_f32 v170, v148, v149
v_mfma_f32_16x16x32_bf16 v[0:3], a[96:99], v[160:163], v[0:3]
v_exp_f32 v152, v152
v_mfma_f32_16x16x32_bf16 v[4:7], a[100:103], v[160:163], v[4:7]
ds_read_b64_tr_b16 a[126:127], v221 offset:13824
v_cvt_pk_bf16_f32 v171, v150, v151
v_mfma_f32_16x16x32_bf16 v[36:39], a[100:103], v[164:167], v[36:39]
v_exp_f32 v153, v153
v_mfma_f32_16x16x32_bf16 v[40:43], a[104:107], v[164:167], v[40:43]
v_exp_f32 v154, v154
v_mfma_f32_16x16x32_bf16 v[8:11], a[104:107], v[160:163], v[8:11]
buffer_load_dwordx4 v208, s[36:39], s78 offen offset:1024 sc1 lds
v_mfma_f32_16x16x32_bf16 v[12:15], a[108:111], v[160:163], v[12:15]
v_exp_f32 v155, v155
v_mfma_f32_16x16x32_bf16 v[44:47], a[108:111], v[164:167], v[44:47]
s_waitcnt lgkmcnt(0)
v_cvt_pk_bf16_f32 v172, v152, v153
v_mfma_f32_16x16x32_bf16 v[48:51], a[112:115], v[164:167], v[48:51]
v_exp_f32 v156, v156
v_mfma_f32_16x16x32_bf16 v[16:19], a[112:115], v[160:163], v[16:19]
v_exp_f32 v157, v157
v_mfma_f32_16x16x32_bf16 v[20:23], a[116:119], v[160:163], v[20:23]
v_cvt_pk_bf16_f32 v173, v154, v155
v_mfma_f32_16x16x32_bf16 v[52:55], a[116:119], v[164:167], v[52:55]
buffer_load_dwordx4 v208, s[36:39], s78 offen offset:2048 sc1 lds
v_mfma_f32_16x16x32_bf16 v[56:59], a[120:123], v[164:167], v[56:59]
s_add_u32 s78, s78, 3072
v_mfma_f32_16x16x32_bf16 v[24:27], a[120:123], v[160:163], v[24:27]
v_exp_f32 v158, v158
v_mfma_f32_16x16x32_bf16 v[28:31], a[124:127], v[160:163], v[28:31]
v_exp_f32 v159, v159
v_mfma_f32_16x16x32_bf16 v[60:63], a[124:127], v[164:167], v[60:63]
v_cvt_pk_bf16_f32 v174, v156, v157
v_cvt_pk_bf16_f32 v175, v158, v159
s_waitcnt vmcnt(15)
s_barrier
v_mfma_f32_16x16x32_bf16 v[144:147], a[64:67], a[32:35], v[184:187]
v_exp_f32 v128, v128
v_mfma_f32_16x16x32_bf16 v[152:155], a[64:67], a[48:51], v[188:191]
v_exp_f32 v129, v129
v_mfma_f32_16x16x32_bf16 v[156:159], a[68:71], a[48:51], v[188:191]
s_add_u32 m0, s71, 49152
ds_read_b128 a[64:67], v219 offset:16384
v_mfma_f32_16x16x32_bf16 v[148:151], a[68:71], a[32:35], v[184:187]
v_exp_f32 v130, v130
v_mfma_f32_16x16x32_bf16 v[144:147], a[72:75], a[36:39], v[144:147]
v_exp_f32 v131, v131
v_mfma_f32_16x16x32_bf16 v[152:155], a[72:75], a[52:55], v[152:155]
ds_read_b128 a[68:71], v219 offset:20480
v_cvt_pk_bf16_f32 v160, v128, v129
v_mfma_f32_16x16x32_bf16 v[156:159], a[76:79], a[52:55], v[156:159]
buffer_load_dwordx4 v222, s[44:47], s72 offen offset:0 lds
v_mfma_f32_16x16x32_bf16 v[148:151], a[76:79], a[36:39], v[148:151]
v_exp_f32 v132, v132
v_mfma_f32_16x16x32_bf16 v[144:147], a[80:83], a[40:43], v[144:147]
ds_read_b128 a[72:75], v219 offset:16896
v_cvt_pk_bf16_f32 v161, v130, v131
v_mfma_f32_16x16x32_bf16 v[152:155], a[80:83], a[56:59], v[152:155]
v_exp_f32 v133, v133
v_mfma_f32_16x16x32_bf16 v[156:159], a[84:87], a[56:59], v[156:159]
buffer_load_dwordx4 a[228:231], v208, s[36:39], s15 offen offset:2048 sc1
v_mfma_f32_16x16x32_bf16 v[148:151], a[84:87], a[40:43], v[148:151]
ds_read_b128 a[76:79], v219 offset:20992
ds_read_b128 a[80:83], v219 offset:17408
v_mfma_f32_16x16x32_bf16 v[144:147], a[88:91], a[44:47], v[144:147]
v_exp_f32 v134, v134
v_mfma_f32_16x16x32_bf16 v[152:155], a[88:91], a[60:63], v[152:155]
buffer_load_dwordx4 v223, s[44:47], s72 offen offset:1024 lds
v_mfma_f32_16x16x32_bf16 v[156:159], a[92:95], a[60:63], v[156:159]
v_exp_f32 v135, v135
v_mfma_f32_16x16x32_bf16 v[148:151], a[92:95], a[44:47], v[148:151]
ds_read_b128 a[84:87], v219 offset:21504
v_cvt_pk_bf16_f32 v162, v132, v133
v_mfma_f32_16x16x32_bf16 v[200:203], v[232:235], v[168:171], v[200:203]
v_exp_f32 v136, v136
v_mfma_f32_16x16x32_bf16 v[204:207], v[232:235], v[172:175], v[204:207]
ds_read_b128 a[88:91], v219 offset:17920
ds_read_b128 a[92:95], v219 offset:22016
v_mfma_f32_16x16x32_bf16 v[96:99], a[96:99], v[172:175], v[96:99]
buffer_load_dwordx4 a[232:235], v208, s[36:39], s15 offen offset:3072 sc1
v_mfma_f32_16x16x32_bf16 v[64:67], a[96:99], v[168:171], v[64:67]
v_exp_f32 v137, v137
v_mfma_f32_16x16x32_bf16 v[68:71], a[100:103], v[168:171], v[68:71]
ds_read_b64_tr_b16 a[96:97], v220 offset:24576
v_cvt_pk_bf16_f32 v163, v134, v135
v_mfma_f32_16x16x32_bf16 v[100:103], a[100:103], v[172:175], v[100:103]
v_exp_f32 v138, v138
v_mfma_f32_16x16x32_bf16 v[104:107], a[104:107], v[172:175], v[104:107]
buffer_load_dwordx4 v224, s[44:47], s72 offen offset:2048 lds
v_mfma_f32_16x16x32_bf16 v[72:75], a[104:107], v[168:171], v[72:75]
v_exp_f32 v139, v139
v_mfma_f32_16x16x32_bf16 v[76:79], a[108:111], v[168:171], v[76:79]
ds_read_b64_tr_b16 a[98:99], v220 offset:28672
v_cvt_pk_bf16_f32 v164, v136, v137
v_mfma_f32_16x16x32_bf16 v[108:111], a[108:111], v[172:175], v[108:111]
v_exp_f32 v140, v140
v_mfma_f32_16x16x32_bf16 v[112:115], a[112:115], v[172:175], v[112:115]
buffer_load_dwordx4 v225, s[44:47], s72 offen offset:3072 lds
v_mfma_f32_16x16x32_bf16 v[80:83], a[112:115], v[168:171], v[80:83]
s_add_u32 s72, s72, 8192
ds_read_b64_tr_b16 a[100:101], v221 offset:24576
v_mfma_f32_16x16x32_bf16 v[84:87], a[116:119], v[168:171], v[84:87]
ds_read_b64_tr_b16 a[102:103], v221 offset:28672
v_cvt_pk_bf16_f32 v165, v138, v139
v_mfma_f32_16x16x32_bf16 v[116:119], a[116:119], v[172:175], v[116:119]
v_exp_f32 v141, v141
v_mfma_f32_16x16x32_bf16 v[120:123], a[120:123], v[172:175], v[120:123]
v_exp_f32 v142, v142
v_mfma_f32_16x16x32_bf16 v[88:91], a[120:123], v[168:171], v[88:91]
s_add_u32 m0, s71, 0
ds_read_b64_tr_b16 a[104:105], v220 offset:25088
v_mfma_f32_16x16x32_bf16 v[92:95], a[124:127], v[168:171], v[92:95]
ds_read_b64_tr_b16 a[106:107], v220 offset:29184
v_cvt_pk_bf16_f32 v166, v140, v141
v_mfma_f32_16x16x32_bf16 v[124:127], a[124:127], v[172:175], v[124:127]
v_exp_f32 v143, v143
s_nop 0
v_cvt_pk_bf16_f32 v167, v142, v143
s_waitcnt lgkmcnt(10)
v_mfma_f32_16x16x32_bf16 v[128:131], a[64:67], a[0:3], v[176:179]
v_exp_f32 v144, v144
v_mfma_f32_16x16x32_bf16 v[136:139], a[64:67], a[16:19], v[180:183]
v_exp_f32 v145, v145
v_mfma_f32_16x16x32_bf16 v[140:143], a[68:71], a[16:19], v[180:183]
buffer_load_dwordx4 v208, s[36:39], s78 offen offset:0 sc1 lds
v_mfma_f32_16x16x32_bf16 v[132:135], a[68:71], a[0:3], v[176:179]
ds_read_b64_tr_b16 a[108:109], v221 offset:25088
ds_read_b64_tr_b16 a[110:111], v221 offset:29184
v_mfma_f32_16x16x32_bf16 v[128:131], a[72:75], a[4:7], v[128:131]
v_exp_f32 v146, v146
v_mfma_f32_16x16x32_bf16 v[136:139], a[72:75], a[20:23], v[136:139]
v_exp_f32 v147, v147
v_mfma_f32_16x16x32_bf16 v[140:143], a[76:79], a[20:23], v[140:143]
ds_read_b64_tr_b16 a[112:113], v220 offset:25600
v_cvt_pk_bf16_f32 v168, v144, v145
v_mfma_f32_16x16x32_bf16 v[132:135], a[76:79], a[4:7], v[132:135]
s_waitcnt lgkmcnt(9)
ds_read_b64_tr_b16 a[114:115], v220 offset:29696
v_mfma_f32_16x16x32_bf16 v[128:131], a[80:83], a[8:11], v[128:131]
v_exp_f32 v148, v148
v_mfma_f32_16x16x32_bf16 v[136:139], a[80:83], a[24:27], v[136:139]
v_exp_f32 v149, v149
v_mfma_f32_16x16x32_bf16 v[140:143], a[84:87], a[24:27], v[140:143]
buffer_load_dwordx4 v208, s[36:39], s78 offen offset:1024 sc1 lds
v_mfma_f32_16x16x32_bf16 v[132:135], a[84:87], a[8:11], v[132:135]
ds_read_b64_tr_b16 a[116:117], v221 offset:25600
ds_read_b64_tr_b16 a[118:119], v221 offset:29696
v_mfma_f32_16x16x32_bf16 v[128:131], a[88:91], a[12:15], v[128:131]
ds_read_b64_tr_b16 a[120:121], v220 offset:26112
v_cvt_pk_bf16_f32 v169, v146, v147
v_mfma_f32_16x16x32_bf16 v[136:139], a[88:91], a[28:31], v[136:139]
v_exp_f32 v150, v150
v_mfma_f32_16x16x32_bf16 v[140:143], a[92:95], a[28:31], v[140:143]
ds_read_b64_tr_b16 a[122:123], v220 offset:30208
v_cvt_pk_bf16_f32 v170, v148, v149
v_mfma_f32_16x16x32_bf16 v[132:135], a[92:95], a[12:15], v[132:135]
v_exp_f32 v151, v151
v_mfma_f32_16x16x32_bf16 v[192:195], v[232:235], v[160:163], v[192:195]
v_exp_f32 v152, v152
v_mfma_f32_16x16x32_bf16 v[196:199], v[232:235], v[164:167], v[196:199]
s_waitcnt lgkmcnt(6)
ds_read_b64_tr_b16 a[124:125], v221 offset:26112
v_mfma_f32_16x16x32_bf16 v[32:35], a[96:99], v[164:167], v[32:35]
ds_read_b64_tr_b16 a[126:127], v221 offset:30208
v_cvt_pk_bf16_f32 v171, v150, v151
v_mfma_f32_16x16x32_bf16 v[0:3], a[96:99], v[160:163], v[0:3]
v_exp_f32 v153, v153
v_mfma_f32_16x16x32_bf16 v[4:7], a[100:103], v[160:163], v[4:7]
buffer_load_dwordx4 v208, s[36:39], s78 offen offset:2048 sc1 lds
v_mfma_f32_16x16x32_bf16 v[36:39], a[100:103], v[164:167], v[36:39]
v_exp_f32 v154, v154
v_mfma_f32_16x16x32_bf16 v[40:43], a[104:107], v[164:167], v[40:43]
v_exp_f32 v155, v155
v_mfma_f32_16x16x32_bf16 v[8:11], a[104:107], v[160:163], v[8:11]
v_cvt_pk_bf16_f32 v172, v152, v153
v_mfma_f32_16x16x32_bf16 v[12:15], a[108:111], v[160:163], v[12:15]
v_exp_f32 v156, v156
v_mfma_f32_16x16x32_bf16 v[44:47], a[108:111], v[164:167], v[44:47]
s_waitcnt lgkmcnt(0)
v_cvt_pk_bf16_f32 v173, v154, v155
v_mfma_f32_16x16x32_bf16 v[48:51], a[112:115], v[164:167], v[48:51]
buffer_load_dwordx4 v208, s[36:39], s78 offen offset:3072 sc1 lds
v_mfma_f32_16x16x32_bf16 v[16:19], a[112:115], v[160:163], v[16:19]
s_add_u32 s78, s78, 4096
v_mfma_f32_16x16x32_bf16 v[20:23], a[116:119], v[160:163], v[20:23]
v_exp_f32 v157, v157
v_mfma_f32_16x16x32_bf16 v[52:55], a[116:119], v[164:167], v[52:55]
v_exp_f32 v158, v158
v_mfma_f32_16x16x32_bf16 v[56:59], a[120:123], v[164:167], v[56:59]
v_exp_f32 v159, v159
v_mfma_f32_16x16x32_bf16 v[24:27], a[120:123], v[160:163], v[24:27]
v_exp_f32 v128, v128
v_mfma_f32_16x16x32_bf16 v[28:31], a[124:127], v[160:163], v[28:31]
v_cvt_pk_bf16_f32 v174, v156, v157
v_cvt_pk_bf16_f32 v175, v158, v159
v_mfma_f32_16x16x32_bf16 v[60:63], a[124:127], v[164:167], v[60:63]
s_waitcnt vmcnt(21)
s_barrier
v_mfma_f32_16x16x32_bf16 v[144:147], a[64:67], a[32:35], v[184:187]
v_exp_f32 v129, v129
v_mfma_f32_16x16x32_bf16 v[152:155], a[64:67], a[48:51], v[188:191]
s_add_u32 s33, s33, 4096
ds_read_b128 a[64:67], v219 offset:32768
v_mfma_f32_16x16x32_bf16 v[156:159], a[68:71], a[48:51], v[188:191]
v_exp_f32 v130, v130
v_mfma_f32_16x16x32_bf16 v[148:151], a[68:71], a[32:35], v[184:187]
v_exp_f32 v131, v131
v_mfma_f32_16x16x32_bf16 v[144:147], a[72:75], a[36:39], v[144:147]
ds_read_b128 a[68:71], v219 offset:36864
v_cvt_pk_bf16_f32 v160, v128, v129
v_mfma_f32_16x16x32_bf16 v[152:155], a[72:75], a[52:55], v[152:155]
buffer_load_dwordx2 v[228:229], v226, s[36:39], s33 offen sc1
v_mfma_f32_16x16x32_bf16 v[156:159], a[76:79], a[52:55], v[156:159]
s_add_u32 m0, s71, 16384
ds_read_b128 a[72:75], v219 offset:33280
v_mfma_f32_16x16x32_bf16 v[148:151], a[76:79], a[36:39], v[148:151]
v_exp_f32 v132, v132
v_mfma_f32_16x16x32_bf16 v[144:147], a[80:83], a[40:43], v[144:147]
buffer_load_dwordx4 v208, s[36:39], s78 offen offset:0 sc1 lds
v_mfma_f32_16x16x32_bf16 v[152:155], a[80:83], a[56:59], v[152:155]
v_exp_f32 v133, v133
v_mfma_f32_16x16x32_bf16 v[156:159], a[84:87], a[56:59], v[156:159]
ds_read_b128 a[76:79], v219 offset:37376
v_cvt_pk_bf16_f32 v161, v130, v131
v_mfma_f32_16x16x32_bf16 v[148:151], a[84:87], a[40:43], v[148:151]
v_exp_f32 v134, v134
v_mfma_f32_16x16x32_bf16 v[144:147], a[88:91], a[44:47], v[144:147]
s_add_u32 s15, s85, 32768
ds_read_b128 a[80:83], v219 offset:33792
v_mfma_f32_16x16x32_bf16 v[152:155], a[88:91], a[60:63], v[152:155]
v_exp_f32 v135, v135
v_mfma_f32_16x16x32_bf16 v[156:159], a[92:95], a[60:63], v[156:159]
ds_read_b128 a[84:87], v219 offset:37888
v_cvt_pk_bf16_f32 v162, v132, v133
v_mfma_f32_16x16x32_bf16 v[148:151], a[92:95], a[44:47], v[148:151]
buffer_load_dwordx4 a[236:239], v208, s[36:39], s15 offen offset:0 sc1
v_mfma_f32_16x16x32_bf16 v[200:203], v[232:235], v[168:171], v[200:203]
v_exp_f32 v136, v136
v_mfma_f32_16x16x32_bf16 v[204:207], v[232:235], v[172:175], v[204:207]
ds_read_b128 a[88:91], v219 offset:34304
ds_read_b128 a[92:95], v219 offset:38400
v_mfma_f32_16x16x32_bf16 v[96:99], a[96:99], v[172:175], v[96:99]
v_exp_f32 v137, v137
v_mfma_f32_16x16x32_bf16 v[64:67], a[96:99], v[168:171], v[64:67]
ds_read_b64_tr_b16 a[96:97], v220 offset:40960
v_cvt_pk_bf16_f32 v163, v134, v135
v_mfma_f32_16x16x32_bf16 v[68:71], a[100:103], v[168:171], v[68:71]
buffer_load_dwordx4 v208, s[36:39], s78 offen offset:1024 sc1 lds
v_mfma_f32_16x16x32_bf16 v[100:103], a[100:103], v[172:175], v[100:103]
v_exp_f32 v138, v138
v_mfma_f32_16x16x32_bf16 v[104:107], a[104:107], v[172:175], v[104:107]
v_exp_f32 v139, v139
v_mfma_f32_16x16x32_bf16 v[72:75], a[104:107], v[168:171], v[72:75]
ds_read_b64_tr_b16 a[98:99], v220 offset:45056
v_cvt_pk_bf16_f32 v164, v136, v137
v_mfma_f32_16x16x32_bf16 v[76:79], a[108:111], v[168:171], v[76:79]
v_exp_f32 v140, v140
v_mfma_f32_16x16x32_bf16 v[108:111], a[108:111], v[172:175], v[108:111]
buffer_load_dwordx4 a[240:243], v208, s[36:39], s15 offen offset:1024 sc1
v_mfma_f32_16x16x32_bf16 v[112:115], a[112:115], v[172:175], v[112:115]
ds_read_b64_tr_b16 a[100:101], v221 offset:40960
ds_read_b64_tr_b16 a[102:103], v221 offset:45056
v_mfma_f32_16x16x32_bf16 v[80:83], a[112:115], v[168:171], v[80:83]
v_exp_f32 v141, v141
v_mfma_f32_16x16x32_bf16 v[84:87], a[116:119], v[168:171], v[84:87]
ds_read_b64_tr_b16 a[104:105], v220 offset:41472
v_cvt_pk_bf16_f32 v165, v138, v139
v_mfma_f32_16x16x32_bf16 v[116:119], a[116:119], v[172:175], v[116:119]
v_exp_f32 v142, v142
v_mfma_f32_16x16x32_bf16 v[120:123], a[120:123], v[172:175], v[120:123]
ds_read_b64_tr_b16 a[106:107], v220 offset:45568
v_cvt_pk_bf16_f32 v166, v140, v141
v_mfma_f32_16x16x32_bf16 v[88:91], a[120:123], v[168:171], v[88:91]
v_exp_f32 v143, v143
v_mfma_f32_16x16x32_bf16 v[92:95], a[124:127], v[168:171], v[92:95]
v_exp_f32 v144, v144
v_mfma_f32_16x16x32_bf16 v[124:127], a[124:127], v[172:175], v[124:127]
v_cvt_pk_bf16_f32 v167, v142, v143
s_waitcnt lgkmcnt(10)
v_mfma_f32_16x16x32_bf16 v[128:131], a[64:67], a[0:3], v[176:179]
buffer_load_dwordx4 v208, s[36:39], s78 offen offset:2048 sc1 lds
v_mfma_f32_16x16x32_bf16 v[136:139], a[64:67], a[16:19], v[180:183]
v_exp_f32 v145, v145
v_mfma_f32_16x16x32_bf16 v[140:143], a[68:71], a[16:19], v[180:183]
ds_read_b64_tr_b16 a[108:109], v221 offset:41472
ds_read_b64_tr_b16 a[110:111], v221 offset:45568
v_mfma_f32_16x16x32_bf16 v[132:135], a[68:71], a[0:3], v[176:179]
v_exp_f32 v146, v146
v_mfma_f32_16x16x32_bf16 v[128:131], a[72:75], a[4:7], v[128:131]
buffer_load_dwordx4 a[244:247], v208, s[36:39], s15 offen offset:2048 sc1
v_mfma_f32_16x16x32_bf16 v[136:139], a[72:75], a[20:23], v[136:139]
v_exp_f32 v147, v147
v_mfma_f32_16x16x32_bf16 v[140:143], a[76:79], a[20:23], v[140:143]
ds_read_b64_tr_b16 a[112:113], v220 offset:41984
v_cvt_pk_bf16_f32 v168, v144, v145
v_mfma_f32_16x16x32_bf16 v[132:135], a[76:79], a[4:7], v[132:135]
s_waitcnt lgkmcnt(9)
ds_read_b64_tr_b16 a[114:115], v220 offset:46080
v_mfma_f32_16x16x32_bf16 v[128:131], a[80:83], a[8:11], v[128:131]
v_exp_f32 v148, v148
v_mfma_f32_16x16x32_bf16 v[136:139], a[80:83], a[24:27], v[136:139]
v_exp_f32 v149, v149
v_mfma_f32_16x16x32_bf16 v[140:143], a[84:87], a[24:27], v[140:143]
ds_read_b64_tr_b16 a[116:117], v221 offset:41984
ds_read_b64_tr_b16 a[118:119], v221 offset:46080
v_mfma_f32_16x16x32_bf16 v[132:135], a[84:87], a[8:11], v[132:135]
ds_read_b64_tr_b16 a[120:121], v220 offset:42496
v_cvt_pk_bf16_f32 v169, v146, v147
v_mfma_f32_16x16x32_bf16 v[128:131], a[88:91], a[12:15], v[128:131]
buffer_load_dwordx4 v208, s[36:39], s78 offen offset:3072 sc1 lds
s_add_u32 s78, s78, 4096
v_mfma_f32_16x16x32_bf16 v[136:139], a[88:91], a[28:31], v[136:139]
v_exp_f32 v150, v150
v_mfma_f32_16x16x32_bf16 v[140:143], a[92:95], a[28:31], v[140:143]
buffer_load_dwordx4 a[248:251], v208, s[36:39], s15 offen offset:3072 sc1
v_mfma_f32_16x16x32_bf16 v[132:135], a[92:95], a[12:15], v[132:135]
ds_read_b64_tr_b16 a[122:123], v220 offset:46592
v_cvt_pk_bf16_f32 v170, v148, v149
v_mfma_f32_16x16x32_bf16 v[192:195], v[232:235], v[160:163], v[192:195]
v_exp_f32 v151, v151
v_mfma_f32_16x16x32_bf16 v[196:199], v[232:235], v[164:167], v[196:199]
s_waitcnt lgkmcnt(6)
ds_read_b64_tr_b16 a[124:125], v221 offset:42496
v_mfma_f32_16x16x32_bf16 v[32:35], a[96:99], v[164:167], v[32:35]
v_exp_f32 v152, v152
v_mfma_f32_16x16x32_bf16 v[0:3], a[96:99], v[160:163], v[0:3]
ds_read_b64_tr_b16 a[126:127], v221 offset:46592
v_cvt_pk_bf16_f32 v171, v150, v151
v_mfma_f32_16x16x32_bf16 v[4:7], a[100:103], v[160:163], v[4:7]
v_exp_f32 v153, v153
v_mfma_f32_16x16x32_bf16 v[36:39], a[100:103], v[164:167], v[36:39]
s_add_u32 s15, s85, 36864
v_mfma_f32_16x16x32_bf16 v[40:43], a[104:107], v[164:167], v[40:43]
v_exp_f32 v154, v154
v_mfma_f32_16x16x32_bf16 v[8:11], a[104:107], v[160:163], v[8:11]
v_exp_f32 v155, v155
v_mfma_f32_16x16x32_bf16 v[12:15], a[108:111], v[160:163], v[12:15]
v_cvt_pk_bf16_f32 v172, v152, v153
v_mfma_f32_16x16x32_bf16 v[44:47], a[108:111], v[164:167], v[44:47]
s_waitcnt lgkmcnt(0)
v_mfma_f32_16x16x32_bf16 v[48:51], a[112:115], v[164:167], v[48:51]
buffer_load_dwordx4 a[252:255], v208, s[36:39], s15 offen offset:0 sc1
v_mfma_f32_16x16x32_bf16 v[16:19], a[112:115], v[160:163], v[16:19]
v_exp_f32 v156, v156
v_mfma_f32_16x16x32_bf16 v[20:23], a[116:119], v[160:163], v[20:23]
v_exp_f32 v157, v157
v_mfma_f32_16x16x32_bf16 v[52:55], a[116:119], v[164:167], v[52:55]
v_cvt_pk_bf16_f32 v173, v154, v155
v_mfma_f32_16x16x32_bf16 v[56:59], a[120:123], v[164:167], v[56:59]
v_exp_f32 v158, v158
v_mfma_f32_16x16x32_bf16 v[24:27], a[120:123], v[160:163], v[24:27]
v_exp_f32 v159, v159
v_mfma_f32_16x16x32_bf16 v[28:31], a[124:127], v[160:163], v[28:31]
v_exp_f32 v128, v128
v_mfma_f32_16x16x32_bf16 v[60:63], a[124:127], v[164:167], v[60:63]
v_cvt_pk_bf16_f32 v174, v156, v157
v_cvt_pk_bf16_f32 v175, v158, v159
v_mfma_f32_16x16x32_bf16 v[144:147], a[64:67], a[32:35], v[184:187]
v_exp_f32 v129, v129
v_mfma_f32_16x16x32_bf16 v[152:155], a[64:67], a[48:51], v[188:191]
v_exp_f32 v130, v130
v_mfma_f32_16x16x32_bf16 v[156:159], a[68:71], a[48:51], v[188:191]
v_exp_f32 v131, v131
v_mfma_f32_16x16x32_bf16 v[148:151], a[68:71], a[32:35], v[184:187]
v_cvt_pk_bf16_f32 v160, v128, v129
v_mfma_f32_16x16x32_bf16 v[144:147], a[72:75], a[36:39], v[144:147]
v_exp_f32 v132, v132
v_mfma_f32_16x16x32_bf16 v[152:155], a[72:75], a[52:55], v[152:155]
v_exp_f32 v133, v133
v_mfma_f32_16x16x32_bf16 v[156:159], a[76:79], a[52:55], v[156:159]
v_exp_f32 v134, v134
v_mfma_f32_16x16x32_bf16 v[148:151], a[76:79], a[36:39], v[148:151]
v_cvt_pk_bf16_f32 v161, v130, v131
v_cvt_pk_bf16_f32 v162, v132, v133
v_mfma_f32_16x16x32_bf16 v[144:147], a[80:83], a[40:43], v[144:147]
v_exp_f32 v135, v135
v_mfma_f32_16x16x32_bf16 v[152:155], a[80:83], a[56:59], v[152:155]
v_exp_f32 v136, v136
v_mfma_f32_16x16x32_bf16 v[156:159], a[84:87], a[56:59], v[156:159]
v_exp_f32 v137, v137
v_mfma_f32_16x16x32_bf16 v[148:151], a[84:87], a[40:43], v[148:151]
v_exp_f32 v138, v138
v_mfma_f32_16x16x32_bf16 v[144:147], a[88:91], a[44:47], v[144:147]
v_cvt_pk_bf16_f32 v163, v134, v135
v_cvt_pk_bf16_f32 v164, v136, v137
v_mfma_f32_16x16x32_bf16 v[152:155], a[88:91], a[60:63], v[152:155]
v_exp_f32 v139, v139
v_mfma_f32_16x16x32_bf16 v[156:159], a[92:95], a[60:63], v[156:159]
v_exp_f32 v140, v140
v_mfma_f32_16x16x32_bf16 v[148:151], a[92:95], a[44:47], v[148:151]
s_add_u32 s41, s85, 73728
v_cvt_pk_bf16_f32 v165, v138, v139
v_mfma_f32_16x16x32_bf16 v[200:203], v[232:235], v[168:171], v[200:203]
v_exp_f32 v141, v141
v_mfma_f32_16x16x32_bf16 v[204:207], v[232:235], v[172:175], v[204:207]
s_waitcnt vmcnt(9)
v_cmp_ne_u64 vcc, s[80:81], v[228:229]
s_nop 0
s_cbranch_vccz .Lk_rt2_ok
s_mov_b32 s89, 0
.Lk_rt2:
buffer_load_dwordx2 v[228:229], v226, s[36:39], s33 offen sc1
s_waitcnt vmcnt(0)
v_cmp_ne_u64 vcc, s[80:81], v[228:229]
s_nop 0
s_cbranch_vccz .Lk_rt2_ok
s_add_u32 s89, s89, 1
s_cmp_lt_u32 s89, 0x20000
s_cbranch_scc1 .Lk_rt2
.Lk_rt2_ok:
s_barrier
v_mfma_f32_16x16x32_bf16 v[96:99], a[96:99], v[172:175], v[96:99]
buffer_load_dwordx4 a[64:67], v208, s[36:39], s41 offen offset:1024 sc1
v_mfma_f32_16x16x32_bf16 v[64:67], a[96:99], v[168:171], v[64:67]
s_add_u32 s41, s85, 73728
ds_read_b64_tr_b16 a[96:97], v220 offset:57344
v_mfma_f32_16x16x32_bf16 v[68:71], a[100:103], v[168:171], v[68:71]
v_exp_f32 v142, v142
v_mfma_f32_16x16x32_bf16 v[100:103], a[100:103], v[172:175], v[100:103]
ds_read_b64_tr_b16 a[98:99], v220 offset:61440
ds_read_b64_tr_b16 a[100:101], v221 offset:57344
v_mfma_f32_16x16x32_bf16 v[104:107], a[104:107], v[172:175], v[104:107]
buffer_load_dwordx4 a[68:71], v208, s[36:39], s41 offen offset:2048 sc1
v_mfma_f32_16x16x32_bf16 v[72:75], a[104:107], v[168:171], v[72:75]
s_add_u32 m0, s71, 32768
ds_read_b64_tr_b16 a[102:103], v221 offset:61440
v_mfma_f32_16x16x32_bf16 v[76:79], a[108:111], v[168:171], v[76:79]
buffer_load_dwordx4 v208, s[36:39], s78 offen offset:0 sc1 lds
v_mfma_f32_16x16x32_bf16 v[108:111], a[108:111], v[172:175], v[108:111]
s_add_u32 s41, s85, 73728
ds_read_b64_tr_b16 a[104:105], v220 offset:57856
v_mfma_f32_16x16x32_bf16 v[112:115], a[112:115], v[172:175], v[112:115]
buffer_load_dwordx4 a[72:75], v208, s[36:39], s41 offen offset:3072 sc1
v_mfma_f32_16x16x32_bf16 v[80:83], a[112:115], v[168:171], v[80:83]
s_add_u32 s41, s85, 77824
ds_read_b64_tr_b16 a[106:107], v220 offset:61952
v_mfma_f32_16x16x32_bf16 v[84:87], a[116:119], v[168:171], v[84:87]
v_exp_f32 v143, v143
v_mfma_f32_16x16x32_bf16 v[116:119], a[116:119], v[172:175], v[116:119]
v_cvt_pk_bf16_f32 v166, v140, v141
v_cvt_pk_bf16_f32 v167, v142, v143
v_mfma_f32_16x16x32_bf16 v[120:123], a[120:123], v[172:175], v[120:123]
v_exp_f32 v144, v144
v_mfma_f32_16x16x32_bf16 v[88:91], a[120:123], v[168:171], v[88:91]
v_exp_f32 v145, v145
v_mfma_f32_16x16x32_bf16 v[92:95], a[124:127], v[168:171], v[92:95]
buffer_load_dwordx4 a[76:79], v208, s[36:39], s41 offen offset:0 sc1
v_mfma_f32_16x16x32_bf16 v[124:127], a[124:127], v[172:175], v[124:127]
s_add_u32 s41, s85, 77824
ds_read_b64_tr_b16 a[108:109], v221 offset:57856
v_mfma_f32_16x16x32_bf16 v[192:195], v[232:235], v[160:163], v[192:195]
v_exp_f32 v146, v146
v_mfma_f32_16x16x32_bf16 v[196:199], v[232:235], v[164:167], v[196:199]
s_waitcnt lgkmcnt(0)
ds_read_b64_tr_b16 a[110:111], v221 offset:61952
v_mfma_f32_16x16x32_bf16 v[32:35], a[96:99], v[164:167], v[32:35]
v_exp_f32 v147, v147
v_mfma_f32_16x16x32_bf16 v[0:3], a[96:99], v[160:163], v[0:3]
v_exp_f32 v148, v148
v_mfma_f32_16x16x32_bf16 v[4:7], a[100:103], v[160:163], v[4:7]
ds_read_b64_tr_b16 a[112:113], v220 offset:58368
v_cvt_pk_bf16_f32 v168, v144, v145
v_mfma_f32_16x16x32_bf16 v[36:39], a[100:103], v[164:167], v[36:39]
buffer_load_dwordx4 a[80:83], v208, s[36:39], s41 offen offset:1024 sc1
ds_read_b64_tr_b16 a[114:115], v220 offset:62464
v_mfma_f32_16x16x32_bf16 v[40:43], a[104:107], v[164:167], v[40:43]
buffer_load_dwordx4 v208, s[36:39], s78 offen offset:1024 sc1 lds
v_mfma_f32_16x16x32_bf16 v[8:11], a[104:107], v[160:163], v[8:11]
ds_read_b64_tr_b16 a[116:117], v221 offset:58368
ds_read_b64_tr_b16 a[118:119], v221 offset:62464
s_waitcnt lgkmcnt(4)
v_mfma_f32_16x16x32_bf16 v[12:15], a[108:111], v[160:163], v[12:15]
v_exp_f32 v149, v149
v_mfma_f32_16x16x32_bf16 v[44:47], a[108:111], v[164:167], v[44:47]
s_add_u32 s41, s85, 77824
buffer_load_dwordx4 a[84:87], v208, s[36:39], s41 offen offset:2048 sc1
ds_read_b64_tr_b16 a[120:121], v220 offset:58880
ds_read_b64_tr_b16 a[122:123], v220 offset:62976
s_waitcnt lgkmcnt(0)
v_mfma_f32_16x16x32_bf16 v[48:51], a[112:115], v[164:167], v[48:51]
v_exp_f32 v150, v150
v_mfma_f32_16x16x32_bf16 v[16:19], a[112:115], v[160:163], v[16:19]
ds_read_b64_tr_b16 a[124:125], v221 offset:58880
ds_read_b64_tr_b16 a[126:127], v221 offset:62976
v_mfma_f32_16x16x32_bf16 v[20:23], a[116:119], v[160:163], v[20:23]
v_exp_f32 v151, v151
v_mfma_f32_16x16x32_bf16 v[52:55], a[116:119], v[164:167], v[52:55]
s_add_u32 s41, s85, 77824
buffer_load_dwordx4 a[88:91], v208, s[36:39], s41 offen offset:3072 sc1
v_mfma_f32_16x16x32_bf16 v[56:59], a[120:123], v[164:167], v[56:59]
v_cvt_pk_bf16_f32 v169, v146, v147
v_cvt_pk_bf16_f32 v170, v148, v149
v_mfma_f32_16x16x32_bf16 v[24:27], a[120:123], v[160:163], v[24:27]
v_exp_f32 v152, v152
s_waitcnt lgkmcnt(0)
v_mfma_f32_16x16x32_bf16 v[28:31], a[124:127], v[160:163], v[28:31]
v_exp_f32 v153, v153
v_exp_f32 v154, v154
v_exp_f32 v155, v155
v_cvt_pk_bf16_f32 v171, v150, v151
v_exp_f32 v156, v156
v_cvt_pk_bf16_f32 v172, v152, v153
v_exp_f32 v157, v157
v_exp_f32 v158, v158
v_exp_f32 v159, v159
v_cvt_pk_bf16_f32 v173, v154, v155
v_cvt_pk_bf16_f32 v174, v156, v157
v_cvt_pk_bf16_f32 v175, v158, v159
v_mfma_f32_16x16x32_bf16 v[60:63], a[124:127], v[164:167], v[60:63]
buffer_load_dwordx4 v208, s[36:39], s78 offen offset:2048 sc1 lds
v_mfma_f32_16x16x32_bf16 v[200:203], v[232:235], v[168:171], v[200:203]
v_mfma_f32_16x16x32_bf16 v[204:207], v[232:235], v[172:175], v[204:207]
v_mfma_f32_16x16x32_bf16 v[96:99], a[96:99], v[172:175], v[96:99]
v_mfma_f32_16x16x32_bf16 v[64:67], a[96:99], v[168:171], v[64:67]
v_mfma_f32_16x16x32_bf16 v[68:71], a[100:103], v[168:171], v[68:71]
v_rcp_f32 v214, v192
v_mfma_f32_16x16x32_bf16 v[100:103], a[100:103], v[172:175], v[100:103]
v_rcp_f32 v215, v196
v_mfma_f32_16x16x32_bf16 v[104:107], a[104:107], v[172:175], v[104:107]
v_mul_f32 v0, v214, v0
v_mul_f32 v1, v214, v1
v_mfma_f32_16x16x32_bf16 v[72:75], a[104:107], v[168:171], v[72:75]
v_mul_f32 v2, v214, v2
v_mul_f32 v3, v214, v3
v_mfma_f32_16x16x32_bf16 v[76:79], a[108:111], v[168:171], v[76:79]
v_mul_f32 v4, v214, v4
v_mul_f32 v5, v214, v5
v_mfma_f32_16x16x32_bf16 v[108:111], a[108:111], v[172:175], v[108:111]
v_mul_f32 v6, v214, v6
v_mul_f32 v7, v214, v7
v_mfma_f32_16x16x32_bf16 v[112:115], a[112:115], v[172:175], v[112:115]
v_cvt_pk_bf16_f32 v0, v0, v1
v_cvt_pk_bf16_f32 v1, v2, v3
v_mfma_f32_16x16x32_bf16 v[80:83], a[112:115], v[168:171], v[80:83]
v_cvt_pk_bf16_f32 v2, v4, v5
v_cvt_pk_bf16_f32 v3, v6, v7
v_mfma_f32_16x16x32_bf16 v[84:87], a[116:119], v[168:171], v[84:87]
buffer_load_dwordx4 v208, s[36:39], s78 offen offset:3072 sc1 lds
s_add_u32 s78, s78, 4096
v_mfma_f32_16x16x32_bf16 v[116:119], a[116:119], v[172:175], v[116:119]
s_xor_b32 s96, s73, 0
v_add_f32 v216, v192, v196
v_mfma_f32_16x16x32_bf16 v[120:123], a[120:123], v[172:175], v[120:123]
buffer_store_dwordx4 v[0:3], v208, s[36:39], s96 offen sc1
v_mfma_f32_16x16x32_bf16 v[88:91], a[120:123], v[168:171], v[88:91]
v_mul_f32 v8, v214, v8
v_mul_f32 v9, v214, v9
v_mfma_f32_16x16x32_bf16 v[92:95], a[124:127], v[168:171], v[92:95]
v_mul_f32 v10, v214, v10
v_mul_f32 v11, v214, v11
v_mfma_f32_16x16x32_bf16 v[124:127], a[124:127], v[172:175], v[124:127]
v_mul_f32 v12, v214, v12
v_mul_f32 v13, v214, v13
v_mul_f32 v14, v214, v14
v_mul_f32 v15, v214, v15
v_cvt_pk_bf16_f32 v8, v8, v9
v_cvt_pk_bf16_f32 v9, v10, v11
v_cvt_pk_bf16_f32 v10, v12, v13
v_cvt_pk_bf16_f32 v11, v14, v15
s_xor_b32 s96, s73, 4096
buffer_store_dwordx4 v[8:11], v208, s[36:39], s96 offen sc1
v_mul_f32 v16, v214, v16
v_mul_f32 v17, v214, v17
v_mul_f32 v18, v214, v18
v_mul_f32 v19, v214, v19
v_mul_f32 v20, v214, v20
v_mul_f32 v21, v214, v21
v_mul_f32 v22, v214, v22
v_mul_f32 v23, v214, v23
v_cvt_pk_bf16_f32 v16, v16, v17
v_cvt_pk_bf16_f32 v17, v18, v19
v_cvt_pk_bf16_f32 v18, v20, v21
v_cvt_pk_bf16_f32 v19, v22, v23
s_xor_b32 s96, s73, 8192
buffer_store_dwordx4 v[16:19], v208, s[36:39], s96 offen sc1
v_mul_f32 v24, v214, v24
v_mul_f32 v25, v214, v25
v_mul_f32 v26, v214, v26
v_mul_f32 v27, v214, v27
v_mul_f32 v28, v214, v28
v_mul_f32 v29, v214, v29
v_mul_f32 v30, v214, v30
v_mul_f32 v31, v214, v31
v_cvt_pk_bf16_f32 v24, v24, v25
v_cvt_pk_bf16_f32 v25, v26, v27
v_cvt_pk_bf16_f32 v26, v28, v29
v_cvt_pk_bf16_f32 v27, v30, v31
s_xor_b32 s96, s73, 12288
buffer_store_dwordx4 v[24:27], v208, s[36:39], s96 offen sc1
v_mul_f32 v32, v215, v32
v_mul_f32 v33, v215, v33
v_mul_f32 v34, v215, v34
v_mul_f32 v35, v215, v35
v_mul_f32 v36, v215, v36
v_mul_f32 v37, v215, v37
v_mul_f32 v38, v215, v38
v_mul_f32 v39, v215, v39
v_cvt_pk_bf16_f32 v32, v32, v33
v_cvt_pk_bf16_f32 v33, v34, v35
v_cvt_pk_bf16_f32 v34, v36, v37
v_cvt_pk_bf16_f32 v35, v38, v39
s_xor_b32 s96, s73, 1024
buffer_store_dwordx4 v[32:35], v208, s[36:39], s96 offen sc1
v_mul_f32 v40, v215, v40
v_mul_f32 v41, v215, v41
v_mul_f32 v42, v215, v42
v_mul_f32 v43, v215, v43
v_mul_f32 v44, v215, v44
v_mul_f32 v45, v215, v45
v_mul_f32 v46, v215, v46
v_mul_f32 v47, v215, v47
v_cvt_pk_bf16_f32 v40, v40, v41
v_cvt_pk_bf16_f32 v41, v42, v43
v_cvt_pk_bf16_f32 v42, v44, v45
v_cvt_pk_bf16_f32 v43, v46, v47
s_xor_b32 s96, s73, 5120
buffer_store_dwordx4 v[40:43], v208, s[36:39], s96 offen sc1
v_mul_f32 v48, v215, v48
v_mul_f32 v49, v215, v49
v_mul_f32 v50, v215, v50
v_mul_f32 v51, v215, v51
v_mul_f32 v52, v215, v52
v_mul_f32 v53, v215, v53
v_mul_f32 v54, v215, v54
v_mul_f32 v55, v215, v55
v_cvt_pk_bf16_f32 v48, v48, v49
v_cvt_pk_bf16_f32 v49, v50, v51
v_cvt_pk_bf16_f32 v50, v52, v53
v_cvt_pk_bf16_f32 v51, v54, v55
s_xor_b32 s96, s73, 9216
buffer_store_dwordx4 v[48:51], v208, s[36:39], s96 offen sc1
v_mul_f32 v56, v215, v56
v_mul_f32 v57, v215, v57
v_mul_f32 v58, v215, v58
v_mul_f32 v59, v215, v59
v_mul_f32 v60, v215, v60
v_mul_f32 v61, v215, v61
v_mul_f32 v62, v215, v62
v_mul_f32 v63, v215, v63
v_cvt_pk_bf16_f32 v56, v56, v57
v_cvt_pk_bf16_f32 v57, v58, v59
v_cvt_pk_bf16_f32 v58, v60, v61
v_cvt_pk_bf16_f32 v59, v62, v63
s_xor_b32 s96, s73, 13312
buffer_store_dwordx4 v[56:59], v208, s[36:39], s96 offen sc1
v_add_f32 v217, v200, v204
v_add_f32 v216, v216, v217
v_cmp_ngt_f32 vcc, 0x5f800000, v216
s_nop 0
s_cbranch_vccnz .Lk_fb
.Lk_fbret:
v_and_b32 v230, 7, v209
v_lshlrev_b32 v230, 3, v230
v_lshlrev_b32 v231, 13, v209
s_lshl_b32 s45, s68, 5
s_add_u32 s45, s45, s67
s_lshl_b32 s45, s45, 3
s_add_u32 s45, s45, 16842752
v_rcp_f32 v214, v200
v_rcp_f32 v215, v204
s_nop 0
v_pk_mul_f32 v[64:65], v[64:65], v[214:215] op_sel_hi:[1,0]
v_pk_mul_f32 v[66:67], v[66:67], v[214:215] op_sel_hi:[1,0]
v_pk_mul_f32 v[68:69], v[68:69], v[214:215] op_sel_hi:[1,0]
v_pk_mul_f32 v[70:71], v[70:71], v[214:215] op_sel_hi:[1,0]
v_cvt_pk_bf16_f32 v64, v64, v65
v_cvt_pk_bf16_f32 v65, v66, v67
v_cvt_pk_bf16_f32 v66, v68, v69
v_cvt_pk_bf16_f32 v67, v70, v71
s_xor_b32 s96, s73, 2048
s_waitcnt vmcnt(0)
s_mov_b64 exec, 15
buffer_atomic_swap_x2 v[210:211], v231, s[36:39], s45 offen sc1
s_mov_b64 exec, -1
buffer_store_dwordx4 v[64:67], v208, s[36:39], s96 offen sc1
s_and_b32 s14, s83, 3
s_lshl_b32 s14, s14, 13
s_and_b32 s40, s82, 1
s_lshl_b32 s40, s40, 2
s_add_u32 s40, s40, s64
s_xor_b32 s46, s82, 1
s_lshl_b32 s46, s46, 3
s_add_u32 s46, s46, s40
s_lshl_b32 s46, s46, 5
s_add_u32 s46, s46, s14
s_andn2_b32 s47, s46, 256
s_add_u32 s46, s46, 16842752
s_add_u32 s47, s47, 16875520
v_pk_mul_f32 v[72:73], v[72:73], v[214:215] op_sel_hi:[1,0]
v_pk_mul_f32 v[74:75], v[74:75], v[214:215] op_sel_hi:[1,0]
v_pk_mul_f32 v[76:77], v[76:77], v[214:215] op_sel_hi:[1,0]
v_pk_mul_f32 v[78:79], v[78:79], v[214:215] op_sel_hi:[1,0]
v_cvt_pk_bf16_f32 v72, v72, v73
v_cvt_pk_bf16_f32 v73, v74, v75
v_cvt_pk_bf16_f32 v74, v76, v77
v_cvt_pk_bf16_f32 v75, v78, v79
s_xor_b32 s96, s73, 6144
buffer_store_dwordx4 v[72:75], v208, s[36:39], s96 offen sc1
v_pk_mul_f32 v[80:81], v[80:81], v[214:215] op_sel_hi:[1,0]
v_pk_mul_f32 v[82:83], v[82:83], v[214:215] op_sel_hi:[1,0]
v_pk_mul_f32 v[84:85], v[84:85], v[214:215] op_sel_hi:[1,0]
v_pk_mul_f32 v[86:87], v[86:87], v[214:215] op_sel_hi:[1,0]
v_cvt_pk_bf16_f32 v80, v80, v81
v_cvt_pk_bf16_f32 v81, v82, v83
v_cvt_pk_bf16_f32 v82, v84, v85
v_cvt_pk_bf16_f32 v83, v86, v87
s_xor_b32 s96, s73, 10240
buffer_store_dwordx4 v[80:83], v208, s[36:39], s96 offen sc1
v_pk_mul_f32 v[88:89], v[88:89], v[214:215] op_sel_hi:[1,0]
v_pk_mul_f32 v[90:91], v[90:91], v[214:215] op_sel_hi:[1,0]
v_pk_mul_f32 v[92:93], v[92:93], v[214:215] op_sel_hi:[1,0]
v_pk_mul_f32 v[94:95], v[94:95], v[214:215] op_sel_hi:[1,0]
v_cvt_pk_bf16_f32 v88, v88, v89
v_cvt_pk_bf16_f32 v89, v90, v91
v_cvt_pk_bf16_f32 v90, v92, v93
v_cvt_pk_bf16_f32 v91, v94, v95
s_xor_b32 s96, s73, 14336
buffer_store_dwordx4 v[88:91], v208, s[36:39], s96 offen sc1
v_pk_mul_f32 v[96:97], v[96:97], v[214:215] op_sel:[0,1] op_sel_hi:[1,1]
v_pk_mul_f32 v[98:99], v[98:99], v[214:215] op_sel:[0,1] op_sel_hi:[1,1]
v_pk_mul_f32 v[100:101], v[100:101], v[214:215] op_sel:[0,1] op_sel_hi:[1,1]
v_pk_mul_f32 v[102:103], v[102:103], v[214:215] op_sel:[0,1] op_sel_hi:[1,1]
v_cvt_pk_bf16_f32 v96, v96, v97
v_cvt_pk_bf16_f32 v97, v98, v99
v_cvt_pk_bf16_f32 v98, v100, v101
v_cvt_pk_bf16_f32 v99, v102, v103
s_xor_b32 s96, s73, 3072
buffer_store_dwordx4 v[96:99], v208, s[36:39], s96 offen sc1
v_pk_mul_f32 v[104:105], v[104:105], v[214:215] op_sel:[0,1] op_sel_hi:[1,1]
v_pk_mul_f32 v[106:107], v[106:107], v[214:215] op_sel:[0,1] op_sel_hi:[1,1]
v_pk_mul_f32 v[108:109], v[108:109], v[214:215] op_sel:[0,1] op_sel_hi:[1,1]
v_pk_mul_f32 v[110:111], v[110:111], v[214:215] op_sel:[0,1] op_sel_hi:[1,1]
v_cvt_pk_bf16_f32 v104, v104, v105
v_cvt_pk_bf16_f32 v105, v106, v107
v_cvt_pk_bf16_f32 v106, v108, v109
v_cvt_pk_bf16_f32 v107, v110, v111
s_xor_b32 s96, s73, 7168
buffer_store_dwordx4 v[104:107], v208, s[36:39], s96 offen sc1
v_pk_mul_f32 v[112:113], v[112:113], v[214:215] op_sel:[0,1] op_sel_hi:[1,1]
v_pk_mul_f32 v[114:115], v[114:115], v[214:215] op_sel:[0,1] op_sel_hi:[1,1]
v_pk_mul_f32 v[116:117], v[116:117], v[214:215] op_sel:[0,1] op_sel_hi:[1,1]
v_pk_mul_f32 v[118:119], v[118:119], v[214:215] op_sel:[0,1] op_sel_hi:[1,1]
v_cvt_pk_bf16_f32 v112, v112, v113
v_cvt_pk_bf16_f32 v113, v114, v115
v_cvt_pk_bf16_f32 v114, v116, v117
v_cvt_pk_bf16_f32 v115, v118, v119
s_xor_b32 s96, s73, 11264
buffer_store_dwordx4 v[112:115], v208, s[36:39], s96 offen sc1
v_pk_mul_f32 v[120:121], v[120:121], v[214:215] op_sel:[0,1] op_sel_hi:[1,1]
v_pk_mul_f32 v[122:123], v[122:123], v[214:215] op_sel:[0,1] op_sel_hi:[1,1]
v_pk_mul_f32 v[124:125], v[124:125], v[214:215] op_sel:[0,1] op_sel_hi:[1,1]
v_pk_mul_f32 v[126:127], v[126:127], v[214:215] op_sel:[0,1] op_sel_hi:[1,1]
v_cvt_pk_bf16_f32 v120, v120, v121
v_cvt_pk_bf16_f32 v121, v122, v123
v_cvt_pk_bf16_f32 v122, v124, v125
v_cvt_pk_bf16_f32 v123, v126, v127
s_xor_b32 s96, s73, 15360
buffer_store_dwordx4 v[120:123], v208, s[36:39], s96 offen sc1
v_mov_b64 v[128:129], 0
s_mov_b64 exec, 15
buffer_atomic_add_x2 v[128:129], v230, s[36:39], s46 offen sc0 sc1
s_mov_b64 exec, -1
v_and_b32 v214, 15, v209
v_lshrrev_b32 v215, 4, v209
s_lshl_b32 s75, s64, 10
v_add_u32 v218, s75, v208
s_and_b32 s98, s82, 3
s_lshl_b32 s98, s98, 10
v_xor_b32 v218, s98, v218
v_xor_b32 v219, 1024, v218
v_xor_b32 v220, 2048, v218
v_xor_b32 v221, 3072, v218
v_mul_u32_u24 v222, 1280, v214
v_lshl_add_u32 v223, v215, 3, v222
v_lshl_add_u32 v222, v215, 4, v222
s_lshl_b32 s75, s82, 2
s_add_u32 s75, s75, s64
s_mul_i32 s86, s75, 20480
s_mul_i32 s76, s83, 160
s_add_u32 s86, s86, s76
s_lshl_b32 s84, s82, 3
s_add_u32 s84, s84, s40
s_lshl_b32 s94, s84, 16
s_lshl_b32 s76, s82, 3
s_add_u32 s76, s76, s83
s_lshl_b32 s76, s76, 2
s_add_u32 s76, s76, s64
s_mul_i32 s41, s76, 5120
s_add_u32 s41, s41, 42549248
s_lshl_b32 s43, s76, 3
s_add_u32 s43, s43, 17006592
s_xor_b32 s76, s76, 32
s_mul_i32 s42, s76, 5120
s_add_u32 s42, s42, 42549248
s_lshl_b32 s44, s76, 3
s_add_u32 s44, s44, 17006592
s_mul_i32 s76, s83, 10
v_add_u32 v216, s76, v215
v_lshrrev_b32 v224, 2, v216
v_and_b32 v216, 3, v216
v_lshlrev_b32 v224, 13, v224
v_lshl_add_u32 v224, v216, 8, v224
v_lshl_add_u32 v224, v214, 4, v224
s_add_u32 s76, s76, 8
v_lshrrev_b32 v216, 1, v215
v_add_u32 v216, s76, v216
v_lshrrev_b32 v225, 2, v216
v_and_b32 v216, 3, v216
v_lshlrev_b32 v225, 13, v225
v_lshl_add_u32 v225, v216, 8, v225
v_lshl_add_u32 v225, v214, 4, v225
v_and_b32 v216, 1, v215
v_lshl_add_u32 v225, v216, 3, v225
s_mul_i32 s76, s64, 24576
s_add_u32 s77, s66, 65536
s_add_u32 s76, s76, s77
v_add_u32 v228, s76, v208
s_add_u32 s76, s76, 23552
v_lshl_add_u32 v226, v215, 5, s76
v_lshl_add_u32 v227, v215, 4, s76
s_xor_b32 s76, s64, 1
s_mul_i32 s76, s76, 24576
s_add_u32 s76, s76, s77
v_add_u32 v229, s76, v208
s_xor_b32 s76, s64, 2
s_mul_i32 s76, s76, 24576
s_add_u32 s76, s76, s77
v_add_u32 v234, s76, v208
s_xor_b32 s76, s64, 3
s_mul_i32 s76, s76, 24576
s_add_u32 s76, s76, s77
v_add_u32 v235, s76, v208
v_add_u32 v232, s71, v208
v_lshlrev_b32 v233, 4, v214
v_and_b32 v216, 1, v215
v_lshl_add_u32 v233, v216, 11, v233
v_lshrrev_b32 v216, 1, v215
v_lshl_add_u32 v233, v216, 10, v233
s_bfe_u32 s75, s65, 0x10002
s_lshl_b32 s75, s75, 3
s_lshr_b32 s99, s65, 5
s_add_u32 s99, s99, s75
s_and_b32 s75, s65, 3
s_lshl_b32 s75, s75, 2
s_bfe_u32 s69, s65, 0x20003
s_add_u32 s69, s69, s75
s_lshr_b32 s75, s69, 1
s_mul_i32 s75, s75, 27
s_and_b32 s76, s69, 1
s_mul_i32 s77, s76, 14
s_add_u32 s75, s75, s77
s_mul_i32 s75, s75, 21504
s_add_u32 s75, s75, 25559040
s_mul_i32 s77, s64, 21504
s_add_u32 s10, s75, s77
s_add_u32 s8, s10, s77
s_add_u32 s9, s8, 21504
s_add_u32 s10, s10, 172032
s_lshr_b32 s77, s64, 1
s_and_b32 s76, s76, s77
s_cselect_b32 s27, 47923200, 0
s_mul_i32 s77, s77, 21504
s_add_u32 s11, s75, s77
s_add_u32 s11, s11, 258048
s_max_u32 s11, s11, s27
v_and_b32 v214, 3, v209
v_bfe_u32 v215, v209, 2, 1
v_lshl_add_u32 v214, v215, 5, v214
v_lshlrev_b32 v214, 3, v214
s_add_u32 s45, s45, 32768
ds_read_b128 a[0:3], v228 offset:0
ds_read_b128 a[4:7], v228 offset:1024
ds_read_b128 a[8:11], v228 offset:2048
ds_read_b128 a[12:15], v228 offset:3072
ds_read_b128 a[16:19], v228 offset:4096
ds_read_b128 a[20:23], v228 offset:5120
ds_read_b128 a[24:27], v228 offset:6144
ds_read_b128 a[28:31], v228 offset:7168
ds_read_b128 a[32:35], v228 offset:8192
ds_read_b128 a[36:39], v228 offset:9216
ds_read_b128 a[40:43], v228 offset:10240
ds_read_b128 a[44:47], v228 offset:11264
ds_read_b128 a[48:51], v228 offset:12288
ds_read_b128 a[52:55], v228 offset:13312
ds_read_b128 a[56:59], v228 offset:14336
ds_read_b128 a[60:63], v228 offset:23552
v_mul_u32_u24 v116, 205, v209
v_lshrrev_b32 v116, 12, v116
v_mul_u32_u24 v117, 20, v116
v_sub_u32 v117, v209, v117
v_cmp_eq_u32 vcc, 1, v116
v_lshlrev_b32 v116, 1, v116
v_min_u32 v116, 3, v116
v_mul_u32_u24 v116, 2560, v116
v_lshl_add_u32 v116, v117, 4, v116
v_cndmask_b32 v117, 0, 1.0, vcc
s_mul_i32 s77, s83, 320
s_add_u32 s77, s77, 17634304
s_waitcnt vmcnt(0)
v_cmp_ne_u64 vcc, s[80:81], v[128:129]
s_nop 0
s_and_b64 vcc, vcc, 15
s_cbranch_scc1 .Lk_nb0
s_xor_b32 s84, s94, 524288
buffer_load_dwordx4 a[92:95], v220, s[36:39], s84 offen sc1
buffer_load_dwordx4 a[96:99], v221, s[36:39], s84 offen sc1
s_xor_b32 s84, s94, 528384
buffer_load_dwordx4 a[100:103], v220, s[36:39], s84 offen sc1
buffer_load_dwordx4 a[104:107], v221, s[36:39], s84 offen sc1
s_xor_b32 s84, s94, 532480
buffer_load_dwordx4 a[108:111], v220, s[36:39], s84 offen sc1
buffer_load_dwordx4 a[112:115], v221, s[36:39], s84 offen sc1
s_xor_b32 s84, s94, 536576
buffer_load_dwordx4 a[116:119], v220, s[36:39], s84 offen sc1
buffer_load_dwordx4 a[120:123], v221, s[36:39], s84 offen sc1
s_xor_b32 s84, s94, 540672
buffer_load_dwordx4 a[124:127], v220, s[36:39], s84 offen sc1
buffer_load_dwordx4 v[196:199], v221, s[36:39], s84 offen sc1
s_xor_b32 s84, s94, 544768
buffer_load_dwordx4 v[200:203], v220, s[36:39], s84 offen sc1
buffer_load_dwordx4 v[204:207], v221, s[36:39], s84 offen sc1
s_mov_b64 exec, 15
buffer_atomic_swap_x2 v[210:211], v231, s[36:39], s45 offen sc1
s_mov_b64 exec, -1
s_branch .Lk_nb1
.Lk_nb0:
s_mov_b64 exec, 15
buffer_atomic_swap_x2 v[210:211], v231, s[36:39], s45 offen sc1
s_mov_b64 exec, -1
v_mov_b64 v[128:129], 0
s_mov_b64 exec, 15
buffer_atomic_add_x2 v[128:129], v230, s[36:39], s46 offen sc0 sc1
s_mov_b64 exec, -1
s_waitcnt vmcnt(0)
v_cmp_ne_u64 vcc, s[80:81], v[128:129]
s_nop 0
s_and_b64 vcc, vcc, 15
s_cbranch_scc0 .Lk_p0_ok
s_mov_b32 s89, 0
.Lk_p0:
v_mov_b64 v[128:129], 0
s_mov_b64 exec, 15
buffer_atomic_add_x2 v[128:129], v230, s[36:39], s46 offen sc0 sc1
s_mov_b64 exec, -1
s_waitcnt vmcnt(0)
v_cmp_ne_u64 vcc, s[80:81], v[128:129]
s_nop 0
s_and_b64 vcc, vcc, 15
s_cbranch_scc0 .Lk_p0_ok
s_add_u32 s89, s89, 1
s_cmp_lt_u32 s89, 0x20000
s_cbranch_scc1 .Lk_p0
.Lk_p0_ok:
s_xor_b32 s84, s94, 524288
buffer_load_dwordx4 a[92:95], v220, s[36:39], s84 offen sc1
buffer_load_dwordx4 a[96:99], v221, s[36:39], s84 offen sc1
s_xor_b32 s84, s94, 528384
buffer_load_dwordx4 a[100:103], v220, s[36:39], s84 offen sc1
buffer_load_dwordx4 a[104:107], v221, s[36:39], s84 offen sc1
s_xor_b32 s84, s94, 532480
buffer_load_dwordx4 a[108:111], v220, s[36:39], s84 offen sc1
buffer_load_dwordx4 a[112:115], v221, s[36:39], s84 offen sc1
s_xor_b32 s84, s94, 536576
buffer_load_dwordx4 a[116:119], v220, s[36:39], s84 offen sc1
buffer_load_dwordx4 a[120:123], v221, s[36:39], s84 offen sc1
s_xor_b32 s84, s94, 540672
buffer_load_dwordx4 a[124:127], v220, s[36:39], s84 offen sc1
buffer_load_dwordx4 v[196:199], v221, s[36:39], s84 offen sc1
s_xor_b32 s84, s94, 544768
buffer_load_dwordx4 v[200:203], v220, s[36:39], s84 offen sc1
buffer_load_dwordx4 v[204:207], v221, s[36:39], s84 offen sc1
.Lk_nb1:
buffer_load_dwordx4 v[100:103], v116, s[36:39], s77 offen sc1
s_add_u32 s77, s77, 15360
buffer_load_dwordx4 v[104:107], v116, s[36:39], s77 offen sc1
s_add_u32 s77, s77, 15360
buffer_load_dwordx4 v[108:111], v116, s[36:39], s77 offen sc1
s_add_u32 s77, s77, 15360
buffer_load_dwordx4 v[112:115], v116, s[36:39], s77 offen sc1
buffer_load_dwordx4 v[168:171], v222, s[56:59], s86 offen
buffer_load_dwordx4 v[172:175], v222, s[56:59], s86 offen offset:64
buffer_load_dwordx2 v[176:177], v223, s[56:59], s86 offen offset:128
s_waitcnt vmcnt(18)
v_mfma_f32_16x16x32_bf16 v[60:63], a[128:131], a[92:95], 0
s_xor_b32 s84, s94, 548864
v_mfma_f32_16x16x32_bf16 v[64:67], a[132:135], a[92:95], 0
buffer_load_dwordx4 v[128:131], v220, s[36:39], s84 offen sc1
v_mfma_f32_16x16x32_bf16 v[68:71], a[136:139], a[92:95], 0
v_mfma_f32_16x16x32_bf16 v[72:75], a[140:143], a[92:95], 0
buffer_load_dwordx4 v[132:135], v221, s[36:39], s84 offen sc1
v_mfma_f32_16x16x32_bf16 v[76:79], a[144:147], a[92:95], 0
s_waitcnt vmcnt(19)
v_mfma_f32_16x16x32_bf16 v[56:59], a[144:147], a[96:99], 0
v_mfma_f32_16x16x32_bf16 v[52:55], a[140:143], a[96:99], 0
v_mfma_f32_16x16x32_bf16 v[48:51], a[136:139], a[96:99], 0
s_xor_b32 s84, s94, 552960
v_mfma_f32_16x16x32_bf16 v[44:47], a[132:135], a[96:99], 0
buffer_load_dwordx4 v[136:139], v220, s[36:39], s84 offen sc1
v_mfma_f32_16x16x32_bf16 v[40:43], a[128:131], a[96:99], 0
s_waitcnt vmcnt(19)
v_mfma_f32_16x16x32_bf16 v[60:63], a[148:151], a[100:103], v[60:63]
v_mfma_f32_16x16x32_bf16 v[64:67], a[152:155], a[100:103], v[64:67]
v_mfma_f32_16x16x32_bf16 v[68:71], a[156:159], a[100:103], v[68:71]
v_mfma_f32_16x16x32_bf16 v[72:75], a[160:163], a[100:103], v[72:75]
buffer_load_dwordx4 v[140:143], v221, s[36:39], s84 offen sc1
v_mfma_f32_16x16x32_bf16 v[76:79], a[164:167], a[100:103], v[76:79]
s_waitcnt vmcnt(19)
v_mfma_f32_16x16x32_bf16 v[56:59], a[164:167], a[104:107], v[56:59]
v_mfma_f32_16x16x32_bf16 v[52:55], a[160:163], a[104:107], v[52:55]
v_mfma_f32_16x16x32_bf16 v[48:51], a[156:159], a[104:107], v[48:51]
s_xor_b32 s84, s94, 557056
v_mfma_f32_16x16x32_bf16 v[44:47], a[152:155], a[104:107], v[44:47]
buffer_load_dwordx4 v[144:147], v220, s[36:39], s84 offen sc1
v_mfma_f32_16x16x32_bf16 v[40:43], a[148:151], a[104:107], v[40:43]
s_waitcnt vmcnt(19)
v_mfma_f32_16x16x32_bf16 v[60:63], a[168:171], a[108:111], v[60:63]
v_mfma_f32_16x16x32_bf16 v[64:67], a[172:175], a[108:111], v[64:67]
v_mfma_f32_16x16x32_bf16 v[68:71], a[176:179], a[108:111], v[68:71]
buffer_load_dwordx4 v[148:151], v221, s[36:39], s84 offen sc1
v_mfma_f32_16x16x32_bf16 v[72:75], a[180:183], a[108:111], v[72:75]
v_mfma_f32_16x16x32_bf16 v[76:79], a[184:187], a[108:111], v[76:79]
s_waitcnt vmcnt(19)
v_mfma_f32_16x16x32_bf16 v[56:59], a[184:187], a[112:115], v[56:59]
v_mfma_f32_16x16x32_bf16 v[52:55], a[180:183], a[112:115], v[52:55]
s_xor_b32 s84, s94, 561152
v_mfma_f32_16x16x32_bf16 v[48:51], a[176:179], a[112:115], v[48:51]
buffer_load_dwordx4 v[152:155], v220, s[36:39], s84 offen sc1
v_mfma_f32_16x16x32_bf16 v[44:47], a[172:175], a[112:115], v[44:47]
v_mfma_f32_16x16x32_bf16 v[40:43], a[168:171], a[112:115], v[40:43]
s_waitcnt vmcnt(19)
v_mfma_f32_16x16x32_bf16 v[60:63], a[188:191], a[116:119], v[60:63]
v_mfma_f32_16x16x32_bf16 v[64:67], v[236:239], a[116:119], v[64:67]
buffer_load_dwordx4 v[156:159], v221, s[36:39], s84 offen sc1
v_mfma_f32_16x16x32_bf16 v[68:71], v[240:243], a[116:119], v[68:71]
v_mfma_f32_16x16x32_bf16 v[72:75], v[244:247], a[116:119], v[72:75]
v_mfma_f32_16x16x32_bf16 v[76:79], v[248:251], a[116:119], v[76:79]
s_waitcnt vmcnt(19)
v_mfma_f32_16x16x32_bf16 v[56:59], v[248:251], a[120:123], v[56:59]
s_xor_b32 s84, s94, 565248
v_mfma_f32_16x16x32_bf16 v[52:55], v[244:247], a[120:123], v[52:55]
buffer_load_dwordx4 v[160:163], v220, s[36:39], s84 offen sc1
v_mfma_f32_16x16x32_bf16 v[48:51], v[240:243], a[120:123], v[48:51]
v_mfma_f32_16x16x32_bf16 v[44:47], v[236:239], a[120:123], v[44:47]
v_mfma_f32_16x16x32_bf16 v[40:43], a[188:191], a[120:123], v[40:43]
s_waitcnt vmcnt(19)
v_mfma_f32_16x16x32_bf16 v[60:63], v[252:255], a[124:127], v[60:63]
buffer_load_dwordx4 v[164:167], v221, s[36:39], s84 offen sc1
v_mfma_f32_16x16x32_bf16 v[64:67], a[192:195], a[124:127], v[64:67]
v_mfma_f32_16x16x32_bf16 v[68:71], a[196:199], a[124:127], v[68:71]
v_mfma_f32_16x16x32_bf16 v[72:75], a[200:203], a[124:127], v[72:75]
v_mfma_f32_16x16x32_bf16 v[76:79], a[204:207], a[124:127], v[76:79]
s_waitcnt vmcnt(19)
s_xor_b32 s84, s94, 569344
v_mfma_f32_16x16x32_bf16 v[56:59], a[204:207], v[196:199], v[56:59]
buffer_load_dwordx4 v[184:187], v220, s[36:39], s84 offen sc1
v_mfma_f32_16x16x32_bf16 v[52:55], a[200:203], v[196:199], v[52:55]
v_mfma_f32_16x16x32_bf16 v[48:51], a[196:199], v[196:199], v[48:51]
v_mfma_f32_16x16x32_bf16 v[44:47], a[192:195], v[196:199], v[44:47]
buffer_load_dwordx4 v[188:191], v221, s[36:39], s84 offen sc1
v_mfma_f32_16x16x32_bf16 v[40:43], v[252:255], v[196:199], v[40:43]
s_waitcnt vmcnt(20)
v_mfma_f32_16x16x32_bf16 v[60:63], a[208:211], v[200:203], v[60:63]
v_mfma_f32_16x16x32_bf16 v[64:67], a[212:215], v[200:203], v[64:67]
v_mfma_f32_16x16x32_bf16 v[68:71], a[216:219], v[200:203], v[68:71]
s_xor_b32 s84, s94, 573440
v_mfma_f32_16x16x32_bf16 v[72:75], a[220:223], v[200:203], v[72:75]
buffer_load_dwordx4 v[192:195], v220, s[36:39], s84 offen sc1
v_mfma_f32_16x16x32_bf16 v[76:79], a[224:227], v[200:203], v[76:79]
s_waitcnt vmcnt(20)
v_mfma_f32_16x16x32_bf16 v[56:59], a[224:227], v[204:207], v[56:59]
v_mfma_f32_16x16x32_bf16 v[52:55], a[220:223], v[204:207], v[52:55]
v_mfma_f32_16x16x32_bf16 v[48:51], a[216:219], v[204:207], v[48:51]
v_mfma_f32_16x16x32_bf16 v[44:47], a[212:215], v[204:207], v[44:47]
buffer_load_dwordx4 v[124:127], v221, s[36:39], s84 offen sc1
v_mfma_f32_16x16x32_bf16 v[40:43], a[208:211], v[204:207], v[40:43]
s_waitcnt vmcnt(13)
v_mov_b64 v[216:217], 0
v_mfma_f32_16x16x32_bf16 v[60:63], a[228:231], v[128:131], v[60:63]
s_mov_b64 exec, 255
buffer_atomic_add_x2 v[216:217], v214, s[36:39], s47 offen sc0 sc1
s_mov_b64 exec, -1
v_mfma_f32_16x16x32_bf16 v[64:67], a[232:235], v[128:131], v[64:67]
v_mfma_f32_16x16x32_bf16 v[68:71], a[236:239], v[128:131], v[68:71]
s_xor_b32 s84, s94, 577536
v_mfma_f32_16x16x32_bf16 v[72:75], a[240:243], v[128:131], v[72:75]
buffer_load_dwordx4 a[92:95], v220, s[36:39], s84 offen sc1
v_mfma_f32_16x16x32_bf16 v[76:79], a[244:247], v[128:131], v[76:79]
s_waitcnt vmcnt(14)
v_mfma_f32_16x16x32_bf16 v[56:59], a[244:247], v[132:135], v[56:59]
v_mfma_f32_16x16x32_bf16 v[52:55], a[240:243], v[132:135], v[52:55]
v_mfma_f32_16x16x32_bf16 v[48:51], a[236:239], v[132:135], v[48:51]
buffer_load_dwordx4 a[96:99], v221, s[36:39], s84 offen sc1
v_mfma_f32_16x16x32_bf16 v[44:47], a[232:235], v[132:135], v[44:47]
v_mfma_f32_16x16x32_bf16 v[40:43], a[228:231], v[132:135], v[40:43]
s_waitcnt vmcnt(14) lgkmcnt(13)
v_mfma_f32_16x16x32_bf16 v[60:63], a[248:251], v[136:139], v[60:63]
v_mfma_f32_16x16x32_bf16 v[64:67], a[252:255], v[136:139], v[64:67]
s_xor_b32 s84, s94, 581632
v_mfma_f32_16x16x32_bf16 v[68:71], a[0:3], v[136:139], v[68:71]
buffer_load_dwordx4 a[100:103], v220, s[36:39], s84 offen sc1
v_mfma_f32_16x16x32_bf16 v[72:75], a[4:7], v[136:139], v[72:75]
v_mfma_f32_16x16x32_bf16 v[76:79], a[8:11], v[136:139], v[76:79]
s_waitcnt vmcnt(14)
v_mfma_f32_16x16x32_bf16 v[56:59], a[8:11], v[140:143], v[56:59]
v_mfma_f32_16x16x32_bf16 v[52:55], a[4:7], v[140:143], v[52:55]
buffer_load_dwordx4 a[104:107], v221, s[36:39], s84 offen sc1
v_mfma_f32_16x16x32_bf16 v[48:51], a[0:3], v[140:143], v[48:51]
v_mfma_f32_16x16x32_bf16 v[44:47], a[252:255], v[140:143], v[44:47]
v_mfma_f32_16x16x32_bf16 v[40:43], a[248:251], v[140:143], v[40:43]
s_waitcnt vmcnt(14) lgkmcnt(8)
v_pk_add_f32 v[100:101], v[100:101], v[104:105]
v_mfma_f32_16x16x32_bf16 v[60:63], a[12:15], v[144:147], v[60:63]
v_pk_add_f32 v[100:101], v[100:101], v[108:109]
s_xor_b32 s84, s94, 585728
v_mfma_f32_16x16x32_bf16 v[64:67], a[16:19], v[144:147], v[64:67]
buffer_load_dwordx4 a[108:111], v220, s[36:39], s84 offen sc1
v_mfma_f32_16x16x32_bf16 v[68:71], a[20:23], v[144:147], v[68:71]
v_pk_add_f32 v[100:101], v[100:101], v[112:113]
v_pk_add_f32 v[102:103], v[102:103], v[106:107]
v_mfma_f32_16x16x32_bf16 v[72:75], a[24:27], v[144:147], v[72:75]
v_pk_add_f32 v[102:103], v[102:103], v[110:111]
v_pk_add_f32 v[102:103], v[102:103], v[114:115]
v_mfma_f32_16x16x32_bf16 v[76:79], a[28:31], v[144:147], v[76:79]
v_cvt_pk_bf16_f32 v104, v100, v101
v_lshlrev_b32 v100, 16, v104
s_waitcnt vmcnt(14)
v_mfma_f32_16x16x32_bf16 v[56:59], a[28:31], v[148:151], v[56:59]
buffer_load_dwordx4 a[112:115], v221, s[36:39], s84 offen sc1
v_mfma_f32_16x16x32_bf16 v[52:55], a[24:27], v[148:151], v[52:55]
v_and_b32 v101, 0xffff0000, v104
v_cvt_pk_bf16_f32 v104, v102, v103
v_mfma_f32_16x16x32_bf16 v[48:51], a[20:23], v[148:151], v[48:51]
v_lshlrev_b32 v102, 16, v104
v_and_b32 v103, 0xffff0000, v104
v_mfma_f32_16x16x32_bf16 v[44:47], a[16:19], v[148:151], v[44:47]
v_add_f32 v100, v100, v117
v_add_f32 v101, v101, v117
v_mfma_f32_16x16x32_bf16 v[40:43], a[12:15], v[148:151], v[40:43]
v_add_f32 v102, v102, v117
v_add_f32 v103, v103, v117
s_waitcnt vmcnt(14) lgkmcnt(3)
v_mfma_f32_16x16x32_bf16 v[60:63], a[32:35], v[152:155], v[60:63]
ds_read_b128 a[116:119], v228 offset:15360
v_cvt_pk_bf16_f32 v104, v100, v101
v_mfma_f32_16x16x32_bf16 v[64:67], a[36:39], v[152:155], v[64:67]
ds_read_b128 a[120:123], v228 offset:16384
v_lshlrev_b32 v100, 16, v104
v_mfma_f32_16x16x32_bf16 v[68:71], a[40:43], v[152:155], v[68:71]
ds_read_b128 a[124:127], v228 offset:17408
v_and_b32 v101, 0xffff0000, v104
v_mfma_f32_16x16x32_bf16 v[72:75], a[44:47], v[152:155], v[72:75]
v_cvt_pk_bf16_f32 v104, v102, v103
v_lshlrev_b32 v102, 16, v104
v_mfma_f32_16x16x32_bf16 v[76:79], a[48:51], v[152:155], v[76:79]
v_and_b32 v103, 0xffff0000, v104
v_mov_b64 v[180:181], v[100:101]
s_waitcnt vmcnt(13)
v_mfma_f32_16x16x32_bf16 v[56:59], a[48:51], v[156:159], v[56:59]
v_mov_b64 v[182:183], v[102:103]
v_mfma_f32_16x16x32_bf16 v[52:55], a[44:47], v[156:159], v[52:55]
ds_write_b128 v228, v[100:103] offset:23552
v_mfma_f32_16x16x32_bf16 v[48:51], a[40:43], v[156:159], v[48:51]
v_mfma_f32_16x16x32_bf16 v[44:47], a[36:39], v[156:159], v[44:47]
v_mfma_f32_16x16x32_bf16 v[40:43], a[32:35], v[156:159], v[40:43]
s_waitcnt vmcnt(12) lgkmcnt(1)
v_mfma_f32_16x16x32_bf16 v[60:63], a[52:55], v[160:163], v[60:63]
ds_read_b128 v[196:199], v228 offset:18432
v_mfma_f32_16x16x32_bf16 v[64:67], a[56:59], v[160:163], v[64:67]
ds_read_b128 v[200:203], v228 offset:19456
v_mfma_f32_16x16x32_bf16 v[68:71], a[116:119], v[160:163], v[68:71]
ds_read_b128 v[204:207], v228 offset:20480
v_mfma_f32_16x16x32_bf16 v[72:75], a[120:123], v[160:163], v[72:75]
ds_read_b128 v[128:131], v228 offset:21504
v_mfma_f32_16x16x32_bf16 v[76:79], a[124:127], v[160:163], v[76:79]
ds_read_b128 v[132:135], v228 offset:22528
s_waitcnt vmcnt(11)
v_mfma_f32_16x16x32_bf16 v[56:59], a[124:127], v[164:167], v[56:59]
v_mfma_f32_16x16x32_bf16 v[52:55], a[120:123], v[164:167], v[52:55]
v_mfma_f32_16x16x32_bf16 v[48:51], a[116:119], v[164:167], v[48:51]
v_mfma_f32_16x16x32_bf16 v[44:47], a[56:59], v[164:167], v[44:47]
v_mfma_f32_16x16x32_bf16 v[40:43], a[52:55], v[164:167], v[40:43]
s_waitcnt vmcnt(6)
v_cmp_ne_u64 vcc, s[80:81], v[216:217]
s_nop 0
s_and_b64 vcc, vcc, 255
s_cbranch_scc0 .Lk_p1_ok
s_mov_b32 s89, 0
.Lk_p1:
v_mov_b64 v[216:217], 0
s_mov_b64 exec, 255
buffer_atomic_add_x2 v[216:217], v214, s[36:39], s47 offen sc0 sc1
s_mov_b64 exec, -1
s_waitcnt vmcnt(0)
v_cmp_ne_u64 vcc, s[80:81], v[216:217]
s_nop 0
s_and_b64 vcc, vcc, 255
s_cbranch_scc0 .Lk_p1_ok
s_add_u32 s89, s89, 1
s_cmp_lt_u32 s89, 0x20000
s_cbranch_scc1 .Lk_p1
.Lk_p1_ok:
buffer_load_dwordx4 v[136:139], v218, s[36:39], s84 offen sc1
buffer_load_dwordx4 v[140:143], v219, s[36:39], s84 offen sc1
s_xor_b32 s84, s94, 581632
buffer_load_dwordx4 v[144:147], v218, s[36:39], s84 offen sc1
buffer_load_dwordx4 v[148:151], v219, s[36:39], s84 offen sc1
s_xor_b32 s84, s94, 577536
buffer_load_dwordx4 v[152:155], v218, s[36:39], s84 offen sc1
buffer_load_dwordx4 v[156:159], v219, s[36:39], s84 offen sc1
s_waitcnt lgkmcnt(0)
v_mfma_f32_16x16x32_bf16 v[60:63], v[196:199], v[184:187], v[60:63]
ds_read_b128 v[160:163], v232 offset:0
v_mfma_f32_16x16x32_bf16 v[64:67], v[200:203], v[184:187], v[64:67]
ds_read_b128 a[124:127], v232 offset:1024
v_mfma_f32_16x16x32_bf16 v[68:71], v[204:207], v[184:187], v[68:71]
ds_read_b128 a[120:123], v232 offset:2048
v_mfma_f32_16x16x32_bf16 v[72:75], v[128:131], v[184:187], v[72:75]
ds_read_b128 a[116:119], v232 offset:3072
v_mfma_f32_16x16x32_bf16 v[76:79], v[132:135], v[184:187], v[76:79]
v_mfma_f32_16x16x32_bf16 v[56:59], v[132:135], v[188:191], v[56:59]
v_mfma_f32_16x16x32_bf16 v[52:55], v[128:131], v[188:191], v[52:55]
v_mfma_f32_16x16x32_bf16 v[48:51], v[204:207], v[188:191], v[48:51]
s_xor_b32 s84, s94, 573440
v_mfma_f32_16x16x32_bf16 v[44:47], v[200:203], v[188:191], v[44:47]
buffer_load_dwordx4 v[164:167], v218, s[36:39], s84 offen sc1
v_mfma_f32_16x16x32_bf16 v[40:43], v[196:199], v[188:191], v[40:43]
s_waitcnt lgkmcnt(0)
v_mfma_f32_16x16x32_bf16 v[60:63], a[60:63], v[192:195], v[60:63]
ds_read_b128 v[184:187], v232 offset:16384
v_mfma_f32_16x16x32_bf16 v[64:67], v[160:163], v[192:195], v[64:67]
ds_read_b128 v[132:135], v232 offset:17408
v_mfma_f32_16x16x32_bf16 v[68:71], a[124:127], v[192:195], v[68:71]
ds_read_b128 v[128:131], v232 offset:18432
v_mfma_f32_16x16x32_bf16 v[72:75], a[120:123], v[192:195], v[72:75]
ds_read_b128 v[204:207], v232 offset:19456
v_mfma_f32_16x16x32_bf16 v[76:79], a[116:119], v[192:195], v[76:79]
ds_read_b128 v[200:203], v232 offset:32768
v_mfma_f32_16x16x32_bf16 v[56:59], a[116:119], v[124:127], v[56:59]
v_mfma_f32_16x16x32_bf16 v[52:55], a[120:123], v[124:127], v[52:55]
v_mfma_f32_16x16x32_bf16 v[48:51], a[124:127], v[124:127], v[48:51]
buffer_load_dwordx4 v[188:191], v219, s[36:39], s84 offen sc1
v_mfma_f32_16x16x32_bf16 v[44:47], v[160:163], v[124:127], v[44:47]
v_mfma_f32_16x16x32_bf16 v[40:43], a[60:63], v[124:127], v[40:43]
s_waitcnt vmcnt(13) lgkmcnt(0)
v_mfma_f32_16x16x32_bf16 v[60:63], v[184:187], a[92:95], v[60:63]
ds_read_b128 v[196:199], v232 offset:33792
v_mfma_f32_16x16x32_bf16 v[64:67], v[132:135], a[92:95], v[64:67]
ds_read_b128 v[192:195], v232 offset:34816
v_mfma_f32_16x16x32_bf16 v[68:71], v[128:131], a[92:95], v[68:71]
ds_read_b128 a[116:119], v232 offset:35840
v_mfma_f32_16x16x32_bf16 v[72:75], v[204:207], a[92:95], v[72:75]
v_mfma_f32_16x16x32_bf16 v[76:79], v[200:203], a[92:95], v[76:79]
s_waitcnt vmcnt(12)
s_xor_b32 s84, s94, 569344
v_mfma_f32_16x16x32_bf16 v[56:59], v[200:203], a[96:99], v[56:59]
buffer_load_dwordx4 a[120:123], v218, s[36:39], s84 offen sc1
v_mfma_f32_16x16x32_bf16 v[52:55], v[204:207], a[96:99], v[52:55]
buffer_load_dwordx4 a[124:127], v219, s[36:39], s84 offen sc1
v_mfma_f32_16x16x32_bf16 v[48:51], v[128:131], a[96:99], v[48:51]
v_mfma_f32_16x16x32_bf16 v[44:47], v[132:135], a[96:99], v[44:47]
v_mfma_f32_16x16x32_bf16 v[40:43], v[184:187], a[96:99], v[40:43]
s_waitcnt vmcnt(13) lgkmcnt(0)
v_mfma_f32_16x16x32_bf16 v[60:63], v[196:199], a[100:103], v[60:63]
s_xor_b32 s84, s94, 565248
v_mfma_f32_16x16x32_bf16 v[64:67], v[192:195], a[100:103], v[64:67]
buffer_load_dwordx4 v[160:163], v218, s[36:39], s84 offen sc1
v_mfma_f32_16x16x32_bf16 v[68:71], a[116:119], a[100:103], v[68:71]
v_mfma_f32_16x16x32_bf16 v[72:75], a[64:67], a[100:103], v[72:75]
v_mfma_f32_16x16x32_bf16 v[76:79], a[68:71], a[100:103], v[76:79]
s_waitcnt vmcnt(13)
v_mfma_f32_16x16x32_bf16 v[56:59], a[68:71], a[104:107], v[56:59]
buffer_load_dwordx4 v[124:127], v219, s[36:39], s84 offen sc1
v_mfma_f32_16x16x32_bf16 v[52:55], a[64:67], a[104:107], v[52:55]
v_mfma_f32_16x16x32_bf16 v[48:51], a[116:119], a[104:107], v[48:51]
v_mfma_f32_16x16x32_bf16 v[44:47], v[192:195], a[104:107], v[44:47]
v_mfma_f32_16x16x32_bf16 v[40:43], v[196:199], a[104:107], v[40:43]
s_waitcnt vmcnt(13)
s_xor_b32 s84, s94, 561152
v_mfma_f32_16x16x32_bf16 v[60:63], a[72:75], a[108:111], v[60:63]
buffer_load_dwordx4 a[92:95], v218, s[36:39], s84 offen sc1
v_mfma_f32_16x16x32_bf16 v[64:67], a[76:79], a[108:111], v[64:67]
v_mfma_f32_16x16x32_bf16 v[68:71], a[80:83], a[108:111], v[68:71]
v_mfma_f32_16x16x32_bf16 v[72:75], a[84:87], a[108:111], v[72:75]
buffer_load_dwordx4 v[200:203], v219, s[36:39], s84 offen sc1
v_mfma_f32_16x16x32_bf16 v[76:79], a[88:91], a[108:111], v[76:79]
s_waitcnt vmcnt(14)
v_mfma_f32_16x16x32_bf16 v[56:59], a[88:91], a[112:115], v[56:59]
v_mfma_f32_16x16x32_bf16 v[52:55], a[84:87], a[112:115], v[52:55]
v_mfma_f32_16x16x32_bf16 v[48:51], a[80:83], a[112:115], v[48:51]
s_xor_b32 s84, s94, 557056
v_mfma_f32_16x16x32_bf16 v[44:47], a[76:79], a[112:115], v[44:47]
buffer_load_dwordx4 v[204:207], v218, s[36:39], s84 offen sc1
v_mfma_f32_16x16x32_bf16 v[40:43], a[72:75], a[112:115], v[40:43]
s_waitcnt vmcnt(14)
v_mfma_f32_16x16x32_bf16 v[20:23], a[72:75], v[136:139], 0
ds_read_b128 v[128:131], v232 offset:33792
v_mfma_f32_16x16x32_bf16 v[24:27], a[76:79], v[136:139], 0
ds_read_b128 v[132:135], v232 offset:34816
ds_read_b128 a[96:99], v232 offset:35840
v_mfma_f32_16x16x32_bf16 v[28:31], a[80:83], v[136:139], 0
buffer_load_dwordx4 v[184:187], v219, s[36:39], s84 offen sc1
v_mfma_f32_16x16x32_bf16 v[32:35], a[84:87], v[136:139], 0
ds_write_b128 v228, v[60:63] offset:10240
v_mfma_f32_16x16x32_bf16 v[36:39], a[88:91], v[136:139], 0
s_waitcnt vmcnt(14)
v_mfma_f32_16x16x32_bf16 v[96:99], a[88:91], v[140:143], 0
v_mfma_f32_16x16x32_bf16 v[92:95], a[84:87], v[140:143], 0
ds_write_b128 v228, v[64:67] offset:11264
v_mfma_f32_16x16x32_bf16 v[88:91], a[80:83], v[140:143], 0
s_xor_b32 s84, s94, 552960
v_mfma_f32_16x16x32_bf16 v[84:87], a[76:79], v[140:143], 0
buffer_load_dwordx4 a[100:103], v218, s[36:39], s84 offen sc1
v_mfma_f32_16x16x32_bf16 v[80:83], a[72:75], v[140:143], 0
s_waitcnt vmcnt(14) lgkmcnt(2)
v_mfma_f32_16x16x32_bf16 v[20:23], v[128:131], v[144:147], v[20:23]
ds_read_b128 a[116:119], v232 offset:16384
v_mfma_f32_16x16x32_bf16 v[24:27], v[132:135], v[144:147], v[24:27]
ds_read_b128 v[192:195], v232 offset:17408
v_mfma_f32_16x16x32_bf16 v[28:31], a[96:99], v[144:147], v[28:31]
buffer_load_dwordx4 a[104:107], v219, s[36:39], s84 offen sc1
v_mfma_f32_16x16x32_bf16 v[32:35], a[64:67], v[144:147], v[32:35]
ds_read_b128 v[196:199], v232 offset:18432
v_mfma_f32_16x16x32_bf16 v[36:39], a[68:71], v[144:147], v[36:39]
ds_read_b128 a[108:111], v232 offset:19456
s_waitcnt vmcnt(14)
v_mfma_f32_16x16x32_bf16 v[96:99], a[68:71], v[148:151], v[96:99]
ds_read_b128 a[112:115], v232 offset:32768
v_mfma_f32_16x16x32_bf16 v[92:95], a[64:67], v[148:151], v[92:95]
ds_write_b128 v228, v[68:71] offset:12288
v_mfma_f32_16x16x32_bf16 v[88:91], a[96:99], v[148:151], v[88:91]
s_xor_b32 s84, s94, 548864
v_mfma_f32_16x16x32_bf16 v[84:87], v[132:135], v[148:151], v[84:87]
buffer_load_dwordx4 v[136:139], v218, s[36:39], s84 offen sc1
v_mfma_f32_16x16x32_bf16 v[80:83], v[128:131], v[148:151], v[80:83]
s_waitcnt vmcnt(14) lgkmcnt(1)
v_mfma_f32_16x16x32_bf16 v[20:23], a[116:119], v[152:155], v[20:23]
ds_read_b128 v[140:143], v232 offset:0
v_mfma_f32_16x16x32_bf16 v[24:27], v[192:195], v[152:155], v[24:27]
ds_read_b128 v[144:147], v232 offset:1024
v_mfma_f32_16x16x32_bf16 v[28:31], v[196:199], v[152:155], v[28:31]
ds_read_b128 a[96:99], v232 offset:2048
ds_read_b128 v[132:135], v232 offset:3072
v_mfma_f32_16x16x32_bf16 v[32:35], a[108:111], v[152:155], v[32:35]
ds_write_b128 v228, v[72:75] offset:13312
v_mfma_f32_16x16x32_bf16 v[36:39], a[112:115], v[152:155], v[36:39]
s_waitcnt vmcnt(13)
v_mfma_f32_16x16x32_bf16 v[96:99], a[112:115], v[156:159], v[96:99]
v_mfma_f32_16x16x32_bf16 v[92:95], a[108:111], v[156:159], v[92:95]
v_mfma_f32_16x16x32_bf16 v[88:91], v[196:199], v[156:159], v[88:91]
buffer_load_dwordx4 v[148:151], v219, s[36:39], s84 offen sc1
v_mfma_f32_16x16x32_bf16 v[84:87], v[192:195], v[156:159], v[84:87]
ds_write_b128 v228, v[76:79] offset:14336
v_mfma_f32_16x16x32_bf16 v[80:83], a[116:119], v[156:159], v[80:83]
s_waitcnt vmcnt(13) lgkmcnt(2)
v_mfma_f32_16x16x32_bf16 v[20:23], a[60:63], v[164:167], v[20:23]
ds_read_b128 v[128:131], v228 offset:18432
v_mfma_f32_16x16x32_bf16 v[24:27], v[140:143], v[164:167], v[24:27]
ds_read_b128 v[152:155], v228 offset:19456
s_xor_b32 s84, s94, 544768
v_mfma_f32_16x16x32_bf16 v[28:31], v[144:147], v[164:167], v[28:31]
buffer_load_dwordx4 a[112:115], v218, s[36:39], s84 offen sc1
v_mfma_f32_16x16x32_bf16 v[32:35], a[96:99], v[164:167], v[32:35]
ds_read_b128 a[108:111], v228 offset:20480
v_mfma_f32_16x16x32_bf16 v[36:39], v[132:135], v[164:167], v[36:39]
ds_read_b128 v[196:199], v228 offset:21504
s_waitcnt vmcnt(13)
v_mfma_f32_16x16x32_bf16 v[96:99], v[132:135], v[188:191], v[96:99]
ds_read_b128 v[192:195], v228 offset:22528
v_mfma_f32_16x16x32_bf16 v[92:95], a[96:99], v[188:191], v[92:95]
ds_write_b128 v228, v[40:43] offset:5120
v_mfma_f32_16x16x32_bf16 v[88:91], v[144:147], v[188:191], v[88:91]
buffer_load_dwordx4 v[156:159], v219, s[36:39], s84 offen sc1
v_mfma_f32_16x16x32_bf16 v[84:87], v[140:143], v[188:191], v[84:87]
v_mfma_f32_16x16x32_bf16 v[80:83], a[60:63], v[188:191], v[80:83]
s_waitcnt vmcnt(13) lgkmcnt(1)
v_mfma_f32_16x16x32_bf16 v[20:23], v[128:131], a[120:123], v[20:23]
ds_read_b128 a[116:119], v228 offset:15360
s_xor_b32 s84, s94, 540672
v_mfma_f32_16x16x32_bf16 v[24:27], v[152:155], a[120:123], v[24:27]
buffer_load_dwordx4 v[164:167], v218, s[36:39], s84 offen sc1
v_mfma_f32_16x16x32_bf16 v[28:31], a[108:111], a[120:123], v[28:31]
ds_read_b128 v[132:135], v228 offset:16384
ds_read_b128 a[96:99], v228 offset:17408
v_mfma_f32_16x16x32_bf16 v[32:35], v[196:199], a[120:123], v[32:35]
ds_write_b128 v228, v[44:47] offset:6144
v_mfma_f32_16x16x32_bf16 v[36:39], v[192:195], a[120:123], v[36:39]
s_waitcnt vmcnt(13)
v_mfma_f32_16x16x32_bf16 v[96:99], v[192:195], a[124:127], v[96:99]
s_xor_b32 s84, s94, 536576
v_mfma_f32_16x16x32_bf16 v[92:95], v[196:199], a[124:127], v[92:95]
buffer_load_dwordx4 v[144:147], v218, s[36:39], s84 offen sc1
v_mfma_f32_16x16x32_bf16 v[88:91], a[108:111], a[124:127], v[88:91]
v_mfma_f32_16x16x32_bf16 v[84:87], v[152:155], a[124:127], v[84:87]
ds_write_b128 v228, v[48:51] offset:7168
v_mfma_f32_16x16x32_bf16 v[80:83], v[128:131], a[124:127], v[80:83]
s_waitcnt vmcnt(13) lgkmcnt(2)
s_xor_b32 s84, s94, 532480
v_mfma_f32_16x16x32_bf16 v[20:23], a[52:55], v[160:163], v[20:23]
buffer_load_dwordx4 v[140:143], v218, s[36:39], s84 offen sc1
v_mfma_f32_16x16x32_bf16 v[24:27], a[56:59], v[160:163], v[24:27]
ds_write_b128 v228, v[52:55] offset:8192
v_mfma_f32_16x16x32_bf16 v[28:31], a[116:119], v[160:163], v[28:31]
s_xor_b32 s84, s94, 528384
v_mfma_f32_16x16x32_bf16 v[32:35], v[132:135], v[160:163], v[32:35]
buffer_load_dwordx4 v[188:191], v218, s[36:39], s84 offen sc1
v_mfma_f32_16x16x32_bf16 v[36:39], a[96:99], v[160:163], v[36:39]
s_waitcnt vmcnt(14)
v_mfma_f32_16x16x32_bf16 v[96:99], a[96:99], v[124:127], v[96:99]
v_mfma_f32_16x16x32_bf16 v[92:95], v[132:135], v[124:127], v[92:95]
ds_write_b128 v228, v[56:59] offset:9216
v_mfma_f32_16x16x32_bf16 v[88:91], a[116:119], v[124:127], v[88:91]
s_xor_b32 s84, s94, 524288
v_mfma_f32_16x16x32_bf16 v[84:87], a[56:59], v[124:127], v[84:87]
buffer_load_dwordx4 a[120:123], v218, s[36:39], s84 offen sc1
v_mfma_f32_16x16x32_bf16 v[80:83], a[52:55], v[124:127], v[80:83]
s_waitcnt vmcnt(14)
v_mfma_f32_16x16x32_bf16 v[20:23], a[32:35], a[92:95], v[20:23]
v_mfma_f32_16x16x32_bf16 v[24:27], a[36:39], a[92:95], v[24:27]
v_mfma_f32_16x16x32_bf16 v[28:31], a[40:43], a[92:95], v[28:31]
s_xor_b32 s84, s94, 540672
v_mfma_f32_16x16x32_bf16 v[32:35], a[44:47], a[92:95], v[32:35]
buffer_load_dwordx4 v[192:195], v219, s[36:39], s84 offen sc1
v_mfma_f32_16x16x32_bf16 v[36:39], a[48:51], a[92:95], v[36:39]
s_waitcnt vmcnt(14)
v_mfma_f32_16x16x32_bf16 v[96:99], a[48:51], v[200:203], v[96:99]
v_mfma_f32_16x16x32_bf16 v[92:95], a[44:47], v[200:203], v[92:95]
v_mfma_f32_16x16x32_bf16 v[88:91], a[40:43], v[200:203], v[88:91]
s_xor_b32 s84, s94, 536576
v_mfma_f32_16x16x32_bf16 v[84:87], a[36:39], v[200:203], v[84:87]
buffer_load_dwordx4 v[196:199], v219, s[36:39], s84 offen sc1
v_mfma_f32_16x16x32_bf16 v[80:83], a[32:35], v[200:203], v[80:83]
s_waitcnt vmcnt(14)
v_mfma_f32_16x16x32_bf16 v[20:23], a[12:15], v[204:207], v[20:23]
v_mfma_f32_16x16x32_bf16 v[24:27], a[16:19], v[204:207], v[24:27]
s_xor_b32 s84, s94, 532480
v_mfma_f32_16x16x32_bf16 v[28:31], a[20:23], v[204:207], v[28:31]
buffer_load_dwordx4 a[108:111], v219, s[36:39], s84 offen sc1
v_mfma_f32_16x16x32_bf16 v[32:35], a[24:27], v[204:207], v[32:35]
v_mfma_f32_16x16x32_bf16 v[36:39], a[28:31], v[204:207], v[36:39]
s_waitcnt vmcnt(14)
v_mfma_f32_16x16x32_bf16 v[96:99], a[28:31], v[184:187], v[96:99]
v_mfma_f32_16x16x32_bf16 v[92:95], a[24:27], v[184:187], v[92:95]
s_xor_b32 s84, s94, 528384
v_mfma_f32_16x16x32_bf16 v[88:91], a[20:23], v[184:187], v[88:91]
buffer_load_dwordx4 v[152:155], v219, s[36:39], s84 offen sc1
v_mfma_f32_16x16x32_bf16 v[84:87], a[16:19], v[184:187], v[84:87]
v_mfma_f32_16x16x32_bf16 v[80:83], a[12:15], v[184:187], v[80:83]
s_waitcnt vmcnt(14)
v_mfma_f32_16x16x32_bf16 v[20:23], a[248:251], a[100:103], v[20:23]
s_xor_b32 s84, s94, 524288
v_mfma_f32_16x16x32_bf16 v[24:27], a[252:255], a[100:103], v[24:27]
buffer_load_dwordx4 a[124:127], v219, s[36:39], s84 offen sc1
v_mfma_f32_16x16x32_bf16 v[28:31], a[0:3], a[100:103], v[28:31]
v_mfma_f32_16x16x32_bf16 v[32:35], a[4:7], a[100:103], v[32:35]
v_mfma_f32_16x16x32_bf16 v[36:39], a[8:11], a[100:103], v[36:39]
s_waitcnt vmcnt(14)
v_mfma_f32_16x16x32_bf16 v[96:99], a[8:11], a[104:107], v[96:99]
s_xor_b32 s84, s94, 0
v_mfma_f32_16x16x32_bf16 v[92:95], a[4:7], a[104:107], v[92:95]
buffer_load_dwordx4 v[128:131], v218, s[36:39], s84 offen sc1
v_mfma_f32_16x16x32_bf16 v[88:91], a[0:3], a[104:107], v[88:91]
v_mfma_f32_16x16x32_bf16 v[84:87], a[252:255], a[104:107], v[84:87]
v_mfma_f32_16x16x32_bf16 v[80:83], a[248:251], a[104:107], v[80:83]
s_waitcnt vmcnt(14)
v_mfma_f32_16x16x32_bf16 v[20:23], a[228:231], v[136:139], v[20:23]
buffer_load_dwordx4 v[160:163], v219, s[36:39], s84 offen sc1
v_mfma_f32_16x16x32_bf16 v[24:27], a[232:235], v[136:139], v[24:27]
v_mfma_f32_16x16x32_bf16 v[28:31], a[236:239], v[136:139], v[28:31]
v_mfma_f32_16x16x32_bf16 v[32:35], a[240:243], v[136:139], v[32:35]
v_mfma_f32_16x16x32_bf16 v[36:39], a[244:247], v[136:139], v[36:39]
s_waitcnt vmcnt(14)
v_mfma_f32_16x16x32_bf16 v[96:99], a[244:247], v[148:151], v[96:99]
buffer_load_dwordx4 a[96:99], v220, s[36:39], s84 offen sc1
v_mfma_f32_16x16x32_bf16 v[92:95], a[240:243], v[148:151], v[92:95]
v_mfma_f32_16x16x32_bf16 v[88:91], a[236:239], v[148:151], v[88:91]
v_mfma_f32_16x16x32_bf16 v[84:87], a[232:235], v[148:151], v[84:87]
buffer_load_dwordx4 v[132:135], v221, s[36:39], s84 offen sc1
v_mfma_f32_16x16x32_bf16 v[80:83], a[228:231], v[148:151], v[80:83]
s_waitcnt vmcnt(15)
v_mfma_f32_16x16x32_bf16 v[20:23], a[208:211], a[112:115], v[20:23]
v_mfma_f32_16x16x32_bf16 v[24:27], a[212:215], a[112:115], v[24:27]
v_mfma_f32_16x16x32_bf16 v[28:31], a[216:219], a[112:115], v[28:31]
s_xor_b32 s84, s94, 4096
v_mfma_f32_16x16x32_bf16 v[32:35], a[220:223], a[112:115], v[32:35]
buffer_load_dwordx4 a[116:119], v218, s[36:39], s84 offen sc1
v_mfma_f32_16x16x32_bf16 v[36:39], a[224:227], a[112:115], v[36:39]
s_waitcnt vmcnt(15)
v_mfma_f32_16x16x32_bf16 v[96:99], a[224:227], v[156:159], v[96:99]
v_mfma_f32_16x16x32_bf16 v[92:95], a[220:223], v[156:159], v[92:95]
v_mfma_f32_16x16x32_bf16 v[88:91], a[216:219], v[156:159], v[88:91]
v_mfma_f32_16x16x32_bf16 v[84:87], a[212:215], v[156:159], v[84:87]
buffer_load_dwordx4 v[124:127], v219, s[36:39], s84 offen sc1
v_mfma_f32_16x16x32_bf16 v[80:83], a[208:211], v[156:159], v[80:83]
s_waitcnt vmcnt(15)
v_mfma_f32_16x16x32_bf16 v[20:23], v[252:255], v[164:167], v[20:23]
v_mfma_f32_16x16x32_bf16 v[24:27], a[192:195], v[164:167], v[24:27]
v_mfma_f32_16x16x32_bf16 v[28:31], a[196:199], v[164:167], v[28:31]
v_mfma_f32_16x16x32_bf16 v[32:35], a[200:203], v[164:167], v[32:35]
buffer_load_dwordx4 a[92:95], v220, s[36:39], s84 offen sc1
v_mfma_f32_16x16x32_bf16 v[36:39], a[204:207], v[164:167], v[36:39]
s_waitcnt vmcnt(15)
v_mfma_f32_16x16x32_bf16 v[20:23], a[188:191], v[144:147], v[20:23]
v_mfma_f32_16x16x32_bf16 v[24:27], v[236:239], v[144:147], v[24:27]
v_mfma_f32_16x16x32_bf16 v[28:31], v[240:243], v[144:147], v[28:31]
buffer_load_dwordx4 v[200:203], v221, s[36:39], s84 offen sc1
v_mfma_f32_16x16x32_bf16 v[32:35], v[244:247], v[144:147], v[32:35]
v_mfma_f32_16x16x32_bf16 v[36:39], v[248:251], v[144:147], v[36:39]
s_waitcnt vmcnt(15)
v_mfma_f32_16x16x32_bf16 v[20:23], a[168:171], v[140:143], v[20:23]
v_mfma_f32_16x16x32_bf16 v[24:27], a[172:175], v[140:143], v[24:27]
s_xor_b32 s84, s94, 8192
v_mfma_f32_16x16x32_bf16 v[28:31], a[176:179], v[140:143], v[28:31]
buffer_load_dwordx4 v[204:207], v218, s[36:39], s84 offen sc1
v_mfma_f32_16x16x32_bf16 v[32:35], a[180:183], v[140:143], v[32:35]
v_mfma_f32_16x16x32_bf16 v[36:39], a[184:187], v[140:143], v[36:39]
s_waitcnt vmcnt(15)
v_mfma_f32_16x16x32_bf16 v[20:23], a[148:151], v[188:191], v[20:23]
v_mfma_f32_16x16x32_bf16 v[24:27], a[152:155], v[188:191], v[24:27]
buffer_load_dwordx4 v[184:187], v219, s[36:39], s84 offen sc1
v_mfma_f32_16x16x32_bf16 v[28:31], a[156:159], v[188:191], v[28:31]
v_mfma_f32_16x16x32_bf16 v[32:35], a[160:163], v[188:191], v[32:35]
v_mfma_f32_16x16x32_bf16 v[36:39], a[164:167], v[188:191], v[36:39]
s_waitcnt vmcnt(15)
v_mfma_f32_16x16x32_bf16 v[20:23], a[128:131], a[120:123], v[20:23]
v_mfma_f32_16x16x32_bf16 v[24:27], a[132:135], a[120:123], v[24:27]
buffer_load_dwordx4 a[100:103], v220, s[36:39], s84 offen sc1
v_mfma_f32_16x16x32_bf16 v[28:31], a[136:139], a[120:123], v[28:31]
v_mfma_f32_16x16x32_bf16 v[32:35], a[140:143], a[120:123], v[32:35]
v_mfma_f32_16x16x32_bf16 v[36:39], a[144:147], a[120:123], v[36:39]
s_waitcnt vmcnt(15)
v_mfma_f32_16x16x32_bf16 v[80:83], v[252:255], v[192:195], v[80:83]
buffer_load_dwordx4 a[104:107], v221, s[36:39], s84 offen sc1
v_mfma_f32_16x16x32_bf16 v[84:87], a[192:195], v[192:195], v[84:87]
v_mfma_f32_16x16x32_bf16 v[88:91], a[196:199], v[192:195], v[88:91]
v_mfma_f32_16x16x32_bf16 v[92:95], a[200:203], v[192:195], v[92:95]
ds_write_b128 v228, v[20:23] offset:0
v_mfma_f32_16x16x32_bf16 v[96:99], a[204:207], v[192:195], v[96:99]
s_waitcnt vmcnt(15)
s_xor_b32 s84, s94, 12288
v_mfma_f32_16x16x32_bf16 v[80:83], a[188:191], v[196:199], v[80:83]
buffer_load_dwordx4 v[136:139], v218, s[36:39], s84 offen sc1
v_mfma_f32_16x16x32_bf16 v[84:87], v[236:239], v[196:199], v[84:87]
v_mfma_f32_16x16x32_bf16 v[88:91], v[240:243], v[196:199], v[88:91]
ds_write_b128 v228, v[24:27] offset:1024
v_mfma_f32_16x16x32_bf16 v[92:95], v[244:247], v[196:199], v[92:95]
buffer_load_dwordx4 v[148:151], v219, s[36:39], s84 offen sc1
v_mfma_f32_16x16x32_bf16 v[96:99], v[248:251], v[196:199], v[96:99]
s_waitcnt vmcnt(16)
v_mfma_f32_16x16x32_bf16 v[80:83], a[168:171], a[108:111], v[80:83]
v_mfma_f32_16x16x32_bf16 v[84:87], a[172:175], a[108:111], v[84:87]
v_mfma_f32_16x16x32_bf16 v[88:91], a[176:179], a[108:111], v[88:91]
ds_write_b128 v228, v[28:31] offset:2048
v_mfma_f32_16x16x32_bf16 v[92:95], a[180:183], a[108:111], v[92:95]
buffer_load_dwordx4 a[112:115], v220, s[36:39], s84 offen sc1
v_mfma_f32_16x16x32_bf16 v[96:99], a[184:187], a[108:111], v[96:99]
s_waitcnt vmcnt(16)
v_mfma_f32_16x16x32_bf16 v[80:83], a[148:151], v[152:155], v[80:83]
v_mfma_f32_16x16x32_bf16 v[84:87], a[152:155], v[152:155], v[84:87]
ds_write_b128 v228, v[32:35] offset:3072
v_mfma_f32_16x16x32_bf16 v[88:91], a[156:159], v[152:155], v[88:91]
v_mfma_f32_16x16x32_bf16 v[92:95], a[160:163], v[152:155], v[92:95]
buffer_load_dwordx4 v[156:159], v221, s[36:39], s84 offen sc1
v_mfma_f32_16x16x32_bf16 v[96:99], a[164:167], v[152:155], v[96:99]
s_waitcnt vmcnt(16)
v_mfma_f32_16x16x32_bf16 v[80:83], a[128:131], a[124:127], v[80:83]
ds_write_b128 v228, v[36:39] offset:4096
v_mfma_f32_16x16x32_bf16 v[84:87], a[132:135], a[124:127], v[84:87]
v_mfma_f32_16x16x32_bf16 v[88:91], a[136:139], a[124:127], v[88:91]
s_xor_b32 s84, s94, 16384
v_mfma_f32_16x16x32_bf16 v[92:95], a[140:143], a[124:127], v[92:95]
buffer_load_dwordx4 v[164:167], v218, s[36:39], s84 offen sc1
v_mfma_f32_16x16x32_bf16 v[96:99], a[144:147], a[124:127], v[96:99]
s_waitcnt lgkmcnt(0)
s_barrier
s_waitcnt vmcnt(16)
v_mfma_f32_16x16x32_bf16 v[0:3], a[128:131], v[128:131], 0
ds_read_b128 v[100:103], v229 offset:0
ds_read_b128 v[104:107], v234 offset:5120
v_mfma_f32_16x16x32_bf16 v[4:7], a[132:135], v[128:131], 0
ds_read_b128 v[108:111], v235 offset:10240
ds_read_b128 v[112:115], v229 offset:1024
v_mfma_f32_16x16x32_bf16 v[8:11], a[136:139], v[128:131], 0
buffer_load_dwordx4 v[144:147], v219, s[36:39], s84 offen sc1
v_mfma_f32_16x16x32_bf16 v[12:15], a[140:143], v[128:131], 0
ds_read_b128 v[116:119], v234 offset:6144
ds_read_b128 v[120:123], v235 offset:11264
v_mfma_f32_16x16x32_bf16 v[16:19], a[144:147], v[128:131], 0
s_waitcnt lgkmcnt(5)
v_add_f32 v80, v80, v100
v_add_f32 v81, v81, v101
s_waitcnt vmcnt(16)
v_mfma_f32_16x16x32_bf16 v[36:39], a[144:147], v[160:163], 0
v_add_f32 v82, v82, v102
v_add_f32 v83, v83, v103
v_mfma_f32_16x16x32_bf16 v[32:35], a[140:143], v[160:163], 0
s_waitcnt lgkmcnt(4)
v_add_f32 v80, v80, v104
v_add_f32 v81, v81, v105
v_mfma_f32_16x16x32_bf16 v[28:31], a[136:139], v[160:163], 0
buffer_load_dwordx4 v[140:143], v220, s[36:39], s84 offen sc1
v_mfma_f32_16x16x32_bf16 v[24:27], a[132:135], v[160:163], 0
v_add_f32 v82, v82, v106
v_add_f32 v83, v83, v107
v_mfma_f32_16x16x32_bf16 v[20:23], a[128:131], v[160:163], 0
s_waitcnt lgkmcnt(3)
v_add_f32 v80, v80, v108
v_add_f32 v81, v81, v109
s_waitcnt vmcnt(16)
v_mfma_f32_16x16x32_bf16 v[40:43], a[128:131], a[96:99], 0
v_add_f32 v82, v82, v110
v_add_f32 v83, v83, v111
v_mfma_f32_16x16x32_bf16 v[44:47], a[132:135], a[96:99], 0
buffer_load_dwordx4 v[188:191], v221, s[36:39], s84 offen sc1
v_mfma_f32_16x16x32_bf16 v[48:51], a[136:139], a[96:99], 0
v_cvt_pk_bf16_f32 v80, v80, v81
v_cvt_pk_bf16_f32 v81, v82, v83
v_mfma_f32_16x16x32_bf16 v[52:55], a[140:143], a[96:99], 0
ds_read_b128 v[100:103], v229 offset:2048
ds_read_b128 v[104:107], v234 offset:7168
v_mfma_f32_16x16x32_bf16 v[56:59], a[144:147], a[96:99], 0
ds_read_b128 v[108:111], v235 offset:12288
s_waitcnt lgkmcnt(5)
v_add_f32 v84, v84, v112
s_waitcnt vmcnt(16)
v_mfma_f32_16x16x32_bf16 v[76:79], a[144:147], v[132:135], 0
v_add_f32 v85, v85, v113
v_add_f32 v86, v86, v114
v_mfma_f32_16x16x32_bf16 v[72:75], a[140:143], v[132:135], 0
s_xor_b32 s84, s94, 20480
buffer_load_dwordx4 a[120:123], v218, s[36:39], s84 offen sc1
v_mfma_f32_16x16x32_bf16 v[68:71], a[136:139], v[132:135], 0
v_add_f32 v87, v87, v115
s_waitcnt lgkmcnt(4)
v_add_f32 v84, v84, v116
v_mfma_f32_16x16x32_bf16 v[64:67], a[132:135], v[132:135], 0
v_add_f32 v85, v85, v117
v_add_f32 v86, v86, v118
v_mfma_f32_16x16x32_bf16 v[60:63], a[128:131], v[132:135], 0
v_add_f32 v87, v87, v119
s_waitcnt lgkmcnt(3)
v_add_f32 v84, v84, v120
s_waitcnt vmcnt(16)
v_mfma_f32_16x16x32_bf16 v[0:3], a[148:151], a[116:119], v[0:3]
buffer_load_dwordx4 v[192:195], v219, s[36:39], s84 offen sc1
v_mfma_f32_16x16x32_bf16 v[4:7], a[152:155], a[116:119], v[4:7]
v_add_f32 v85, v85, v121
v_add_f32 v86, v86, v122
v_mfma_f32_16x16x32_bf16 v[8:11], a[156:159], a[116:119], v[8:11]
v_add_f32 v87, v87, v123
v_cvt_pk_bf16_f32 v82, v84, v85
v_mfma_f32_16x16x32_bf16 v[12:15], a[160:163], a[116:119], v[12:15]
v_cvt_pk_bf16_f32 v83, v86, v87
v_mfma_f32_16x16x32_bf16 v[16:19], a[164:167], a[116:119], v[16:19]
buffer_store_dwordx4 v[80:83], v208, s[36:39], s42 offen offset:0 sc1
s_waitcnt vmcnt(17)
v_mfma_f32_16x16x32_bf16 v[36:39], a[164:167], v[124:127], v[36:39]
buffer_load_dwordx4 v[196:199], v220, s[36:39], s84 offen sc1
v_mfma_f32_16x16x32_bf16 v[32:35], a[160:163], v[124:127], v[32:35]
ds_read_b128 v[112:115], v229 offset:3072
ds_read_b128 v[116:119], v234 offset:8192
v_mfma_f32_16x16x32_bf16 v[28:31], a[156:159], v[124:127], v[28:31]
ds_read_b128 v[120:123], v235 offset:13312
s_waitcnt lgkmcnt(5)
v_add_f32 v88, v88, v100
v_mfma_f32_16x16x32_bf16 v[24:27], a[152:155], v[124:127], v[24:27]
v_add_f32 v89, v89, v101
v_add_f32 v90, v90, v102
v_mfma_f32_16x16x32_bf16 v[20:23], a[148:151], v[124:127], v[20:23]
buffer_load_dwordx4 a[108:111], v221, s[36:39], s84 offen sc1
s_waitcnt vmcnt(18)
v_mfma_f32_16x16x32_bf16 v[40:43], a[148:151], a[92:95], v[40:43]
v_add_f32 v91, v91, v103
s_waitcnt lgkmcnt(4)
v_add_f32 v88, v88, v104
v_mfma_f32_16x16x32_bf16 v[44:47], a[152:155], a[92:95], v[44:47]
v_add_f32 v89, v89, v105
v_add_f32 v90, v90, v106
v_mfma_f32_16x16x32_bf16 v[48:51], a[156:159], a[92:95], v[48:51]
v_add_f32 v91, v91, v107
s_waitcnt lgkmcnt(3)
v_add_f32 v88, v88, v108
v_mfma_f32_16x16x32_bf16 v[52:55], a[160:163], a[92:95], v[52:55]
v_add_f32 v89, v89, v109
v_add_f32 v90, v90, v110
v_mfma_f32_16x16x32_bf16 v[56:59], a[164:167], a[92:95], v[56:59]
s_xor_b32 s84, s94, 24576
buffer_load_dwordx4 v[152:155], v218, s[36:39], s84 offen sc1
s_waitcnt vmcnt(18)
v_mfma_f32_16x16x32_bf16 v[76:79], a[164:167], v[200:203], v[76:79]
v_add_f32 v91, v91, v111
v_cvt_pk_bf16_f32 v84, v88, v89
v_mfma_f32_16x16x32_bf16 v[72:75], a[160:163], v[200:203], v[72:75]
ds_read_b128 v[100:103], v229 offset:4096
v_cvt_pk_bf16_f32 v85, v90, v91
v_mfma_f32_16x16x32_bf16 v[68:71], a[156:159], v[200:203], v[68:71]
ds_read_b128 v[104:107], v234 offset:9216
ds_read_b128 v[108:111], v235 offset:14336
v_mfma_f32_16x16x32_bf16 v[64:67], a[152:155], v[200:203], v[64:67]
buffer_load_dwordx4 a[124:127], v219, s[36:39], s84 offen sc1
v_mfma_f32_16x16x32_bf16 v[60:63], a[148:151], v[200:203], v[60:63]
s_waitcnt lgkmcnt(5)
v_add_f32 v92, v92, v112
v_add_f32 v93, v93, v113
s_waitcnt vmcnt(18)
v_mfma_f32_16x16x32_bf16 v[0:3], a[168:171], v[204:207], v[0:3]
v_add_f32 v94, v94, v114
v_add_f32 v95, v95, v115
v_mfma_f32_16x16x32_bf16 v[4:7], a[172:175], v[204:207], v[4:7]
s_waitcnt lgkmcnt(4)
v_add_f32 v92, v92, v116
v_add_f32 v93, v93, v117
v_mfma_f32_16x16x32_bf16 v[8:11], a[176:179], v[204:207], v[8:11]
v_add_f32 v94, v94, v118
v_add_f32 v95, v95, v119
v_mfma_f32_16x16x32_bf16 v[12:15], a[180:183], v[204:207], v[12:15]
buffer_load_dwordx4 v[128:131], v220, s[36:39], s84 offen sc1
v_mfma_f32_16x16x32_bf16 v[16:19], a[184:187], v[204:207], v[16:19]
s_waitcnt lgkmcnt(3)
v_add_f32 v92, v92, v120
v_add_f32 v93, v93, v121
s_waitcnt vmcnt(18)
v_mfma_f32_16x16x32_bf16 v[36:39], a[184:187], v[184:187], v[36:39]
v_add_f32 v94, v94, v122
v_add_f32 v95, v95, v123
v_mfma_f32_16x16x32_bf16 v[32:35], a[180:183], v[184:187], v[32:35]
v_cvt_pk_bf16_f32 v86, v92, v93
v_cvt_pk_bf16_f32 v87, v94, v95
v_mfma_f32_16x16x32_bf16 v[28:31], a[176:179], v[184:187], v[28:31]
buffer_load_dwordx4 v[160:163], v221, s[36:39], s84 offen sc1
v_mfma_f32_16x16x32_bf16 v[24:27], a[172:175], v[184:187], v[24:27]
buffer_store_dwordx4 v[84:87], v208, s[36:39], s42 offen offset:1024 sc1
v_mfma_f32_16x16x32_bf16 v[20:23], a[168:171], v[184:187], v[20:23]
s_waitcnt lgkmcnt(2)
v_add_f32 v96, v96, v100
v_add_f32 v97, v97, v101
s_waitcnt vmcnt(19)
v_mfma_f32_16x16x32_bf16 v[40:43], a[168:171], a[100:103], v[40:43]
v_add_f32 v98, v98, v102
v_add_f32 v99, v99, v103
v_mfma_f32_16x16x32_bf16 v[44:47], a[172:175], a[100:103], v[44:47]
s_waitcnt lgkmcnt(1)
v_add_f32 v96, v96, v104
v_add_f32 v97, v97, v105
v_mfma_f32_16x16x32_bf16 v[48:51], a[176:179], a[100:103], v[48:51]
s_xor_b32 s84, s94, 28672
buffer_load_dwordx4 a[96:99], v218, s[36:39], s84 offen sc1
v_mfma_f32_16x16x32_bf16 v[52:55], a[180:183], a[100:103], v[52:55]
v_add_f32 v98, v98, v106
v_add_f32 v99, v99, v107
v_mfma_f32_16x16x32_bf16 v[56:59], a[184:187], a[100:103], v[56:59]
s_waitcnt lgkmcnt(0)
v_add_f32 v96, v96, v108
v_add_f32 v97, v97, v109
s_waitcnt vmcnt(19)
v_mfma_f32_16x16x32_bf16 v[76:79], a[184:187], a[104:107], v[76:79]
v_add_f32 v98, v98, v110
v_add_f32 v99, v99, v111
v_mfma_f32_16x16x32_bf16 v[72:75], a[180:183], a[104:107], v[72:75]
buffer_load_dwordx4 a[144:147], v219, s[36:39], s84 offen sc1
v_mfma_f32_16x16x32_bf16 v[68:71], a[176:179], a[104:107], v[68:71]
v_cvt_pk_bf16_f32 v88, v96, v97
v_cvt_pk_bf16_f32 v89, v98, v99
v_mfma_f32_16x16x32_bf16 v[64:67], a[172:175], a[104:107], v[64:67]
v_lshrrev_b32 v215, 1, v208
v_mfma_f32_16x16x32_bf16 v[60:63], a[168:171], a[104:107], v[60:63]
buffer_store_dwordx2 v[88:89], v215, s[36:39], s42 offen offset:2048 sc1
s_waitcnt vmcnt(20)
v_mfma_f32_16x16x32_bf16 v[0:3], a[188:191], v[136:139], v[0:3]
v_mfma_f32_16x16x32_bf16 v[4:7], v[236:239], v[136:139], v[4:7]
buffer_load_dwordx4 a[140:143], v220, s[36:39], s84 offen sc1
v_mfma_f32_16x16x32_bf16 v[8:11], v[240:243], v[136:139], v[8:11]
v_mfma_f32_16x16x32_bf16 v[12:15], v[244:247], v[136:139], v[12:15]
v_mfma_f32_16x16x32_bf16 v[16:19], v[248:251], v[136:139], v[16:19]
s_waitcnt vmcnt(20)
v_mfma_f32_16x16x32_bf16 v[36:39], v[248:251], v[148:151], v[36:39]
buffer_load_dwordx4 a[136:139], v221, s[36:39], s84 offen sc1
v_mfma_f32_16x16x32_bf16 v[32:35], v[244:247], v[148:151], v[32:35]
v_mfma_f32_16x16x32_bf16 v[28:31], v[240:243], v[148:151], v[28:31]
v_mfma_f32_16x16x32_bf16 v[24:27], v[236:239], v[148:151], v[24:27]
v_mfma_f32_16x16x32_bf16 v[20:23], a[188:191], v[148:151], v[20:23]
s_waitcnt vmcnt(20)
s_xor_b32 s84, s94, 32768
v_mfma_f32_16x16x32_bf16 v[40:43], a[188:191], a[112:115], v[40:43]
buffer_load_dwordx4 a[132:135], v218, s[36:39], s84 offen sc1
v_mfma_f32_16x16x32_bf16 v[44:47], v[236:239], a[112:115], v[44:47]
v_mfma_f32_16x16x32_bf16 v[48:51], v[240:243], a[112:115], v[48:51]
v_mfma_f32_16x16x32_bf16 v[52:55], v[244:247], a[112:115], v[52:55]
buffer_load_dwordx4 v[132:135], v219, s[36:39], s84 offen sc1
v_mfma_f32_16x16x32_bf16 v[56:59], v[248:251], a[112:115], v[56:59]
s_waitcnt vmcnt(21)
v_mfma_f32_16x16x32_bf16 v[76:79], v[248:251], v[156:159], v[76:79]
v_mfma_f32_16x16x32_bf16 v[72:75], v[244:247], v[156:159], v[72:75]
v_mfma_f32_16x16x32_bf16 v[68:71], v[240:243], v[156:159], v[68:71]
v_mfma_f32_16x16x32_bf16 v[64:67], v[236:239], v[156:159], v[64:67]
buffer_load_dwordx4 a[128:131], v220, s[36:39], s84 offen sc1
v_mfma_f32_16x16x32_bf16 v[60:63], a[188:191], v[156:159], v[60:63]
s_waitcnt vmcnt(21)
v_mfma_f32_16x16x32_bf16 v[0:3], v[252:255], v[164:167], v[0:3]
v_mfma_f32_16x16x32_bf16 v[4:7], a[192:195], v[164:167], v[4:7]
v_mfma_f32_16x16x32_bf16 v[8:11], a[196:199], v[164:167], v[8:11]
v_mfma_f32_16x16x32_bf16 v[12:15], a[200:203], v[164:167], v[12:15]
buffer_load_dwordx4 a[116:119], v221, s[36:39], s84 offen sc1
v_mfma_f32_16x16x32_bf16 v[16:19], a[204:207], v[164:167], v[16:19]
s_waitcnt vmcnt(21)
v_mfma_f32_16x16x32_bf16 v[36:39], a[204:207], v[144:147], v[36:39]
v_mfma_f32_16x16x32_bf16 v[32:35], a[200:203], v[144:147], v[32:35]
v_mfma_f32_16x16x32_bf16 v[28:31], a[196:199], v[144:147], v[28:31]
s_xor_b32 s84, s94, 36864
v_mfma_f32_16x16x32_bf16 v[24:27], a[192:195], v[144:147], v[24:27]
buffer_load_dwordx4 v[124:127], v218, s[36:39], s84 offen sc1
v_mfma_f32_16x16x32_bf16 v[20:23], v[252:255], v[144:147], v[20:23]
s_waitcnt vmcnt(21)
v_mfma_f32_16x16x32_bf16 v[40:43], v[252:255], v[140:143], v[40:43]
v_mfma_f32_16x16x32_bf16 v[44:47], a[192:195], v[140:143], v[44:47]
v_mfma_f32_16x16x32_bf16 v[48:51], a[196:199], v[140:143], v[48:51]
buffer_load_dwordx4 a[92:95], v219, s[36:39], s84 offen sc1
v_mfma_f32_16x16x32_bf16 v[52:55], a[200:203], v[140:143], v[52:55]
v_mfma_f32_16x16x32_bf16 v[56:59], a[204:207], v[140:143], v[56:59]
s_waitcnt vmcnt(21)
v_mfma_f32_16x16x32_bf16 v[76:79], a[204:207], v[188:191], v[76:79]
v_mfma_f32_16x16x32_bf16 v[72:75], a[200:203], v[188:191], v[72:75]
v_mfma_f32_16x16x32_bf16 v[68:71], a[196:199], v[188:191], v[68:71]
buffer_load_dwordx4 a[164:167], v220, s[36:39], s84 offen sc1
v_mfma_f32_16x16x32_bf16 v[64:67], a[192:195], v[188:191], v[64:67]
v_mfma_f32_16x16x32_bf16 v[60:63], v[252:255], v[188:191], v[60:63]
s_waitcnt vmcnt(21)
v_mfma_f32_16x16x32_bf16 v[0:3], a[208:211], a[120:123], v[0:3]
v_lshlrev_b32 v236, 16, v168
v_and_b32 v237, 0xffff0000, v168
v_mfma_f32_16x16x32_bf16 v[4:7], a[212:215], a[120:123], v[4:7]
buffer_load_dwordx4 a[160:163], v221, s[36:39], s84 offen sc1
v_mfma_f32_16x16x32_bf16 v[8:11], a[216:219], a[120:123], v[8:11]
v_lshlrev_b32 v238, 16, v169
v_and_b32 v239, 0xffff0000, v169
v_mfma_f32_16x16x32_bf16 v[12:15], a[220:223], a[120:123], v[12:15]
v_lshlrev_b32 v240, 16, v170
v_and_b32 v241, 0xffff0000, v170
v_mfma_f32_16x16x32_bf16 v[16:19], a[224:227], a[120:123], v[16:19]
v_lshlrev_b32 v242, 16, v171
v_and_b32 v243, 0xffff0000, v171
s_waitcnt vmcnt(21)
v_mfma_f32_16x16x32_bf16 v[36:39], a[224:227], v[192:195], v[36:39]
v_lshlrev_b32 v244, 16, v172
v_and_b32 v245, 0xffff0000, v172
v_mfma_f32_16x16x32_bf16 v[32:35], a[220:223], v[192:195], v[32:35]
s_xor_b32 s84, s94, 40960
buffer_load_dwordx4 a[156:159], v221, s[36:39], s84 offen sc1
v_mfma_f32_16x16x32_bf16 v[28:31], a[216:219], v[192:195], v[28:31]
v_lshlrev_b32 v246, 16, v173
v_and_b32 v247, 0xffff0000, v173
v_mfma_f32_16x16x32_bf16 v[24:27], a[212:215], v[192:195], v[24:27]
v_lshlrev_b32 v248, 16, v174
v_and_b32 v249, 0xffff0000, v174
v_mfma_f32_16x16x32_bf16 v[20:23], a[208:211], v[192:195], v[20:23]
v_lshlrev_b32 v250, 16, v175
v_and_b32 v251, 0xffff0000, v175
s_waitcnt vmcnt(20)
v_mfma_f32_16x16x32_bf16 v[40:43], a[208:211], v[196:199], v[40:43]
s_xor_b32 s84, s94, 45056
buffer_load_dwordx4 a[152:155], v221, s[36:39], s84 offen sc1
v_mfma_f32_16x16x32_bf16 v[44:47], a[212:215], v[196:199], v[44:47]
v_lshlrev_b32 v252, 16, v176
v_and_b32 v253, 0xffff0000, v176
v_mfma_f32_16x16x32_bf16 v[48:51], a[216:219], v[196:199], v[48:51]
v_lshlrev_b32 v254, 16, v177
v_and_b32 v255, 0xffff0000, v177
v_mfma_f32_16x16x32_bf16 v[52:55], a[220:223], v[196:199], v[52:55]
s_lshr_b32 s50, s65, 3
s_mul_i32 s50, s50, 512
v_mfma_f32_16x16x32_bf16 v[56:59], a[224:227], v[196:199], v[56:59]
s_add_u32 s50, s50, .Lk_pp_ok-.Lk_start
v_and_b32 v214, 3, v209
s_waitcnt vmcnt(20)
v_mfma_f32_16x16x32_bf16 v[76:79], a[224:227], a[108:111], v[76:79]
s_xor_b32 s84, s94, 49152
v_mfma_f32_16x16x32_bf16 v[72:75], a[220:223], a[108:111], v[72:75]
buffer_load_dwordx4 v[200:203], v221, s[36:39], s84 offen sc1
v_mfma_f32_16x16x32_bf16 v[68:71], a[216:219], a[108:111], v[68:71]
v_lshlrev_b32 v214, 7, v214
v_mfma_f32_16x16x32_bf16 v[64:67], a[212:215], a[108:111], v[64:67]
buffer_load_dword v214, v214, s[60:63], s50 offen
v_mfma_f32_16x16x32_bf16 v[60:63], a[208:211], a[108:111], v[60:63]
s_xor_b32 s84, s94, 53248
buffer_load_dwordx4 a[148:151], v221, s[36:39], s84 offen sc1
s_waitcnt vmcnt(22)
v_mfma_f32_16x16x32_bf16 v[0:3], a[228:231], v[152:155], v[0:3]
s_waitcnt vmcnt(15)
s_barrier
s_mov_b64 exec, 1
buffer_atomic_swap_x2 v[210:211], v231, s[36:39], s44 offen sc1
s_mov_b64 exec, -1
v_mfma_f32_16x16x32_bf16 v[4:7], a[232:235], v[152:155], v[4:7]
v_mfma_f32_16x16x32_bf16 v[8:11], a[236:239], v[152:155], v[8:11]
v_mfma_f32_16x16x32_bf16 v[12:15], a[240:243], v[152:155], v[12:15]
s_xor_b32 s84, s94, 57344
v_mfma_f32_16x16x32_bf16 v[16:19], a[244:247], v[152:155], v[16:19]
buffer_load_dwordx4 v[204:207], v221, s[36:39], s84 offen sc1
v_mfma_f32_16x16x32_bf16 v[36:39], a[244:247], a[124:127], v[36:39]
v_mfma_f32_16x16x32_bf16 v[32:35], a[240:243], a[124:127], v[32:35]
v_mfma_f32_16x16x32_bf16 v[28:31], a[236:239], a[124:127], v[28:31]
s_xor_b32 s84, s94, 61440
v_mfma_f32_16x16x32_bf16 v[24:27], a[232:235], a[124:127], v[24:27]
buffer_load_dwordx4 v[184:187], v221, s[36:39], s84 offen sc1
v_mfma_f32_16x16x32_bf16 v[20:23], a[228:231], a[124:127], v[20:23]
v_mfma_f32_16x16x32_bf16 v[40:43], a[228:231], v[128:131], v[40:43]
v_mfma_f32_16x16x32_bf16 v[44:47], a[232:235], v[128:131], v[44:47]
v_mfma_f32_16x16x32_bf16 v[48:51], a[236:239], v[128:131], v[48:51]
s_xor_b32 s84, s94, 40960
v_mfma_f32_16x16x32_bf16 v[52:55], a[240:243], v[128:131], v[52:55]
buffer_load_dwordx4 a[100:103], v220, s[36:39], s84 offen sc1
v_mfma_f32_16x16x32_bf16 v[56:59], a[244:247], v[128:131], v[56:59]
v_mfma_f32_16x16x32_bf16 v[76:79], a[244:247], v[160:163], v[76:79]
v_mfma_f32_16x16x32_bf16 v[72:75], a[240:243], v[160:163], v[72:75]
s_xor_b32 s84, s94, 45056
v_mfma_f32_16x16x32_bf16 v[68:71], a[236:239], v[160:163], v[68:71]
buffer_load_dwordx4 a[184:187], v220, s[36:39], s84 offen sc1
v_mfma_f32_16x16x32_bf16 v[64:67], a[232:235], v[160:163], v[64:67]
v_mfma_f32_16x16x32_bf16 v[60:63], a[228:231], v[160:163], v[60:63]
v_mfma_f32_16x16x32_bf16 v[0:3], a[248:251], a[96:99], v[0:3]
v_mfma_f32_16x16x32_bf16 v[4:7], a[252:255], a[96:99], v[4:7]
s_xor_b32 s84, s94, 49152
v_mfma_f32_16x16x32_bf16 v[8:11], a[0:3], a[96:99], v[8:11]
buffer_load_dwordx4 a[180:183], v220, s[36:39], s84 offen sc1
v_mfma_f32_16x16x32_bf16 v[12:15], a[4:7], a[96:99], v[12:15]
v_mfma_f32_16x16x32_bf16 v[16:19], a[8:11], a[96:99], v[16:19]
v_mfma_f32_16x16x32_bf16 v[36:39], a[8:11], a[144:147], v[36:39]
s_xor_b32 s84, s94, 53248
v_mfma_f32_16x16x32_bf16 v[32:35], a[4:7], a[144:147], v[32:35]
buffer_load_dwordx4 a[176:179], v220, s[36:39], s84 offen sc1
v_mfma_f32_16x16x32_bf16 v[28:31], a[0:3], a[144:147], v[28:31]
v_mfma_f32_16x16x32_bf16 v[24:27], a[252:255], a[144:147], v[24:27]
v_mfma_f32_16x16x32_bf16 v[20:23], a[248:251], a[144:147], v[20:23]
s_waitcnt vmcnt(21)
v_mfma_f32_16x16x32_bf16 v[40:43], a[248:251], a[140:143], v[40:43]
s_xor_b32 s84, s94, 57344
v_mfma_f32_16x16x32_bf16 v[44:47], a[252:255], a[140:143], v[44:47]
buffer_load_dwordx4 a[172:175], v220, s[36:39], s84 offen sc1
v_mfma_f32_16x16x32_bf16 v[48:51], a[0:3], a[140:143], v[48:51]
v_mfma_f32_16x16x32_bf16 v[52:55], a[4:7], a[140:143], v[52:55]
v_mfma_f32_16x16x32_bf16 v[56:59], a[8:11], a[140:143], v[56:59]
s_waitcnt vmcnt(21)
s_xor_b32 s84, s94, 61440
v_mfma_f32_16x16x32_bf16 v[76:79], a[8:11], a[136:139], v[76:79]
buffer_load_dwordx4 a[104:107], v220, s[36:39], s84 offen sc1
v_mfma_f32_16x16x32_bf16 v[72:75], a[4:7], a[136:139], v[72:75]
v_mfma_f32_16x16x32_bf16 v[68:71], a[0:3], a[136:139], v[68:71]
v_mfma_f32_16x16x32_bf16 v[64:67], a[252:255], a[136:139], v[64:67]
v_mfma_f32_16x16x32_bf16 v[60:63], a[248:251], a[136:139], v[60:63]
s_waitcnt vmcnt(21)
s_xor_b32 s84, s94, 40960
v_mfma_f32_16x16x32_bf16 v[0:3], a[12:15], a[132:135], v[0:3]
buffer_load_dwordx4 a[168:171], v219, s[36:39], s84 offen sc1
v_mfma_f32_16x16x32_bf16 v[4:7], a[16:19], a[132:135], v[4:7]
ds_read_b128 a[112:115], v228 offset:15360
ds_read_b128 a[188:191], v228 offset:16384
v_mfma_f32_16x16x32_bf16 v[8:11], a[20:23], a[132:135], v[8:11]
ds_read_b128 a[204:207], v228 offset:17408
s_xor_b32 s84, s94, 45056
v_mfma_f32_16x16x32_bf16 v[12:15], a[24:27], a[132:135], v[12:15]
buffer_load_dwordx4 a[200:203], v219, s[36:39], s84 offen sc1
v_mfma_f32_16x16x32_bf16 v[16:19], a[28:31], a[132:135], v[16:19]
s_waitcnt vmcnt(22)
v_mfma_f32_16x16x32_bf16 v[36:39], a[28:31], v[132:135], v[36:39]
ds_read_b128 a[196:199], v228 offset:18432
v_mfma_f32_16x16x32_bf16 v[32:35], a[24:27], v[132:135], v[32:35]
ds_read_b128 a[192:195], v228 offset:19456
ds_read_b128 v[188:191], v228 offset:20480
v_mfma_f32_16x16x32_bf16 v[28:31], a[20:23], v[132:135], v[28:31]
ds_read_b128 v[100:103], v228 offset:21504
s_xor_b32 s84, s94, 49152
v_mfma_f32_16x16x32_bf16 v[24:27], a[16:19], v[132:135], v[24:27]
buffer_load_dwordx4 v[104:107], v219, s[36:39], s84 offen sc1
v_mfma_f32_16x16x32_bf16 v[20:23], a[12:15], v[132:135], v[20:23]
s_waitcnt vmcnt(22)
v_mfma_f32_16x16x32_bf16 v[40:43], a[12:15], a[128:131], v[40:43]
ds_read_b128 v[108:111], v228 offset:22528
v_mfma_f32_16x16x32_bf16 v[44:47], a[16:19], a[128:131], v[44:47]
v_mfma_f32_16x16x32_bf16 v[48:51], a[20:23], a[128:131], v[48:51]
s_xor_b32 s84, s94, 53248
v_mfma_f32_16x16x32_bf16 v[52:55], a[24:27], a[128:131], v[52:55]
buffer_load_dwordx4 v[112:115], v219, s[36:39], s84 offen sc1
v_mfma_f32_16x16x32_bf16 v[56:59], a[28:31], a[128:131], v[56:59]
s_waitcnt vmcnt(22)
v_mfma_f32_16x16x32_bf16 v[76:79], a[28:31], a[116:119], v[76:79]
v_mfma_f32_16x16x32_bf16 v[72:75], a[24:27], a[116:119], v[72:75]
v_mfma_f32_16x16x32_bf16 v[68:71], a[20:23], a[116:119], v[68:71]
s_xor_b32 s84, s94, 57344
v_mfma_f32_16x16x32_bf16 v[64:67], a[16:19], a[116:119], v[64:67]
buffer_load_dwordx4 v[116:119], v219, s[36:39], s84 offen sc1
v_mfma_f32_16x16x32_bf16 v[60:63], a[12:15], a[116:119], v[60:63]
s_waitcnt vmcnt(22)
v_mfma_f32_16x16x32_bf16 v[0:3], a[32:35], v[124:127], v[0:3]
ds_read_b128 v[120:123], v232 offset:0
v_mfma_f32_16x16x32_bf16 v[4:7], a[36:39], v[124:127], v[4:7]
ds_read_b128 a[120:123], v232 offset:1024
s_xor_b32 s84, s94, 61440
v_mfma_f32_16x16x32_bf16 v[8:11], a[40:43], v[124:127], v[8:11]
buffer_load_dwordx4 v[192:195], v219, s[36:39], s84 offen sc1
v_mfma_f32_16x16x32_bf16 v[12:15], a[44:47], v[124:127], v[12:15]
ds_read_b128 v[196:199], v232 offset:2048
v_mfma_f32_16x16x32_bf16 v[16:19], a[48:51], v[124:127], v[16:19]
ds_read_b128 a[224:227], v232 offset:3072
s_waitcnt vmcnt(22)
v_mfma_f32_16x16x32_bf16 v[36:39], a[48:51], a[92:95], v[36:39]
v_mfma_f32_16x16x32_bf16 v[32:35], a[44:47], a[92:95], v[32:35]
s_xor_b32 s84, s94, 40960
v_mfma_f32_16x16x32_bf16 v[28:31], a[40:43], a[92:95], v[28:31]
buffer_load_dwordx4 a[220:223], v218, s[36:39], s84 offen sc1
v_mfma_f32_16x16x32_bf16 v[24:27], a[36:39], a[92:95], v[24:27]
v_mfma_f32_16x16x32_bf16 v[20:23], a[32:35], a[92:95], v[20:23]
s_waitcnt vmcnt(22)
v_mfma_f32_16x16x32_bf16 v[40:43], a[32:35], a[164:167], v[40:43]
s_xor_b32 s84, s94, 45056
v_mfma_f32_16x16x32_bf16 v[44:47], a[36:39], a[164:167], v[44:47]
buffer_load_dwordx4 a[216:219], v218, s[36:39], s84 offen sc1
v_mfma_f32_16x16x32_bf16 v[48:51], a[40:43], a[164:167], v[48:51]
v_mfma_f32_16x16x32_bf16 v[52:55], a[44:47], a[164:167], v[52:55]
v_mfma_f32_16x16x32_bf16 v[56:59], a[48:51], a[164:167], v[56:59]
s_waitcnt vmcnt(22)
v_mfma_f32_16x16x32_bf16 v[76:79], a[48:51], a[160:163], v[76:79]
s_xor_b32 s84, s94, 49152
v_mfma_f32_16x16x32_bf16 v[72:75], a[44:47], a[160:163], v[72:75]
buffer_load_dwordx4 a[212:215], v218, s[36:39], s84 offen sc1
v_mfma_f32_16x16x32_bf16 v[68:71], a[40:43], a[160:163], v[68:71]
v_mfma_f32_16x16x32_bf16 v[64:67], a[36:39], a[160:163], v[64:67]
v_mfma_f32_16x16x32_bf16 v[60:63], a[32:35], a[160:163], v[60:63]
s_waitcnt vmcnt(22) lgkmcnt(9)
s_xor_b32 s84, s94, 53248
v_mfma_f32_16x16x32_bf16 v[60:63], a[52:55], a[156:159], v[60:63]
buffer_load_dwordx4 a[108:111], v218, s[36:39], s84 offen sc1
v_mfma_f32_16x16x32_bf16 v[64:67], a[56:59], a[156:159], v[64:67]
ds_read_b128 a[208:211], v232 offset:16384
v_mfma_f32_16x16x32_bf16 v[68:71], a[112:115], a[156:159], v[68:71]
ds_read_b128 a[124:127], v232 offset:17408
v_mfma_f32_16x16x32_bf16 v[72:75], a[188:191], a[156:159], v[72:75]
ds_read_b128 a[244:247], v232 offset:18432
ds_read_b128 a[240:243], v232 offset:19456
v_mfma_f32_16x16x32_bf16 v[76:79], a[204:207], a[156:159], v[76:79]
s_waitcnt vmcnt(22) lgkmcnt(8)
s_xor_b32 s84, s94, 57344
v_mfma_f32_16x16x32_bf16 v[60:63], a[196:199], a[152:155], v[60:63]
buffer_load_dwordx4 a[236:239], v218, s[36:39], s84 offen sc1
v_mfma_f32_16x16x32_bf16 v[64:67], a[192:195], a[152:155], v[64:67]
ds_read_b128 a[232:235], v232 offset:32768
ds_read_b128 a[228:231], v232 offset:33792
v_mfma_f32_16x16x32_bf16 v[68:71], v[188:191], a[152:155], v[68:71]
ds_read_b128 a[96:99], v232 offset:34816
s_xor_b32 s84, s94, 61440
v_mfma_f32_16x16x32_bf16 v[72:75], v[100:103], a[152:155], v[72:75]
buffer_load_dwordx4 a[144:147], v218, s[36:39], s84 offen sc1
v_mfma_f32_16x16x32_bf16 v[76:79], v[108:111], a[152:155], v[76:79]
s_waitcnt vmcnt(23) lgkmcnt(7)
v_mfma_f32_16x16x32_bf16 v[60:63], a[60:63], v[200:203], v[60:63]
ds_read_b128 a[140:143], v232 offset:35840
v_mov_b64 v[216:217], 0
v_mfma_f32_16x16x32_bf16 v[64:67], v[120:123], v[200:203], v[64:67]
s_mov_b64 exec, 1
buffer_atomic_add_x2 v[216:217], v231, s[36:39], s43 offen sc0 sc1
s_mov_b64 exec, -1
v_mfma_f32_16x16x32_bf16 v[68:71], a[120:123], v[200:203], v[68:71]
s_add_u32 s16, s8, 0
v_mfma_f32_16x16x32_bf16 v[72:75], v[196:199], v[200:203], v[72:75]
buffer_load_dwordx4 a[8:11], v208, s[36:39], s16 offen offset:0 sc1
v_mfma_f32_16x16x32_bf16 v[76:79], a[224:227], v[200:203], v[76:79]
s_waitcnt vmcnt(23) lgkmcnt(3)
v_mfma_f32_16x16x32_bf16 v[60:63], a[208:211], a[148:151], v[60:63]
v_mfma_f32_16x16x32_bf16 v[64:67], a[124:127], a[148:151], v[64:67]
s_add_u32 s17, s9, 0
v_mfma_f32_16x16x32_bf16 v[68:71], a[244:247], a[148:151], v[68:71]
buffer_load_dwordx4 a[4:7], v208, s[36:39], s17 offen offset:0 sc1
v_mfma_f32_16x16x32_bf16 v[72:75], a[240:243], a[148:151], v[72:75]
v_mfma_f32_16x16x32_bf16 v[76:79], a[232:235], a[148:151], v[76:79]
s_waitcnt vmcnt(22) lgkmcnt(0)
v_mfma_f32_16x16x32_bf16 v[60:63], a[228:231], v[204:207], v[60:63]
v_mfma_f32_16x16x32_bf16 v[64:67], a[96:99], v[204:207], v[64:67]
buffer_load_dwordx4 a[0:3], v208, s[36:39], s16 offen offset:1024 sc1
v_mfma_f32_16x16x32_bf16 v[68:71], a[140:143], v[204:207], v[68:71]
v_mfma_f32_16x16x32_bf16 v[72:75], a[64:67], v[204:207], v[72:75]
v_mfma_f32_16x16x32_bf16 v[76:79], a[68:71], v[204:207], v[76:79]
s_waitcnt vmcnt(22)
v_mfma_f32_16x16x32_bf16 v[60:63], a[72:75], v[184:187], v[60:63]
buffer_load_dwordx4 a[252:255], v208, s[36:39], s17 offen offset:1024 sc1
v_mfma_f32_16x16x32_bf16 v[64:67], a[76:79], v[184:187], v[64:67]
v_mfma_f32_16x16x32_bf16 v[68:71], a[80:83], v[184:187], v[68:71]
v_mfma_f32_16x16x32_bf16 v[72:75], a[84:87], v[184:187], v[72:75]
buffer_load_dwordx4 a[136:139], v208, s[36:39], s16 offen offset:2048 sc1
v_mfma_f32_16x16x32_bf16 v[76:79], a[88:91], v[184:187], v[76:79]
s_waitcnt vmcnt(23)
v_mfma_f32_16x16x32_bf16 v[40:43], a[52:55], a[100:103], v[40:43]
v_mfma_f32_16x16x32_bf16 v[44:47], a[56:59], a[100:103], v[44:47]
v_mfma_f32_16x16x32_bf16 v[48:51], a[112:115], a[100:103], v[48:51]
buffer_load_dwordx4 a[248:251], v208, s[36:39], s17 offen offset:2048 sc1
v_mfma_f32_16x16x32_bf16 v[52:55], a[188:191], a[100:103], v[52:55]
ds_write_b128 v228, v[60:63] offset:10240
v_mfma_f32_16x16x32_bf16 v[56:59], a[204:207], a[100:103], v[56:59]
s_waitcnt vmcnt(23)
v_mfma_f32_16x16x32_bf16 v[40:43], a[196:199], a[184:187], v[40:43]
v_mfma_f32_16x16x32_bf16 v[44:47], a[192:195], a[184:187], v[44:47]
v_mfma_f32_16x16x32_bf16 v[48:51], v[188:191], a[184:187], v[48:51]
buffer_load_dwordx4 a[132:135], v208, s[36:39], s16 offen offset:3072 sc1
v_mfma_f32_16x16x32_bf16 v[52:55], v[100:103], a[184:187], v[52:55]
ds_write_b128 v228, v[64:67] offset:11264
v_mfma_f32_16x16x32_bf16 v[56:59], v[108:111], a[184:187], v[56:59]
s_waitcnt vmcnt(23)
v_mfma_f32_16x16x32_bf16 v[40:43], a[60:63], a[180:183], v[40:43]
v_mfma_f32_16x16x32_bf16 v[44:47], v[120:123], a[180:183], v[44:47]
buffer_load_dwordx4 a[128:131], v208, s[36:39], s17 offen offset:3072 sc1
v_mfma_f32_16x16x32_bf16 v[48:51], a[120:123], a[180:183], v[48:51]
ds_write_b128 v228, v[68:71] offset:12288
v_mfma_f32_16x16x32_bf16 v[52:55], v[196:199], a[180:183], v[52:55]
v_mfma_f32_16x16x32_bf16 v[56:59], a[224:227], a[180:183], v[56:59]
s_waitcnt vmcnt(23)
s_add_u32 s16, s8, 4096
v_mfma_f32_16x16x32_bf16 v[40:43], a[208:211], a[176:179], v[40:43]
buffer_load_dwordx4 a[28:31], v208, s[36:39], s16 offen offset:0 sc1
v_mfma_f32_16x16x32_bf16 v[44:47], a[124:127], a[176:179], v[44:47]
ds_write_b128 v228, v[72:75] offset:13312
v_mfma_f32_16x16x32_bf16 v[48:51], a[244:247], a[176:179], v[48:51]
s_add_u32 s17, s9, 4096
v_mfma_f32_16x16x32_bf16 v[52:55], a[240:243], a[176:179], v[52:55]
buffer_load_dwordx4 a[24:27], v208, s[36:39], s17 offen offset:0 sc1
v_mfma_f32_16x16x32_bf16 v[56:59], a[232:235], a[176:179], v[56:59]
s_waitcnt vmcnt(24)
v_mfma_f32_16x16x32_bf16 v[40:43], a[228:231], a[172:175], v[40:43]
ds_write_b128 v228, v[76:79] offset:14336
v_mfma_f32_16x16x32_bf16 v[44:47], a[96:99], a[172:175], v[44:47]
v_mfma_f32_16x16x32_bf16 v[48:51], a[140:143], a[172:175], v[48:51]
v_mfma_f32_16x16x32_bf16 v[52:55], a[64:67], a[172:175], v[52:55]
buffer_load_dwordx4 a[20:23], v208, s[36:39], s16 offen offset:1024 sc1
v_mfma_f32_16x16x32_bf16 v[56:59], a[68:71], a[172:175], v[56:59]
s_waitcnt vmcnt(24)
v_mfma_f32_16x16x32_bf16 v[40:43], a[72:75], a[104:107], v[40:43]
v_mfma_f32_16x16x32_bf16 v[44:47], a[76:79], a[104:107], v[44:47]
v_mfma_f32_16x16x32_bf16 v[48:51], a[80:83], a[104:107], v[48:51]
buffer_load_dwordx4 a[16:19], v208, s[36:39], s17 offen offset:1024 sc1
v_mfma_f32_16x16x32_bf16 v[52:55], a[84:87], a[104:107], v[52:55]
v_mfma_f32_16x16x32_bf16 v[56:59], a[88:91], a[104:107], v[56:59]
s_waitcnt vmcnt(12)
v_cmp_ne_u64 vcc, s[80:81], v[216:217]
s_nop 0
s_and_b64 vcc, vcc, 1
s_cbranch_scc0 .Lk_pp_ok
s_mov_b32 s89, 0
.Lk_pp:
v_mov_b64 v[216:217], 0
s_mov_b64 exec, 1
buffer_atomic_add_x2 v[216:217], v231, s[36:39], s43 offen sc0 sc1
s_mov_b64 exec, -1
s_waitcnt vmcnt(0)
v_cmp_ne_u64 vcc, s[80:81], v[216:217]
s_nop 0
s_and_b64 vcc, vcc, 1
s_cbranch_scc0 .Lk_pp_ok
s_add_u32 s89, s89, 1
s_cmp_lt_u32 s89, 0x20000
s_cbranch_scc1 .Lk_pp
.Lk_pp_ok:
v_mfma_f32_16x16x32_bf16 v[20:23], a[52:55], a[168:171], v[20:23]
ds_read_b128 v[128:131], v226 offset:0
ds_read_b128 v[132:135], v226 offset:16
v_mfma_f32_16x16x32_bf16 v[24:27], a[56:59], a[168:171], v[24:27]
ds_read_b128 v[136:139], v226 offset:128
ds_read_b128 v[140:143], v226 offset:144
v_mfma_f32_16x16x32_bf16 v[28:31], a[112:115], a[168:171], v[28:31]
buffer_load_dwordx4 v[168:171], v208, s[36:39], s41 offen offset:0 sc1
v_mfma_f32_16x16x32_bf16 v[32:35], a[188:191], a[168:171], v[32:35]
ds_read_b128 v[144:147], v227 offset:256
ds_read_b128 v[148:151], v226 offset:320
v_mfma_f32_16x16x32_bf16 v[36:39], a[204:207], a[168:171], v[36:39]
ds_write_b128 v228, v[40:43] offset:5120
v_mfma_f32_16x16x32_bf16 v[20:23], a[196:199], a[200:203], v[20:23]
ds_read_b128 v[152:155], v226 offset:336
ds_read_b128 v[156:159], v226 offset:448
v_mfma_f32_16x16x32_bf16 v[24:27], a[192:195], a[200:203], v[24:27]
buffer_load_dwordx4 v[172:175], v208, s[36:39], s41 offen offset:1024 sc1
v_mfma_f32_16x16x32_bf16 v[28:31], v[188:191], a[200:203], v[28:31]
ds_read_b128 v[160:163], v226 offset:464
ds_read_b128 v[164:167], v227 offset:576
v_mfma_f32_16x16x32_bf16 v[32:35], v[100:103], a[200:203], v[32:35]
ds_write_b128 v228, v[44:47] offset:6144
v_mfma_f32_16x16x32_bf16 v[36:39], v[108:111], a[200:203], v[36:39]
v_mfma_f32_16x16x32_bf16 v[20:23], a[60:63], v[104:107], v[20:23]
v_mfma_f32_16x16x32_bf16 v[24:27], v[120:123], v[104:107], v[24:27]
buffer_load_dwordx2 v[176:177], v215, s[36:39], s41 offen offset:2048 sc1
v_mfma_f32_16x16x32_bf16 v[28:31], a[120:123], v[104:107], v[28:31]
ds_write_b128 v228, v[48:51] offset:7168
v_mfma_f32_16x16x32_bf16 v[32:35], v[196:199], v[104:107], v[32:35]
v_mfma_f32_16x16x32_bf16 v[36:39], a[224:227], v[104:107], v[36:39]
v_mfma_f32_16x16x32_bf16 v[20:23], a[208:211], v[112:115], v[20:23]
buffer_load_dwordx4 a[116:119], v208, s[36:39], s16 offen offset:2048 sc1
v_mfma_f32_16x16x32_bf16 v[24:27], a[124:127], v[112:115], v[24:27]
ds_write_b128 v228, v[52:55] offset:8192
v_mfma_f32_16x16x32_bf16 v[28:31], a[244:247], v[112:115], v[28:31]
v_mfma_f32_16x16x32_bf16 v[32:35], a[240:243], v[112:115], v[32:35]
v_mfma_f32_16x16x32_bf16 v[36:39], a[232:235], v[112:115], v[36:39]
buffer_load_dwordx4 a[12:15], v208, s[36:39], s17 offen offset:2048 sc1
v_mfma_f32_16x16x32_bf16 v[20:23], a[228:231], v[116:119], v[20:23]
ds_write_b128 v228, v[56:59] offset:9216
v_mfma_f32_16x16x32_bf16 v[24:27], a[96:99], v[116:119], v[24:27]
v_mfma_f32_16x16x32_bf16 v[28:31], a[140:143], v[116:119], v[28:31]
v_mfma_f32_16x16x32_bf16 v[32:35], a[64:67], v[116:119], v[32:35]
buffer_load_dwordx4 v[124:127], v208, s[36:39], s16 offen offset:3072 sc1
v_mfma_f32_16x16x32_bf16 v[36:39], a[68:71], v[116:119], v[36:39]
v_mfma_f32_16x16x32_bf16 v[20:23], a[72:75], v[192:195], v[20:23]
v_mfma_f32_16x16x32_bf16 v[24:27], a[76:79], v[192:195], v[24:27]
v_mfma_f32_16x16x32_bf16 v[28:31], a[80:83], v[192:195], v[28:31]
buffer_load_dwordx4 a[92:95], v208, s[36:39], s17 offen offset:3072 sc1
v_mfma_f32_16x16x32_bf16 v[32:35], a[84:87], v[192:195], v[32:35]
v_mfma_f32_16x16x32_bf16 v[36:39], a[88:91], v[192:195], v[36:39]
v_mfma_f32_16x16x32_bf16 v[0:3], a[52:55], a[220:223], v[0:3]
s_add_u32 s16, s8, 8192
v_mfma_f32_16x16x32_bf16 v[4:7], a[56:59], a[220:223], v[4:7]
buffer_load_dwordx4 a[164:167], v208, s[36:39], s16 offen offset:0 sc1
v_mfma_f32_16x16x32_bf16 v[8:11], a[112:115], a[220:223], v[8:11]
v_mfma_f32_16x16x32_bf16 v[12:15], a[188:191], a[220:223], v[12:15]
ds_write_b128 v228, v[20:23] offset:0
v_mfma_f32_16x16x32_bf16 v[16:19], a[204:207], a[220:223], v[16:19]
s_add_u32 s17, s9, 8192
v_mfma_f32_16x16x32_bf16 v[0:3], a[196:199], a[216:219], v[0:3]
buffer_load_dwordx4 a[48:51], v208, s[36:39], s17 offen offset:0 sc1
v_mfma_f32_16x16x32_bf16 v[4:7], a[192:195], a[216:219], v[4:7]
v_mfma_f32_16x16x32_bf16 v[8:11], v[188:191], a[216:219], v[8:11]
v_mfma_f32_16x16x32_bf16 v[12:15], v[100:103], a[216:219], v[12:15]
ds_write_b128 v228, v[24:27] offset:1024
v_mfma_f32_16x16x32_bf16 v[16:19], v[108:111], a[216:219], v[16:19]
buffer_load_dwordx4 a[44:47], v208, s[36:39], s16 offen offset:1024 sc1
v_mfma_f32_16x16x32_bf16 v[0:3], a[60:63], a[212:215], v[0:3]
v_mfma_f32_16x16x32_bf16 v[4:7], v[120:123], a[212:215], v[4:7]
v_mfma_f32_16x16x32_bf16 v[8:11], a[120:123], a[212:215], v[8:11]
ds_write_b128 v228, v[28:31] offset:2048
v_mfma_f32_16x16x32_bf16 v[12:15], v[196:199], a[212:215], v[12:15]
buffer_load_dwordx4 a[40:43], v208, s[36:39], s17 offen offset:1024 sc1
v_mfma_f32_16x16x32_bf16 v[16:19], a[224:227], a[212:215], v[16:19]
v_mfma_f32_16x16x32_bf16 v[0:3], a[208:211], a[108:111], v[0:3]
v_mfma_f32_16x16x32_bf16 v[4:7], a[124:127], a[108:111], v[4:7]
ds_write_b128 v228, v[32:35] offset:3072
v_mfma_f32_16x16x32_bf16 v[8:11], a[244:247], a[108:111], v[8:11]
buffer_load_dwordx4 a[36:39], v208, s[36:39], s16 offen offset:2048 sc1
v_mfma_f32_16x16x32_bf16 v[12:15], a[240:243], a[108:111], v[12:15]
v_mfma_f32_16x16x32_bf16 v[16:19], a[232:235], a[108:111], v[16:19]
s_waitcnt vmcnt(11)
v_lshlrev_b32 v80, 16, v168
v_and_b32 v81, 0xffff0000, v168
v_mfma_f32_16x16x32_bf16 v[0:3], a[228:231], a[236:239], v[0:3]
v_lshlrev_b32 v82, 16, v169
v_and_b32 v83, 0xffff0000, v169
ds_write_b128 v228, v[36:39] offset:4096
v_mfma_f32_16x16x32_bf16 v[4:7], a[96:99], a[236:239], v[4:7]
v_lshlrev_b32 v84, 16, v170
v_and_b32 v85, 0xffff0000, v170
buffer_load_dwordx4 a[160:163], v208, s[36:39], s17 offen offset:2048 sc1
v_mfma_f32_16x16x32_bf16 v[8:11], a[140:143], a[236:239], v[8:11]
v_lshlrev_b32 v86, 16, v171
v_and_b32 v87, 0xffff0000, v171
v_mfma_f32_16x16x32_bf16 v[12:15], a[64:67], a[236:239], v[12:15]
s_waitcnt vmcnt(11)
v_lshlrev_b32 v88, 16, v172
v_and_b32 v89, 0xffff0000, v172
v_mfma_f32_16x16x32_bf16 v[16:19], a[68:71], a[236:239], v[16:19]
v_lshlrev_b32 v90, 16, v173
v_and_b32 v91, 0xffff0000, v173
v_mfma_f32_16x16x32_bf16 v[0:3], a[72:75], a[144:147], v[0:3]
v_lshlrev_b32 v92, 16, v174
v_and_b32 v93, 0xffff0000, v174
buffer_load_dwordx4 a[32:35], v208, s[36:39], s16 offen offset:3072 sc1
v_mfma_f32_16x16x32_bf16 v[4:7], a[76:79], a[144:147], v[4:7]
v_lshlrev_b32 v94, 16, v175
v_and_b32 v95, 0xffff0000, v175
v_mfma_f32_16x16x32_bf16 v[8:11], a[80:83], a[144:147], v[8:11]
s_waitcnt vmcnt(11)
v_lshlrev_b32 v96, 16, v176
v_and_b32 v97, 0xffff0000, v176
v_mfma_f32_16x16x32_bf16 v[12:15], a[84:87], a[144:147], v[12:15]
v_lshlrev_b32 v98, 16, v177
v_and_b32 v99, 0xffff0000, v177
v_mfma_f32_16x16x32_bf16 v[16:19], a[88:91], a[144:147], v[16:19]
buffer_load_dwordx4 a[156:159], v208, s[36:39], s17 offen offset:3072 sc1
v_pk_add_f32 v[0:1], v[0:1], v[80:81]
v_pk_add_f32 v[2:3], v[2:3], v[82:83]
v_pk_add_f32 v[4:5], v[4:5], v[84:85]
v_pk_add_f32 v[6:7], v[6:7], v[86:87]
v_pk_add_f32 v[8:9], v[8:9], v[88:89]
v_pk_add_f32 v[10:11], v[10:11], v[90:91]
v_pk_add_f32 v[12:13], v[12:13], v[92:93]
v_pk_add_f32 v[14:15], v[14:15], v[94:95]
v_pk_add_f32 v[16:17], v[16:17], v[96:97]
v_pk_add_f32 v[18:19], v[18:19], v[98:99]
s_waitcnt lgkmcnt(0)
s_barrier
ds_read_b128 v[20:23], v229 offset:0
ds_read_b128 v[40:43], v234 offset:5120
ds_read_b128 v[60:63], v235 offset:10240
ds_read_b128 v[24:27], v229 offset:1024
ds_read_b128 v[44:47], v234 offset:6144
ds_read_b128 v[64:67], v235 offset:11264
ds_read_b128 v[28:31], v229 offset:2048
ds_read_b128 v[48:51], v234 offset:7168
ds_read_b128 v[68:71], v235 offset:12288
ds_read_b128 v[32:35], v229 offset:3072
ds_read_b128 v[52:55], v234 offset:8192
ds_read_b128 v[72:75], v235 offset:13312
ds_read_b128 v[36:39], v229 offset:4096
ds_read_b128 v[56:59], v234 offset:9216
ds_read_b128 v[76:79], v235 offset:14336
s_add_u32 s16, s8, 12288
buffer_load_dwordx4 a[152:155], v208, s[36:39], s16 offen offset:0 sc1
s_waitcnt lgkmcnt(14)
v_pk_add_f32 v[0:1], v[0:1], v[20:21]
s_waitcnt lgkmcnt(12)
v_pk_add_f32 v[40:41], v[40:41], v[60:61]
v_pk_add_f32 v[0:1], v[0:1], v[40:41]
v_pk_add_f32 v[2:3], v[2:3], v[22:23]
v_pk_add_f32 v[42:43], v[42:43], v[62:63]
v_pk_add_f32 v[2:3], v[2:3], v[42:43]
s_waitcnt lgkmcnt(11)
v_pk_add_f32 v[4:5], v[4:5], v[24:25]
s_waitcnt lgkmcnt(9)
v_pk_add_f32 v[44:45], v[44:45], v[64:65]
v_pk_add_f32 v[4:5], v[4:5], v[44:45]
v_pk_add_f32 v[6:7], v[6:7], v[26:27]
v_pk_add_f32 v[46:47], v[46:47], v[66:67]
v_pk_add_f32 v[6:7], v[6:7], v[46:47]
s_waitcnt lgkmcnt(8)
v_pk_add_f32 v[8:9], v[8:9], v[28:29]
s_waitcnt lgkmcnt(6)
v_pk_add_f32 v[48:49], v[48:49], v[68:69]
s_add_u32 s17, s9, 12288
buffer_load_dwordx4 v[200:203], v208, s[36:39], s17 offen offset:0 sc1
v_pk_add_f32 v[8:9], v[8:9], v[48:49]
v_pk_add_f32 v[10:11], v[10:11], v[30:31]
v_pk_add_f32 v[50:51], v[50:51], v[70:71]
v_pk_add_f32 v[10:11], v[10:11], v[50:51]
s_waitcnt lgkmcnt(5)
v_pk_add_f32 v[12:13], v[12:13], v[32:33]
s_waitcnt lgkmcnt(3)
v_pk_add_f32 v[52:53], v[52:53], v[72:73]
v_pk_add_f32 v[12:13], v[12:13], v[52:53]
v_pk_add_f32 v[14:15], v[14:15], v[34:35]
v_pk_add_f32 v[54:55], v[54:55], v[74:75]
v_pk_add_f32 v[14:15], v[14:15], v[54:55]
s_waitcnt lgkmcnt(2)
v_pk_add_f32 v[16:17], v[16:17], v[36:37]
s_waitcnt lgkmcnt(0)
v_pk_add_f32 v[56:57], v[56:57], v[76:77]
v_pk_add_f32 v[16:17], v[16:17], v[56:57]
v_pk_add_f32 v[18:19], v[18:19], v[38:39]
buffer_load_dwordx4 a[148:151], v208, s[36:39], s16 offen offset:1024 sc1
v_pk_add_f32 v[58:59], v[58:59], v[78:79]
v_pk_add_f32 v[18:19], v[18:19], v[58:59]
v_pk_fma_f32 v[236:237], v[128:129], v[0:1], v[236:237]
v_pk_mul_f32 v[148:149], v[148:149], v[236:237]
v_pk_fma_f32 v[238:239], v[130:131], v[2:3], v[238:239]
v_pk_mul_f32 v[150:151], v[150:151], v[238:239]
v_pk_fma_f32 v[240:241], v[132:133], v[4:5], v[240:241]
v_pk_mul_f32 v[152:153], v[152:153], v[240:241]
v_pk_fma_f32 v[242:243], v[134:135], v[6:7], v[242:243]
v_pk_mul_f32 v[154:155], v[154:155], v[242:243]
v_pk_fma_f32 v[244:245], v[136:137], v[8:9], v[244:245]
v_pk_mul_f32 v[156:157], v[156:157], v[244:245]
v_pk_fma_f32 v[246:247], v[138:139], v[10:11], v[246:247]
v_pk_mul_f32 v[158:159], v[158:159], v[246:247]
v_pk_fma_f32 v[248:249], v[140:141], v[12:13], v[248:249]
v_pk_mul_f32 v[160:161], v[160:161], v[248:249]
buffer_load_dwordx4 v[204:207], v208, s[36:39], s17 offen offset:1024 sc1
v_pk_fma_f32 v[250:251], v[142:143], v[14:15], v[250:251]
v_pk_mul_f32 v[162:163], v[162:163], v[250:251]
v_pk_fma_f32 v[252:253], v[144:145], v[16:17], v[252:253]
v_pk_mul_f32 v[164:165], v[164:165], v[252:253]
v_pk_fma_f32 v[254:255], v[146:147], v[18:19], v[254:255]
v_pk_mul_f32 v[166:167], v[166:167], v[254:255]
v_cvt_pk_bf16_f32 v148, v148, v149
v_cvt_pk_bf16_f32 v149, v150, v151
v_cvt_pk_bf16_f32 v150, v152, v153
v_cvt_pk_bf16_f32 v151, v154, v155
v_cvt_pk_bf16_f32 v152, v156, v157
v_cvt_pk_bf16_f32 v153, v158, v159
v_cvt_pk_bf16_f32 v154, v160, v161
v_cvt_pk_bf16_f32 v155, v162, v163
buffer_load_dwordx4 v[184:187], v208, s[36:39], s16 offen offset:2048 sc1
v_cvt_pk_bf16_f32 v156, v164, v165
v_cvt_pk_bf16_f32 v157, v166, v167
s_lshr_b32 s84, s82, 1
s_mul_i32 s84, s84, 80
s_add_u32 s84, s84, s64
s_lshl_b32 s84, s84, 1
s_and_b32 s75, s82, 1
s_add_u32 s84, s84, s75
s_lshl_b32 s84, s84, 10
s_add_u32 s84, s84, 32718848
s_add_u32 s75, s84, 8192
buffer_store_dwordx4 v[148:151], v224, s[36:39], s84 offen sc1
buffer_store_dwordx4 v[152:155], v224, s[36:39], s75 offen sc1
buffer_store_dwordx2 v[156:157], v225, s[36:39], s84 offen sc1
v_pk_add_f32 v[0:1], v[236:237], v[238:239]
v_pk_mul_f32 v[2:3], v[236:237], v[236:237]
v_pk_fma_f32 v[2:3], v[238:239], v[238:239], v[2:3]
v_pk_add_f32 v[0:1], v[0:1], v[240:241]
v_pk_fma_f32 v[2:3], v[240:241], v[240:241], v[2:3]
v_pk_add_f32 v[0:1], v[0:1], v[242:243]
v_pk_fma_f32 v[2:3], v[242:243], v[242:243], v[2:3]
v_pk_add_f32 v[0:1], v[0:1], v[244:245]
v_pk_fma_f32 v[2:3], v[244:245], v[244:245], v[2:3]
v_pk_add_f32 v[0:1], v[0:1], v[246:247]
v_pk_fma_f32 v[2:3], v[246:247], v[246:247], v[2:3]
v_pk_add_f32 v[0:1], v[0:1], v[248:249]
v_pk_fma_f32 v[2:3], v[248:249], v[248:249], v[2:3]
v_pk_add_f32 v[0:1], v[0:1], v[250:251]
v_pk_fma_f32 v[2:3], v[250:251], v[250:251], v[2:3]
v_pk_add_f32 v[0:1], v[0:1], v[252:253]
v_pk_fma_f32 v[2:3], v[252:253], v[252:253], v[2:3]
v_pk_add_f32 v[0:1], v[0:1], v[254:255]
v_pk_fma_f32 v[2:3], v[254:255], v[254:255], v[2:3]
v_add_f32 v0, v0, v1
v_add_f32 v1, v2, v3
v_mov_b32 v2, v0
v_mov_b32 v3, v1
s_nop 0
v_permlane32_swap_b32 v0, v2
v_permlane32_swap_b32 v1, v3
v_pk_add_f32 v[0:1], v[0:1], v[2:3]
v_mov_b32 v2, v0
v_mov_b32 v3, v1
s_nop 0
v_permlane16_swap_b32 v0, v2
v_permlane16_swap_b32 v1, v3
v_pk_add_f32 v[0:1], v[0:1], v[2:3]
v_lshrrev_b32 v2, 4, v209
v_lshlrev_b32 v2, 28, v2
v_lshl_add_u32 v2, v209, 3, v2
s_lshl_b32 s75, s82, 3
s_add_u32 s75, s75, s83
s_lshl_b32 s75, s75, 2
s_add_u32 s75, s75, s64
s_lshl_b32 s75, s75, 7
s_add_u32 s75, s75, 35340288
buffer_store_dwordx2 v[0:1], v2, s[36:39], s75 offen sc1
buffer_load_dwordx4 a[100:103], v208, s[36:39], s17 offen offset:2048 sc1
buffer_load_dwordx4 a[184:187], v208, s[36:39], s16 offen offset:3072 sc1
buffer_load_dwordx4 a[180:183], v208, s[36:39], s17 offen offset:3072 sc1
s_add_u32 s16, s8, 16384
buffer_load_dwordx4 a[176:179], v208, s[36:39], s16 offen offset:0 sc1
s_add_u32 s17, s9, 16384
buffer_load_dwordx4 a[172:175], v208, s[36:39], s17 offen offset:0 sc1
buffer_load_dwordx4 a[104:107], v208, s[36:39], s16 offen offset:1024 sc1
s_lshl_b32 s76, s82, 2
s_add_u32 s76, s76, s64
s_lshl_b32 s76, s76, 6
s_add_u32 s76, s76, 16908288
s_lshl_b32 s75, s83, 3
s_add_u32 s75, s75, s76
s_lshl_b32 s76, s99, 3
s_add_u32 s76, s76, s64
s_lshl_b32 s76, s76, 6
s_add_u32 s76, s76, 16908288
s_and_b32 s77, s69, 3
s_lshl_b32 s77, s77, 13
s_add_u32 s76, s76, s77
s_mul_i32 s84, s99, 163840
s_lshl_b32 s77, s64, 11
s_add_u32 s84, s84, s77
s_add_u32 s84, s84, 32718848
s_add_u32 m0, s77, s66
v_bfe_u32 v214, v209, 3, 1
v_lshl_add_u32 v230, v214, 8, v230
v_xor_b32 v218, s98, v218
v_xor_b32 v219, s98, v219
v_xor_b32 v220, s98, v220
v_xor_b32 v221, s98, v221
s_mul_i32 s77, s83, 276480
s_add_u32 s77, s77, 30375936
s_mul_i32 s85, s64, 40960
s_add_u32 s85, s85, s77
s_mul_i32 s74, s64, 6
s_min_u32 s74, s74, 17
s_add_u32 s74, s74, 32
s_mul_i32 s78, s74, 5120
s_add_u32 s78, s78, s77
s_add_u32 s79, s78, 25600
s_cmp_ge_u32 s64, 2
s_cselect_b32 s79, 47923200, s79
s_waitcnt vmcnt(6)
s_mov_b64 exec, 15
buffer_atomic_swap_x2 v[210:211], v231, s[36:39], s75 offen sc1
s_mov_b64 exec, -1
v_mov_b64 v[216:217], 0
s_mov_b64 exec, 65535
buffer_atomic_add_x2 v[216:217], v230, s[36:39], s76 offen sc0 sc1
s_mov_b64 exec, -1
buffer_load_dwordx4 a[168:171], v208, s[36:39], s17 offen offset:1024 sc1
buffer_load_dwordx4 a[200:203], v208, s[36:39], s16 offen offset:2048 sc1
buffer_load_dwordx4 v[104:107], v208, s[36:39], s17 offen offset:2048 sc1
buffer_load_dwordx4 v[112:115], v208, s[36:39], s16 offen offset:3072 sc1
buffer_load_dwordx4 v[116:119], v208, s[36:39], s17 offen offset:3072 sc1
s_add_u32 s16, s8, 20480
buffer_load_dwordx4 v[192:195], v208, s[36:39], s16 offen offset:0 sc1
s_barrier
s_waitcnt vmcnt(6)
v_cmp_ne_u64 vcc, s[80:81], v[216:217]
s_nop 0
s_and_b64 vcc, vcc, 65535
s_cbranch_scc0 .Lk_p2_ok
s_mov_b32 s89, 0
.Lk_p2:
v_mov_b64 v[216:217], 0
s_mov_b64 exec, 65535
buffer_atomic_add_x2 v[216:217], v230, s[36:39], s76 offen sc0 sc1
s_mov_b64 exec, -1
s_waitcnt vmcnt(0)
v_cmp_ne_u64 vcc, s[80:81], v[216:217]
s_nop 0
s_and_b64 vcc, vcc, 65535
s_cbranch_scc0 .Lk_p2_ok
s_add_u32 s89, s89, 1
s_cmp_lt_u32 s89, 0x20000
s_cbranch_scc1 .Lk_p2
.Lk_p2_ok:
buffer_load_dwordx4 v208, s[36:39], s84 offen offset:0 sc1 lds
buffer_load_dwordx4 v208, s[36:39], s84 offen offset:1024 sc1 lds
s_add_u32 m0, m0, 8192
s_add_u32 s84, s84, 8192
buffer_load_dwordx4 v208, s[36:39], s84 offen offset:0 sc1 lds
buffer_load_dwordx4 v208, s[36:39], s84 offen offset:1024 sc1 lds
s_add_u32 m0, m0, 8192
s_add_u32 s84, s84, 8192
buffer_load_dwordx4 v208, s[36:39], s84 offen offset:0 sc1 lds
buffer_load_dwordx4 v208, s[36:39], s84 offen offset:1024 sc1 lds
s_add_u32 m0, m0, 8192
s_add_u32 s84, s84, 8192
buffer_load_dwordx4 v208, s[36:39], s84 offen offset:0 sc1 lds
buffer_load_dwordx4 v208, s[36:39], s84 offen offset:1024 sc1 lds
s_add_u32 m0, m0, 8192
s_add_u32 s84, s84, 8192
buffer_load_dwordx4 v208, s[36:39], s84 offen offset:0 sc1 lds
buffer_load_dwordx4 v208, s[36:39], s84 offen offset:1024 sc1 lds
s_add_u32 m0, m0, 8192
s_add_u32 s84, s84, 8192
buffer_load_dwordx4 v208, s[36:39], s84 offen offset:0 sc1 lds
buffer_load_dwordx4 v208, s[36:39], s84 offen offset:1024 sc1 lds
s_and_b32 s75, s64, 1
s_lshl_b32 s76, s99, 1
s_add_u32 s20, s76, s75
s_xor_b32 s21, s20, 1
s_lshl_b32 s75, s75, 10
v_add_u32 v232, s75, v208
v_xor_b32 v225, 1024, v232
v_add_u32 v232, s66, v232
v_add_u32 v225, s66, v225
v_and_b32 v64, 31, v209
v_lshrrev_b32 v163, 5, v209
v_lshlrev_b32 v214, 3, v64
v_lshl_add_u32 v214, v163, 11, v214
s_lshl_b32 s12, s99, 1
s_lshr_b32 s75, s64, 1
s_add_u32 s12, s12, s75
s_lshl_b32 s12, s12, 4
s_and_b32 s75, s64, 1
s_add_u32 s12, s12, s75
s_lshl_b32 s12, s12, 8
s_add_u32 s12, s12, 35340288
s_add_u32 s13, s66, 155648
s_lshl_b32 s76, s64, 5
v_add_u32 v64, s76, v64
v_lshl_add_u32 v65, v64, 2, s13
v_lshl_add_u32 v64, v64, 4, s13
s_lshl_b32 s76, s75, 6
v_and_b32 v66, 15, v209
v_add_u32 v66, s76, v66
v_xor_b32 v67, 64, v66
v_lshl_add_u32 v68, v163, 4, v66
v_lshl_add_u32 v69, v163, 4, v67
v_lshl_add_u32 v66, v66, 4, s13
v_lshl_add_u32 v67, v67, 4, s13
v_lshl_add_u32 v68, v68, 2, s13
v_lshl_add_u32 v69, v69, 2, s13
v_mov_b32 v171, 0
s_mul_i32 s20, s20, 221184
s_add_u32 s20, s20, 35471360
s_mul_i32 s21, s21, 221184
s_add_u32 s21, s21, 35471360
s_lshl_b32 s77, s69, 2
s_add_u32 s77, s77, s64
s_lshr_b32 s88, s77, 1
s_lshl_b32 s88, s88, 3
s_and_b32 s75, s64, 1
s_add_u32 s88, s88, s75
s_lshl_b32 s88, s88, 9
s_add_u32 s22, s88, s20
s_add_u32 s23, s88, s21
s_lshr_b32 s88, s69, 1
s_mul_i32 s88, s88, 11
s_and_b32 s75, s69, 1
s_mul_i32 s75, s75, 6
s_add_u32 s88, s88, s75
s_add_u32 s88, s88, 128
s_add_u32 s75, s88, s64
s_lshr_b32 s24, s75, 2
s_lshl_b32 s24, s24, 4
s_and_b32 s75, s75, 3
s_add_u32 s24, s24, s75
s_lshl_b32 s24, s24, 8
s_lshr_b32 s75, s64, 1
s_add_u32 s75, s75, s88
s_add_u32 s75, s75, 4
s_lshr_b32 s26, s75, 2
s_lshl_b32 s26, s26, 4
s_and_b32 s75, s75, 3
s_add_u32 s26, s26, s75
s_lshl_b32 s26, s26, 8
s_add_u32 s25, s24, s21
s_add_u32 s24, s24, s20
s_add_u32 s26, s26, s20
s_max_u32 s26, s26, s27
s_waitcnt vmcnt(8)
s_barrier
ds_read_b128 a[52:55], v232 offset:0
ds_read_b128 a[56:59], v232 offset:2048
ds_read_b128 a[112:115], v232 offset:4096
ds_read_b128 a[188:191], v232 offset:6144
ds_read_b128 a[220:223], v225 offset:0
ds_read_b128 a[204:207], v225 offset:2048
ds_read_b128 a[196:199], v225 offset:4096
ds_read_b128 a[192:195], v225 offset:6144
s_waitcnt lgkmcnt(4)
v_mfma_f32_16x16x32_bf16 v[0:3], a[8:11], a[52:55], 0
ds_read_b128 v[188:191], v232 offset:8192
v_mfma_f32_16x16x32_bf16 v[32:35], a[4:7], a[52:55], 0
v_mfma_f32_16x16x32_bf16 v[36:39], a[4:7], a[56:59], 0
buffer_load_dwordx2 v[160:161], v214, s[36:39], s12 offen offset:0 sc1
v_mfma_f32_16x16x32_bf16 v[4:7], a[8:11], a[56:59], 0
ds_read_b128 a[216:219], v232 offset:10240
v_mfma_f32_16x16x32_bf16 v[8:11], a[8:11], a[112:115], 0
ds_read_b128 a[60:63], v232 offset:12288
v_mfma_f32_16x16x32_bf16 v[40:43], a[4:7], a[112:115], 0
buffer_load_dwordx2 v[162:163], v214, s[36:39], s12 offen offset:512 sc1
v_mfma_f32_16x16x32_bf16 v[44:47], a[4:7], a[188:191], 0
ds_read_b128 v[120:123], v232 offset:14336
v_mfma_f32_16x16x32_bf16 v[12:15], a[8:11], a[188:191], 0
s_waitcnt lgkmcnt(4)
v_mfma_f32_16x16x32_bf16 v[16:19], a[8:11], a[220:223], 0
buffer_load_dwordx2 v[164:165], v214, s[36:39], s12 offen offset:1024 sc1
v_mfma_f32_16x16x32_bf16 v[48:51], a[4:7], a[220:223], 0
ds_read_b128 a[120:123], v225 offset:8192
v_mfma_f32_16x16x32_bf16 v[52:55], a[4:7], a[204:207], 0
ds_read_b128 v[196:199], v225 offset:10240
v_mfma_f32_16x16x32_bf16 v[20:23], a[8:11], a[204:207], 0
buffer_load_dwordx2 v[166:167], v214, s[36:39], s12 offen offset:1536 sc1
v_mfma_f32_16x16x32_bf16 v[24:27], a[8:11], a[196:199], 0
ds_read_b128 a[212:215], v225 offset:12288
ds_read_b128 a[224:227], v225 offset:14336
v_mfma_f32_16x16x32_bf16 v[56:59], a[4:7], a[196:199], 0
s_add_u32 m0, m0, 8192
s_add_u32 s84, s84, 8192
v_mfma_f32_16x16x32_bf16 v[60:63], a[4:7], a[192:195], 0
buffer_load_dwordx4 v208, s[36:39], s84 offen offset:0 sc1 lds
v_mfma_f32_16x16x32_bf16 v[28:31], a[8:11], a[192:195], 0
s_waitcnt lgkmcnt(4)
v_mfma_f32_16x16x32_bf16 v[0:3], a[0:3], v[188:191], v[0:3]
s_waitcnt vmcnt(5)
s_barrier
v_mfma_f32_16x16x32_bf16 v[32:35], a[252:255], v[188:191], v[32:35]
ds_read_b128 a[208:211], v232 offset:16384
ds_read_b128 a[124:127], v232 offset:18432
v_mfma_f32_16x16x32_bf16 v[36:39], a[252:255], a[216:219], v[36:39]
buffer_load_dwordx4 v208, s[36:39], s84 offen offset:1024 sc1 lds
v_mfma_f32_16x16x32_bf16 v[4:7], a[0:3], a[216:219], v[4:7]
ds_read_b128 a[244:247], v232 offset:20480
s_add_u32 m0, m0, 8192
v_mfma_f32_16x16x32_bf16 v[8:11], a[0:3], a[60:63], v[8:11]
s_add_u32 s84, s84, 8192
v_mfma_f32_16x16x32_bf16 v[40:43], a[252:255], a[60:63], v[40:43]
buffer_load_dwordx4 v208, s[36:39], s84 offen offset:0 sc1 lds
v_mfma_f32_16x16x32_bf16 v[44:47], a[252:255], v[120:123], v[44:47]
ds_read_b128 a[240:243], v232 offset:22528
v_mfma_f32_16x16x32_bf16 v[12:15], a[0:3], v[120:123], v[12:15]
buffer_load_dwordx4 v208, s[36:39], s84 offen offset:1024 sc1 lds
s_waitcnt lgkmcnt(4)
v_mfma_f32_16x16x32_bf16 v[16:19], a[0:3], a[120:123], v[16:19]
ds_read_b128 a[108:111], v225 offset:16384
s_add_u32 m0, m0, 8192
v_mfma_f32_16x16x32_bf16 v[48:51], a[252:255], a[120:123], v[48:51]
s_add_u32 s84, s84, 8192
buffer_load_dwordx4 v208, s[36:39], s84 offen offset:0 sc1 lds
v_mfma_f32_16x16x32_bf16 v[52:55], a[252:255], v[196:199], v[52:55]
ds_read_b128 a[232:235], v225 offset:18432
v_mfma_f32_16x16x32_bf16 v[20:23], a[0:3], v[196:199], v[20:23]
buffer_load_dwordx4 v208, s[36:39], s84 offen offset:1024 sc1 lds
v_mfma_f32_16x16x32_bf16 v[24:27], a[0:3], a[212:215], v[24:27]
ds_read_b128 a[228:231], v225 offset:20480
s_add_u32 m0, m0, 8192
v_mfma_f32_16x16x32_bf16 v[56:59], a[252:255], a[212:215], v[56:59]
s_add_u32 s84, s84, 8192
buffer_load_dwordx4 v208, s[36:39], s84 offen offset:0 sc1 lds
v_mfma_f32_16x16x32_bf16 v[60:63], a[252:255], a[224:227], v[60:63]
ds_read_b128 a[96:99], v225 offset:22528
v_mfma_f32_16x16x32_bf16 v[28:31], a[0:3], a[224:227], v[28:31]
buffer_load_dwordx4 v208, s[36:39], s84 offen offset:1024 sc1 lds
s_waitcnt lgkmcnt(4)
v_mfma_f32_16x16x32_bf16 v[0:3], a[136:139], a[208:211], v[0:3]
ds_read_b128 a[140:143], v232 offset:24576
v_mfma_f32_16x16x32_bf16 v[32:35], a[248:251], a[208:211], v[32:35]
ds_read_b128 a[64:67], v232 offset:26624
v_mfma_f32_16x16x32_bf16 v[36:39], a[248:251], a[124:127], v[36:39]
s_add_u32 m0, m0, 8192
s_add_u32 s84, s84, 8192
v_mfma_f32_16x16x32_bf16 v[4:7], a[136:139], a[124:127], v[4:7]
buffer_load_dwordx4 v208, s[36:39], s84 offen offset:0 sc1 lds
v_mfma_f32_16x16x32_bf16 v[8:11], a[136:139], a[244:247], v[8:11]
ds_read_b128 a[236:239], v232 offset:28672
v_mfma_f32_16x16x32_bf16 v[40:43], a[248:251], a[244:247], v[40:43]
ds_read_b128 a[68:71], v232 offset:30720
v_mfma_f32_16x16x32_bf16 v[44:47], a[248:251], a[240:243], v[44:47]
buffer_load_dwordx4 v208, s[36:39], s84 offen offset:1024 sc1 lds
v_mfma_f32_16x16x32_bf16 v[12:15], a[136:139], a[240:243], v[12:15]
s_waitcnt lgkmcnt(4)
v_mfma_f32_16x16x32_bf16 v[16:19], a[136:139], a[108:111], v[16:19]
ds_read_b128 a[72:75], v225 offset:24576
v_mfma_f32_16x16x32_bf16 v[48:51], a[248:251], a[108:111], v[48:51]
ds_read_b128 a[76:79], v225 offset:26624
v_mfma_f32_16x16x32_bf16 v[52:55], a[248:251], a[232:235], v[52:55]
s_add_u32 m0, m0, 8192
s_add_u32 s84, s84, 8192
v_mfma_f32_16x16x32_bf16 v[20:23], a[136:139], a[232:235], v[20:23]
buffer_load_dwordx4 v208, s[36:39], s84 offen offset:0 sc1 lds
v_mfma_f32_16x16x32_bf16 v[24:27], a[136:139], a[228:231], v[24:27]
ds_read_b128 a[80:83], v225 offset:28672
v_mfma_f32_16x16x32_bf16 v[56:59], a[248:251], a[228:231], v[56:59]
ds_read_b128 a[84:87], v225 offset:30720
v_mfma_f32_16x16x32_bf16 v[60:63], a[248:251], a[96:99], v[60:63]
buffer_load_dwordx4 v208, s[36:39], s84 offen offset:1024 sc1 lds
v_mfma_f32_16x16x32_bf16 v[28:31], a[136:139], a[96:99], v[28:31]
s_waitcnt lgkmcnt(4)
ds_read_b128 a[144:147], v232 offset:32768
v_mfma_f32_16x16x32_bf16 v[0:3], a[132:135], a[140:143], v[0:3]
s_waitcnt vmcnt(12)
v_add_f32 v160, v160, v162
v_mfma_f32_16x16x32_bf16 v[32:35], a[128:131], a[140:143], v[32:35]
ds_read_b128 a[88:91], v232 offset:34816
v_add_f32 v161, v161, v163
v_mfma_f32_16x16x32_bf16 v[36:39], a[128:131], a[64:67], v[36:39]
v_add_f32 v160, v160, v164
s_add_u32 m0, m0, 8192
v_mfma_f32_16x16x32_bf16 v[4:7], a[132:135], a[64:67], v[4:7]
s_add_u32 s84, s84, 8192
buffer_load_dwordx4 v208, s[36:39], s84 offen offset:0 sc1 lds
v_mfma_f32_16x16x32_bf16 v[8:11], a[132:135], a[236:239], v[8:11]
ds_read_b128 a[52:55], v232 offset:36864
v_add_f32 v161, v161, v165
v_mfma_f32_16x16x32_bf16 v[40:43], a[128:131], a[236:239], v[40:43]
v_add_f32 v160, v160, v166
v_add_f32 v161, v161, v167
v_mfma_f32_16x16x32_bf16 v[44:47], a[128:131], a[68:71], v[44:47]
ds_read_b128 a[56:59], v232 offset:38912
v_mov_b32 v162, v160
v_mfma_f32_16x16x32_bf16 v[12:15], a[132:135], a[68:71], v[12:15]
buffer_load_dwordx4 v208, s[36:39], s84 offen offset:1024 sc1 lds
s_waitcnt lgkmcnt(4)
v_mfma_f32_16x16x32_bf16 v[16:19], a[132:135], a[72:75], v[16:19]
ds_read_b128 a[112:115], v225 offset:32768
v_mov_b32 v163, v161
v_mfma_f32_16x16x32_bf16 v[48:51], a[128:131], a[72:75], v[48:51]
v_permlane32_swap_b32 v160, v162
v_permlane32_swap_b32 v161, v163
v_mfma_f32_16x16x32_bf16 v[52:55], a[128:131], a[76:79], v[52:55]
ds_read_b128 a[188:191], v225 offset:34816
v_add_f32 v160, v160, v162
v_mfma_f32_16x16x32_bf16 v[20:23], a[132:135], a[76:79], v[20:23]
s_add_u32 m0, m0, 8192
s_add_u32 s84, s84, 8192
buffer_load_dwordx4 v208, s[36:39], s84 offen offset:0 sc1 lds
v_mfma_f32_16x16x32_bf16 v[24:27], a[132:135], a[80:83], v[24:27]
ds_read_b128 a[220:223], v225 offset:36864
v_add_f32 v161, v161, v163
v_mfma_f32_16x16x32_bf16 v[56:59], a[128:131], a[80:83], v[56:59]
v_mul_f32 v160, 0xbacccccd, v160
v_mul_f32 v161, 0x3acccccd, v161
v_mfma_f32_16x16x32_bf16 v[60:63], a[128:131], a[84:87], v[60:63]
ds_read_b128 a[204:207], v225 offset:38912
v_fma_f32 v161, -v160, v160, v161
v_mfma_f32_16x16x32_bf16 v[28:31], a[132:135], a[84:87], v[28:31]
buffer_load_dwordx4 v208, s[36:39], s84 offen offset:1024 sc1 lds
s_waitcnt lgkmcnt(4)
v_mfma_f32_16x16x32_bf16 v[0:3], a[28:31], a[144:147], v[0:3]
ds_read_b128 a[196:199], v232 offset:40960
v_max_f32 v161, 0, v161
v_mfma_f32_16x16x32_bf16 v[32:35], a[24:27], a[144:147], v[32:35]
ds_read_b128 a[4:7], v232 offset:43008
v_add_f32 v161, 0x3727c5ac, v161
v_mfma_f32_16x16x32_bf16 v[36:39], a[24:27], a[88:91], v[36:39]
s_add_u32 m0, m0, 8192
s_add_u32 s84, s84, 8192
v_mfma_f32_16x16x32_bf16 v[4:7], a[28:31], a[88:91], v[4:7]
buffer_load_dwordx4 v208, s[36:39], s84 offen offset:0 sc1 lds
v_mfma_f32_16x16x32_bf16 v[8:11], a[28:31], a[52:55], v[8:11]
ds_read_b128 a[8:11], v232 offset:45056
v_mfma_f32_16x16x32_bf16 v[40:43], a[24:27], a[52:55], v[40:43]
v_sqrt_f32 v162, v161
v_mfma_f32_16x16x32_bf16 v[44:47], a[24:27], a[56:59], v[44:47]
ds_read_b128 a[192:195], v232 offset:47104
v_mfma_f32_16x16x32_bf16 v[12:15], a[28:31], a[56:59], v[12:15]
buffer_load_dwordx4 v208, s[36:39], s84 offen offset:1024 sc1 lds
s_waitcnt lgkmcnt(4)
v_mfma_f32_16x16x32_bf16 v[16:19], a[28:31], a[112:115], v[16:19]
ds_read_b128 v[188:191], v225 offset:40960
v_mfma_f32_16x16x32_bf16 v[48:51], a[24:27], a[112:115], v[48:51]
v_rcp_f32 v161, v161
v_mfma_f32_16x16x32_bf16 v[52:55], a[24:27], a[188:191], v[52:55]
ds_read_b128 a[216:219], v225 offset:43008
v_cvt_pk_bf16_f32 v168, v160, v160
v_mfma_f32_16x16x32_bf16 v[20:23], a[28:31], a[188:191], v[20:23]
s_add_u32 m0, m0, 8192
s_add_u32 s84, s84, 8192
buffer_load_dwordx4 v208, s[36:39], s84 offen offset:0 sc1 lds
v_mfma_f32_16x16x32_bf16 v[24:27], a[28:31], a[220:223], v[24:27]
ds_read_b128 a[60:63], v225 offset:45056
v_lshlrev_b32 v163, 16, v168
v_mfma_f32_16x16x32_bf16 v[56:59], a[24:27], a[220:223], v[56:59]
v_sub_f32 v163, v160, v163
v_cvt_pk_bf16_f32 v169, v163, v162
v_mfma_f32_16x16x32_bf16 v[60:63], a[24:27], a[204:207], v[60:63]
ds_read_b128 v[120:123], v225 offset:47104
v_and_b32 v163, 0xffff0000, v169
v_mfma_f32_16x16x32_bf16 v[28:31], a[28:31], a[204:207], v[28:31]
buffer_load_dwordx4 v208, s[36:39], s84 offen offset:1024 sc1 lds
s_waitcnt lgkmcnt(4)
v_mfma_f32_16x16x32_bf16 v[0:3], a[20:23], a[196:199], v[0:3]
v_sub_f32 v163, v162, v163
v_cvt_pk_bf16_f32 v170, v162, v163
ds_write_b32 v65, v161 offset:2048
ds_write_b128 v64, v[168:171]
s_waitcnt lgkmcnt(0)
s_waitcnt vmcnt(12)
s_barrier
ds_read_b128 v[128:131], v66 offset:0
v_mfma_f32_16x16x32_bf16 v[32:35], a[16:19], a[196:199], v[32:35]
ds_read_b128 a[120:123], v232 offset:49152
ds_read_b128 v[132:135], v66 offset:256
v_mfma_f32_16x16x32_bf16 v[36:39], a[16:19], a[4:7], v[36:39]
ds_read_b128 v[196:199], v232 offset:51200
ds_read_b128 v[136:139], v66 offset:512
v_mfma_f32_16x16x32_bf16 v[4:7], a[20:23], a[4:7], v[4:7]
s_add_u32 m0, m0, 8192
s_add_u32 s84, s84, 8192
buffer_load_dwordx4 v208, s[36:39], s84 offen offset:0 sc1 lds
v_mfma_f32_16x16x32_bf16 v[8:11], a[20:23], a[8:11], v[8:11]
ds_read_b128 a[212:215], v232 offset:53248
ds_read_b128 v[140:143], v66 offset:768
v_mfma_f32_16x16x32_bf16 v[40:43], a[16:19], a[8:11], v[40:43]
ds_read_b128 v[144:147], v67 offset:0
ds_read_b128 v[148:151], v67 offset:256
v_mfma_f32_16x16x32_bf16 v[44:47], a[16:19], a[192:195], v[44:47]
ds_read_b128 a[252:255], v232 offset:55296
ds_read_b128 v[152:155], v67 offset:512
v_mfma_f32_16x16x32_bf16 v[12:15], a[20:23], a[192:195], v[12:15]
buffer_load_dwordx4 v208, s[36:39], s84 offen offset:1024 sc1 lds
v_mfma_f32_16x16x32_bf16 v[16:19], a[20:23], v[188:191], v[16:19]
ds_read_b128 a[0:3], v225 offset:49152
ds_read_b128 v[156:159], v67 offset:768
v_mfma_f32_16x16x32_bf16 v[48:51], a[16:19], v[188:191], v[48:51]
ds_read_b32 v176, v68 offset:2048
ds_read_b32 v177, v68 offset:2176
v_mfma_f32_16x16x32_bf16 v[52:55], a[16:19], a[216:219], v[52:55]
ds_read_b128 a[224:227], v225 offset:51200
ds_read_b32 v178, v69 offset:2048
v_mfma_f32_16x16x32_bf16 v[20:23], a[20:23], a[216:219], v[20:23]
s_add_u32 m0, m0, 8192
s_add_u32 s84, s84, 8192
v_mfma_f32_16x16x32_bf16 v[24:27], a[20:23], a[60:63], v[24:27]
buffer_load_dwordx4 v208, s[36:39], s84 offen offset:0 sc1 lds
v_mfma_f32_16x16x32_bf16 v[56:59], a[16:19], a[60:63], v[56:59]
ds_read_b128 a[208:211], v225 offset:53248
ds_read_b32 v179, v69 offset:2176
v_mfma_f32_16x16x32_bf16 v[60:63], a[16:19], v[120:123], v[60:63]
ds_read_b128 a[124:127], v225 offset:55296
v_mfma_f32_16x16x32_bf16 v[28:31], a[20:23], v[120:123], v[28:31]
buffer_load_dwordx4 v208, s[36:39], s84 offen offset:1024 sc1 lds
s_waitcnt lgkmcnt(10)
v_mfma_f32_16x16x32_bf16 v[0:3], a[116:119], a[120:123], v[0:3]
ds_read_b128 a[244:247], v232 offset:57344
v_mfma_f32_16x16x32_bf16 v[32:35], a[12:15], a[120:123], v[32:35]
ds_read_b128 a[240:243], v232 offset:59392
v_mfma_f32_16x16x32_bf16 v[36:39], a[12:15], v[196:199], v[36:39]
s_add_u32 m0, m0, 8192
s_add_u32 s84, s84, 8192
v_mfma_f32_16x16x32_bf16 v[4:7], a[116:119], v[196:199], v[4:7]
buffer_load_dwordx4 v208, s[36:39], s84 offen offset:0 sc1 lds
v_mfma_f32_16x16x32_bf16 v[8:11], a[116:119], a[212:215], v[8:11]
ds_read_b128 a[108:111], v232 offset:61440
v_mfma_f32_16x16x32_bf16 v[40:43], a[12:15], a[212:215], v[40:43]
ds_read_b128 a[232:235], v232 offset:63488
v_mfma_f32_16x16x32_bf16 v[44:47], a[12:15], a[252:255], v[44:47]
buffer_load_dwordx4 v208, s[36:39], s84 offen offset:1024 sc1 lds
v_mfma_f32_16x16x32_bf16 v[12:15], a[116:119], a[252:255], v[12:15]
s_waitcnt lgkmcnt(4)
v_mfma_f32_16x16x32_bf16 v[16:19], a[116:119], a[0:3], v[16:19]
ds_read_b128 a[228:231], v225 offset:57344
v_mfma_f32_16x16x32_bf16 v[48:51], a[12:15], a[0:3], v[48:51]
v_mfma_f32_16x16x32_bf16 v[52:55], a[12:15], a[224:227], v[52:55]
ds_read_b128 a[248:251], v225 offset:59392
v_mfma_f32_16x16x32_bf16 v[20:23], a[116:119], a[224:227], v[20:23]
s_add_u32 s17, s9, 20480
v_mfma_f32_16x16x32_bf16 v[24:27], a[116:119], a[208:211], v[24:27]
buffer_load_dwordx4 a[136:139], v208, s[36:39], s17 offen offset:0 sc1
v_mfma_f32_16x16x32_bf16 v[56:59], a[12:15], a[208:211], v[56:59]
ds_read_b128 a[96:99], v225 offset:61440
v_mfma_f32_16x16x32_bf16 v[60:63], a[12:15], a[124:127], v[60:63]
ds_read_b128 a[140:143], v225 offset:63488
v_mfma_f32_16x16x32_bf16 v[28:31], a[116:119], a[124:127], v[28:31]
s_waitcnt lgkmcnt(4)
s_add_u32 s18, s10, 0
v_mfma_f32_16x16x32_bf16 v[0:3], v[124:127], a[244:247], v[0:3]
buffer_load_dwordx4 a[64:67], v208, s[36:39], s18 offen offset:0 sc1
v_mfma_f32_16x16x32_bf16 v[32:35], a[92:95], a[244:247], v[32:35]
v_add_u32 v232, 0x10000, v232
v_add_u32 v225, 0x10000, v225
v_mfma_f32_16x16x32_bf16 v[36:39], a[92:95], a[240:243], v[36:39]
ds_read_b128 a[236:239], v232 offset:0
v_mfma_f32_16x16x32_bf16 v[4:7], v[124:127], a[240:243], v[4:7]
ds_read_b128 a[68:71], v232 offset:2048
v_mfma_f32_16x16x32_bf16 v[8:11], v[124:127], a[108:111], v[8:11]
ds_read_b128 a[72:75], v232 offset:4096
s_add_u32 s19, s11, 0
v_mfma_f32_16x16x32_bf16 v[40:43], a[92:95], a[108:111], v[40:43]
buffer_load_dwordx4 a[76:79], v208, s[36:39], s19 offen offset:0 sc1
v_mfma_f32_16x16x32_bf16 v[44:47], a[92:95], a[232:235], v[44:47]
ds_read_b128 a[80:83], v232 offset:6144
v_mfma_f32_16x16x32_bf16 v[12:15], v[124:127], a[232:235], v[12:15]
s_waitcnt lgkmcnt(4)
v_mfma_f32_16x16x32_bf16 v[16:19], v[124:127], a[228:231], v[16:19]
ds_read_b128 a[128:131], v225 offset:0
v_mfma_f32_16x16x32_bf16 v[48:51], a[92:95], a[228:231], v[48:51]
buffer_load_dwordx4 a[132:135], v208, s[36:39], s18 offen offset:1024 sc1
v_mfma_f32_16x16x32_bf16 v[52:55], a[92:95], a[248:251], v[52:55]
ds_read_b128 a[84:87], v225 offset:2048
v_mfma_f32_16x16x32_bf16 v[20:23], v[124:127], a[248:251], v[20:23]
v_mfma_f32_16x16x32_bf16 v[24:27], v[124:127], a[96:99], v[24:27]
ds_read_b128 a[144:147], v225 offset:4096
v_mfma_f32_16x16x32_bf16 v[56:59], a[92:95], a[96:99], v[56:59]
buffer_load_dwordx4 a[88:91], v208, s[36:39], s19 offen offset:1024 sc1
v_mfma_f32_16x16x32_bf16 v[60:63], a[92:95], a[140:143], v[60:63]
ds_read_b128 a[52:55], v225 offset:6144
v_mfma_f32_16x16x32_bf16 v[28:31], v[124:127], a[140:143], v[28:31]
s_waitcnt lgkmcnt(4)
v_mfma_f32_16x16x32_bf16 v[0:3], a[164:167], a[236:239], v[0:3]
ds_read_b128 a[56:59], v232 offset:8192
v_mfma_f32_16x16x32_bf16 v[32:35], a[48:51], a[236:239], v[32:35]
v_mfma_f32_16x16x32_bf16 v[36:39], a[48:51], a[68:71], v[36:39]
buffer_load_dwordx4 a[112:115], v208, s[36:39], s18 offen offset:2048 sc1
v_mfma_f32_16x16x32_bf16 v[4:7], a[164:167], a[68:71], v[4:7]
ds_read_b128 a[188:191], v232 offset:10240
v_mfma_f32_16x16x32_bf16 v[8:11], a[164:167], a[72:75], v[8:11]
ds_read_b128 a[220:223], v232 offset:12288
v_mfma_f32_16x16x32_bf16 v[40:43], a[48:51], a[72:75], v[40:43]
v_mfma_f32_16x16x32_bf16 v[44:47], a[48:51], a[80:83], v[44:47]
buffer_load_dwordx4 a[24:27], v208, s[36:39], s19 offen offset:2048 sc1
v_mfma_f32_16x16x32_bf16 v[12:15], a[164:167], a[80:83], v[12:15]
ds_read_b128 a[28:31], v232 offset:14336
s_waitcnt lgkmcnt(4)
v_mfma_f32_16x16x32_bf16 v[16:19], a[164:167], a[128:131], v[16:19]
ds_read_b128 a[204:207], v225 offset:8192
v_mfma_f32_16x16x32_bf16 v[48:51], a[48:51], a[128:131], v[48:51]
v_mfma_f32_16x16x32_bf16 v[52:55], a[48:51], a[84:87], v[52:55]
ds_read_b128 a[196:199], v225 offset:10240
v_mfma_f32_16x16x32_bf16 v[20:23], a[164:167], a[84:87], v[20:23]
buffer_load_dwordx4 a[4:7], v208, s[36:39], s18 offen offset:3072 sc1
v_mfma_f32_16x16x32_bf16 v[24:27], a[164:167], a[144:147], v[24:27]
ds_read_b128 a[8:11], v225 offset:12288
v_mfma_f32_16x16x32_bf16 v[56:59], a[48:51], a[144:147], v[56:59]
ds_read_b128 a[192:195], v225 offset:14336
v_mfma_f32_16x16x32_bf16 v[60:63], a[48:51], a[52:55], v[60:63]
buffer_load_dwordx4 v[188:191], v208, s[36:39], s19 offen offset:3072 sc1
v_mfma_f32_16x16x32_bf16 v[28:31], a[164:167], a[52:55], v[28:31]
s_waitcnt lgkmcnt(4)
v_mfma_f32_16x16x32_bf16 v[0:3], a[44:47], a[56:59], v[0:3]
s_waitcnt vmcnt(19)
s_barrier
v_mfma_f32_16x16x32_bf16 v[32:35], a[40:43], a[56:59], v[32:35]
ds_read_b128 a[216:219], v232 offset:16384
ds_read_b128 a[60:63], v232 offset:18432
v_mfma_f32_16x16x32_bf16 v[36:39], a[40:43], a[188:191], v[36:39]
s_add_u32 m0, m0, 8192
s_add_u32 s84, s84, 8192
v_mfma_f32_16x16x32_bf16 v[4:7], a[44:47], a[188:191], v[4:7]
buffer_load_dwordx4 v208, s[36:39], s84 offen offset:0 sc1 lds
v_mfma_f32_16x16x32_bf16 v[8:11], a[44:47], a[220:223], v[8:11]
ds_read_b128 a[16:19], v232 offset:20480
v_mfma_f32_16x16x32_bf16 v[40:43], a[40:43], a[220:223], v[40:43]
buffer_load_dwordx4 v208, s[36:39], s84 offen offset:1024 sc1 lds
v_mfma_f32_16x16x32_bf16 v[44:47], a[40:43], a[28:31], v[44:47]
ds_read_b128 a[20:23], v232 offset:22528
v_mfma_f32_16x16x32_bf16 v[12:15], a[44:47], a[28:31], v[12:15]
s_waitcnt lgkmcnt(4)
v_mfma_f32_16x16x32_bf16 v[16:19], a[44:47], a[204:207], v[16:19]
ds_read_b128 v[120:123], v225 offset:16384
v_mfma_f32_16x16x32_bf16 v[48:51], a[40:43], a[204:207], v[48:51]
s_add_u32 s18, s10, 4096
v_mfma_f32_16x16x32_bf16 v[52:55], a[40:43], a[196:199], v[52:55]
buffer_load_dwordx4 a[120:123], v208, s[36:39], s18 offen offset:0 sc1
v_mfma_f32_16x16x32_bf16 v[20:23], a[44:47], a[196:199], v[20:23]
ds_read_b128 v[196:199], v225 offset:18432
v_mfma_f32_16x16x32_bf16 v[24:27], a[44:47], a[8:11], v[24:27]
ds_read_b128 a[212:215], v225 offset:20480
v_mfma_f32_16x16x32_bf16 v[56:59], a[40:43], a[8:11], v[56:59]
s_add_u32 s19, s11, 4096
v_mfma_f32_16x16x32_bf16 v[60:63], a[40:43], a[192:195], v[60:63]
buffer_load_dwordx4 a[252:255], v208, s[36:39], s19 offen offset:0 sc1
v_mfma_f32_16x16x32_bf16 v[28:31], a[44:47], a[192:195], v[28:31]
ds_read_b128 a[0:3], v225 offset:22528
s_waitcnt lgkmcnt(4)
v_mfma_f32_16x16x32_bf16 v[0:3], a[36:39], a[216:219], v[0:3]
ds_read_b128 a[224:227], v232 offset:24576
v_mfma_f32_16x16x32_bf16 v[32:35], a[160:163], a[216:219], v[32:35]
v_mfma_f32_16x16x32_bf16 v[36:39], a[160:163], a[60:63], v[36:39]
ds_read_b128 a[208:211], v232 offset:26624
v_mfma_f32_16x16x32_bf16 v[4:7], a[36:39], a[60:63], v[4:7]
buffer_load_dwordx4 a[12:15], v208, s[36:39], s18 offen offset:1024 sc1
v_mfma_f32_16x16x32_bf16 v[8:11], a[36:39], a[16:19], v[8:11]
ds_read_b128 a[116:119], v232 offset:28672
v_mfma_f32_16x16x32_bf16 v[40:43], a[160:163], a[16:19], v[40:43]
ds_read_b128 a[124:127], v232 offset:30720
v_mfma_f32_16x16x32_bf16 v[44:47], a[160:163], a[20:23], v[44:47]
buffer_load_dwordx4 a[244:247], v208, s[36:39], s19 offen offset:1024 sc1
v_mfma_f32_16x16x32_bf16 v[12:15], a[36:39], a[20:23], v[12:15]
s_waitcnt lgkmcnt(4)
v_mfma_f32_16x16x32_bf16 v[16:19], a[36:39], v[120:123], v[16:19]
ds_read_b128 a[240:243], v225 offset:24576
v_mfma_f32_16x16x32_bf16 v[48:51], a[160:163], v[120:123], v[48:51]
v_mfma_f32_16x16x32_bf16 v[52:55], a[160:163], v[196:199], v[52:55]
ds_read_b128 a[108:111], v225 offset:26624
v_mfma_f32_16x16x32_bf16 v[20:23], a[36:39], v[196:199], v[20:23]
buffer_load_dwordx4 a[232:235], v208, s[36:39], s18 offen offset:2048 sc1
v_mfma_f32_16x16x32_bf16 v[24:27], a[36:39], a[212:215], v[24:27]
ds_read_b128 a[228:231], v225 offset:28672
v_mfma_f32_16x16x32_bf16 v[56:59], a[160:163], a[212:215], v[56:59]
v_mfma_f32_16x16x32_bf16 v[60:63], a[160:163], a[0:3], v[60:63]
ds_read_b128 a[248:251], v225 offset:30720
v_mfma_f32_16x16x32_bf16 v[28:31], a[36:39], a[0:3], v[28:31]
s_waitcnt lgkmcnt(4)
v_mfma_f32_16x16x32_bf16 v[0:3], a[32:35], a[224:227], v[0:3]
buffer_load_dwordx4 a[96:99], v208, s[36:39], s19 offen offset:2048 sc1
v_mfma_f32_16x16x32_bf16 v[32:35], a[156:159], a[224:227], v[32:35]
ds_read_b128 a[92:95], v232 offset:32768
v_mfma_f32_16x16x32_bf16 v[36:39], a[156:159], a[208:211], v[36:39]
ds_read_b128 v[124:127], v232 offset:34816
v_mfma_f32_16x16x32_bf16 v[4:7], a[32:35], a[208:211], v[4:7]
v_mfma_f32_16x16x32_bf16 v[8:11], a[32:35], a[116:119], v[8:11]
buffer_load_dwordx4 a[140:143], v208, s[36:39], s18 offen offset:3072 sc1
v_mfma_f32_16x16x32_bf16 v[40:43], a[156:159], a[116:119], v[40:43]
ds_read_b128 a[236:239], v232 offset:36864
v_mfma_f32_16x16x32_bf16 v[44:47], a[156:159], a[124:127], v[44:47]
ds_read_b128 a[68:71], v232 offset:38912
v_mfma_f32_16x16x32_bf16 v[12:15], a[32:35], a[124:127], v[12:15]
s_waitcnt lgkmcnt(4)
v_mfma_f32_16x16x32_bf16 v[16:19], a[32:35], a[240:243], v[16:19]
ds_read_b128 a[72:75], v225 offset:32768
v_mfma_f32_16x16x32_bf16 v[48:51], a[156:159], a[240:243], v[48:51]
buffer_load_dwordx4 a[80:83], v208, s[36:39], s19 offen offset:3072 sc1
v_mfma_f32_16x16x32_bf16 v[52:55], a[156:159], a[108:111], v[52:55]
ds_read_b128 a[128:131], v225 offset:34816
v_mfma_f32_16x16x32_bf16 v[20:23], a[32:35], a[108:111], v[20:23]
v_mfma_f32_16x16x32_bf16 v[24:27], a[32:35], a[228:231], v[24:27]
ds_read_b128 a[84:87], v225 offset:36864
s_add_u32 s18, s10, 8192
v_mfma_f32_16x16x32_bf16 v[56:59], a[156:159], a[228:231], v[56:59]
buffer_load_dwordx4 a[144:147], v208, s[36:39], s18 offen offset:0 sc1
v_mfma_f32_16x16x32_bf16 v[60:63], a[156:159], a[248:251], v[60:63]
ds_read_b128 a[48:51], v225 offset:38912
v_mfma_f32_16x16x32_bf16 v[28:31], a[32:35], a[248:251], v[28:31]
s_waitcnt lgkmcnt(4)
ds_read_b128 a[164:167], v232 offset:40960
v_mfma_f32_16x16x32_bf16 v[0:3], a[152:155], a[92:95], v[0:3]
s_lshr_b32 s50, s65, 3
s_add_u32 s19, s11, 8192
v_mfma_f32_16x16x32_bf16 v[32:35], v[200:203], a[92:95], v[32:35]
buffer_load_dwordx4 a[52:55], v208, s[36:39], s19 offen offset:0 sc1
v_mfma_f32_16x16x32_bf16 v[36:39], v[200:203], v[124:127], v[36:39]
ds_read_b128 v[160:163], v232 offset:43008
s_mul_i32 s50, s50, 512
v_mfma_f32_16x16x32_bf16 v[4:7], a[152:155], v[124:127], v[4:7]
s_add_u32 s50, s50, .Lk_p3a_ok-.Lk_start-5120
v_and_b32 v214, 3, v209
v_mfma_f32_16x16x32_bf16 v[8:11], a[152:155], a[236:239], v[8:11]
ds_read_b128 v[164:167], v232 offset:45056
v_lshlrev_b32 v214, 7, v214
v_mfma_f32_16x16x32_bf16 v[40:43], v[200:203], a[236:239], v[40:43]
buffer_load_dword v214, v214, s[60:63], s50 offen
v_mfma_f32_16x16x32_bf16 v[44:47], v[200:203], a[68:71], v[44:47]
buffer_load_dwordx4 v[168:171], v208, s[36:39], s18 offen offset:1024 sc1
v_mfma_f32_16x16x32_bf16 v[12:15], a[152:155], a[68:71], v[12:15]
ds_read_b128 v[172:175], v232 offset:47104
s_waitcnt lgkmcnt(4)
v_mfma_f32_16x16x32_bf16 v[16:19], a[152:155], a[72:75], v[16:19]
ds_read_b128 a[56:59], v225 offset:40960
v_mfma_f32_16x16x32_bf16 v[48:51], v[200:203], a[72:75], v[48:51]
v_mfma_f32_16x16x32_bf16 v[52:55], v[200:203], a[128:131], v[52:55]
buffer_load_dwordx4 a[188:191], v208, s[36:39], s19 offen offset:1024 sc1
v_mfma_f32_16x16x32_bf16 v[20:23], a[152:155], a[128:131], v[20:23]
ds_read_b128 a[220:223], v225 offset:43008
v_mfma_f32_16x16x32_bf16 v[24:27], a[152:155], a[84:87], v[24:27]
ds_read_b128 a[28:31], v225 offset:45056
v_mfma_f32_16x16x32_bf16 v[56:59], v[200:203], a[84:87], v[56:59]
ds_read_b128 a[204:207], v225 offset:47104
v_mfma_f32_16x16x32_bf16 v[60:63], v[200:203], a[48:51], v[60:63]
buffer_load_dwordx4 a[196:199], v208, s[36:39], s18 offen offset:2048 sc1
v_mfma_f32_16x16x32_bf16 v[28:31], a[152:155], a[48:51], v[28:31]
s_waitcnt lgkmcnt(4)
v_mfma_f32_16x16x32_bf16 v[0:3], a[148:151], a[164:167], v[0:3]
s_waitcnt vmcnt(14)
s_barrier
v_mfma_f32_16x16x32_bf16 v[32:35], v[204:207], a[164:167], v[32:35]
ds_read_b128 a[8:11], v232 offset:49152
v_mfma_f32_16x16x32_bf16 v[36:39], v[204:207], v[160:163], v[36:39]
ds_read_b128 a[40:43], v232 offset:51200
v_mfma_f32_16x16x32_bf16 v[4:7], a[148:151], v[160:163], v[4:7]
buffer_load_dwordx4 a[44:47], v208, s[36:39], s19 offen offset:2048 sc1
v_mfma_f32_16x16x32_bf16 v[8:11], a[148:151], v[164:167], v[8:11]
ds_read_b128 a[192:195], v232 offset:53248
v_mfma_f32_16x16x32_bf16 v[40:43], v[204:207], v[164:167], v[40:43]
ds_read_b128 a[216:219], v232 offset:55296
v_mfma_f32_16x16x32_bf16 v[44:47], v[204:207], v[172:175], v[44:47]
buffer_load_dwordx4 a[60:63], v208, s[36:39], s18 offen offset:3072 sc1
v_mfma_f32_16x16x32_bf16 v[12:15], a[148:151], v[172:175], v[12:15]
s_waitcnt lgkmcnt(4)
v_mfma_f32_16x16x32_bf16 v[16:19], a[148:151], a[56:59], v[16:19]
ds_read_b128 a[16:19], v225 offset:49152
v_mfma_f32_16x16x32_bf16 v[48:51], v[204:207], a[56:59], v[48:51]
v_mfma_f32_16x16x32_bf16 v[52:55], v[204:207], a[220:223], v[52:55]
ds_read_b128 a[20:23], v225 offset:51200
v_mfma_f32_16x16x32_bf16 v[20:23], a[148:151], a[220:223], v[20:23]
v_mfma_f32_16x16x32_bf16 v[24:27], a[148:151], a[28:31], v[24:27]
buffer_load_dwordx4 v[120:123], v208, s[36:39], s19 offen offset:3072 sc1
v_mfma_f32_16x16x32_bf16 v[56:59], v[204:207], a[28:31], v[56:59]
ds_read_b128 v[196:199], v225 offset:53248
v_mfma_f32_16x16x32_bf16 v[60:63], v[204:207], a[204:207], v[60:63]
ds_read_b128 a[212:215], v225 offset:55296
v_mfma_f32_16x16x32_bf16 v[28:31], a[148:151], a[204:207], v[28:31]
s_waitcnt lgkmcnt(4)
s_add_u32 s18, s10, 12288
v_mfma_f32_16x16x32_bf16 v[0:3], v[184:187], a[8:11], v[0:3]
buffer_load_dwordx4 a[160:163], v208, s[36:39], s18 offen offset:0 sc1
v_mfma_f32_16x16x32_bf16 v[32:35], a[100:103], a[8:11], v[32:35]
ds_read_b128 a[36:39], v232 offset:57344
v_mfma_f32_16x16x32_bf16 v[36:39], a[100:103], a[40:43], v[36:39]
ds_read_b128 a[0:3], v232 offset:59392
v_mfma_f32_16x16x32_bf16 v[4:7], v[184:187], a[40:43], v[4:7]
v_mfma_f32_16x16x32_bf16 v[8:11], v[184:187], a[192:195], v[8:11]
ds_read_b128 a[224:227], v232 offset:61440
s_add_u32 s19, s11, 12288
v_mfma_f32_16x16x32_bf16 v[40:43], a[100:103], a[192:195], v[40:43]
buffer_load_dwordx4 a[208:211], v208, s[36:39], s19 offen offset:0 sc1
v_mfma_f32_16x16x32_bf16 v[44:47], a[100:103], a[216:219], v[44:47]
ds_read_b128 a[116:119], v232 offset:63488
v_mfma_f32_16x16x32_bf16 v[12:15], v[184:187], a[216:219], v[12:15]
s_waitcnt lgkmcnt(4)
v_mfma_f32_16x16x32_bf16 v[16:19], v[184:187], a[16:19], v[16:19]
ds_read_b128 a[124:127], v225 offset:57344
v_mfma_f32_16x16x32_bf16 v[48:51], a[100:103], a[16:19], v[48:51]
buffer_load_dwordx4 a[240:243], v208, s[36:39], s18 offen offset:1024 sc1
v_mfma_f32_16x16x32_bf16 v[52:55], a[100:103], a[20:23], v[52:55]
ds_read_b128 a[108:111], v225 offset:59392
v_mfma_f32_16x16x32_bf16 v[20:23], v[184:187], a[20:23], v[20:23]
v_mfma_f32_16x16x32_bf16 v[24:27], v[184:187], v[196:199], v[24:27]
ds_read_b128 a[228:231], v225 offset:61440
v_mfma_f32_16x16x32_bf16 v[56:59], a[100:103], v[196:199], v[56:59]
buffer_load_dwordx4 a[156:159], v208, s[36:39], s19 offen offset:1024 sc1
v_mfma_f32_16x16x32_bf16 v[60:63], a[100:103], a[212:215], v[60:63]
ds_read_b128 a[32:35], v225 offset:63488
v_mfma_f32_16x16x32_bf16 v[28:31], v[184:187], a[212:215], v[28:31]
s_waitcnt lgkmcnt(4)
v_mfma_f32_16x16x32_bf16 v[0:3], a[184:187], a[36:39], v[0:3]
v_add_u32 v232, 0x10000, v232
v_add_u32 v225, 0x10000, v225
v_mfma_f32_16x16x32_bf16 v[32:35], a[180:183], a[36:39], v[32:35]
ds_read_b128 a[248:251], v232 offset:0
v_mfma_f32_16x16x32_bf16 v[36:39], a[180:183], a[0:3], v[36:39]
buffer_load_dwordx4 a[92:95], v208, s[36:39], s18 offen offset:2048 sc1
v_mfma_f32_16x16x32_bf16 v[4:7], a[184:187], a[0:3], v[4:7]
ds_read_b128 v[124:127], v232 offset:2048
v_mfma_f32_16x16x32_bf16 v[8:11], a[184:187], a[224:227], v[8:11]
ds_read_b128 a[236:239], v232 offset:4096
v_mfma_f32_16x16x32_bf16 v[40:43], a[180:183], a[224:227], v[40:43]
v_mfma_f32_16x16x32_bf16 v[44:47], a[180:183], a[116:119], v[44:47]
buffer_load_dwordx4 a[68:71], v208, s[36:39], s19 offen offset:2048 sc1
v_mfma_f32_16x16x32_bf16 v[12:15], a[184:187], a[116:119], v[12:15]
ds_read_b128 a[72:75], v232 offset:6144
s_waitcnt lgkmcnt(4)
v_mfma_f32_16x16x32_bf16 v[16:19], a[184:187], a[124:127], v[16:19]
ds_read_b128 a[128:131], v225 offset:0
v_mfma_f32_16x16x32_bf16 v[48:51], a[180:183], a[124:127], v[48:51]
v_mfma_f32_16x16x32_bf16 v[52:55], a[180:183], a[108:111], v[52:55]
ds_read_b128 a[84:87], v225 offset:2048
v_mfma_f32_16x16x32_bf16 v[20:23], a[184:187], a[108:111], v[20:23]
buffer_load_dwordx4 v[200:203], v208, s[36:39], s18 offen offset:3072 sc1
v_mfma_f32_16x16x32_bf16 v[24:27], a[184:187], a[228:231], v[24:27]
ds_read_b128 a[152:155], v225 offset:4096
v_mfma_f32_16x16x32_bf16 v[56:59], a[180:183], a[228:231], v[56:59]
ds_read_b128 a[48:51], v225 offset:6144
v_mfma_f32_16x16x32_bf16 v[60:63], a[180:183], a[32:35], v[60:63]
buffer_load_dwordx4 a[164:167], v208, s[36:39], s19 offen offset:3072 sc1
v_mfma_f32_16x16x32_bf16 v[28:31], a[184:187], a[32:35], v[28:31]
s_waitcnt lgkmcnt(4)
v_mfma_f32_16x16x32_bf16 v[0:3], a[176:179], a[248:251], v[0:3]
ds_read_b128 v[160:163], v232 offset:8192
v_mfma_f32_16x16x32_bf16 v[32:35], a[172:175], a[248:251], v[32:35]
v_mfma_f32_16x16x32_bf16 v[36:39], a[172:175], v[124:127], v[36:39]
ds_read_b128 v[164:167], v232 offset:10240
s_add_u32 s18, s10, 16384
v_mfma_f32_16x16x32_bf16 v[4:7], a[176:179], v[124:127], v[4:7]
buffer_load_dwordx4 v[172:175], v208, s[36:39], s18 offen offset:0 sc1
v_mfma_f32_16x16x32_bf16 v[8:11], a[176:179], a[236:239], v[8:11]
ds_read_b128 a[56:59], v232 offset:12288
v_mfma_f32_16x16x32_bf16 v[40:43], a[172:175], a[236:239], v[40:43]
v_mfma_f32_16x16x32_bf16 v[44:47], a[172:175], a[72:75], v[44:47]
ds_read_b128 a[220:223], v232 offset:14336
v_mfma_f32_16x16x32_bf16 v[12:15], a[176:179], a[72:75], v[12:15]
s_waitcnt lgkmcnt(4)
s_add_u32 s19, s11, 16384
v_mfma_f32_16x16x32_bf16 v[16:19], a[176:179], a[128:131], v[16:19]
buffer_load_dwordx4 a[28:31], v208, s[36:39], s19 offen offset:0 sc1
v_mfma_f32_16x16x32_bf16 v[48:51], a[172:175], a[128:131], v[48:51]
ds_read_b128 v[204:207], v225 offset:8192
v_mfma_f32_16x16x32_bf16 v[52:55], a[172:175], a[84:87], v[52:55]
ds_read_b128 a[148:151], v225 offset:10240
v_mfma_f32_16x16x32_bf16 v[20:23], a[176:179], a[84:87], v[20:23]
v_mfma_f32_16x16x32_bf16 v[24:27], a[176:179], a[152:155], v[24:27]
buffer_load_dwordx4 a[204:207], v208, s[36:39], s18 offen offset:1024 sc1
v_mfma_f32_16x16x32_bf16 v[56:59], a[172:175], a[152:155], v[56:59]
ds_read_b128 a[8:11], v225 offset:12288
v_mfma_f32_16x16x32_bf16 v[60:63], a[172:175], a[48:51], v[60:63]
ds_read_b128 a[40:43], v225 offset:14336
v_mfma_f32_16x16x32_bf16 v[28:31], a[176:179], a[48:51], v[28:31]
s_waitcnt lgkmcnt(4)
v_mfma_f32_16x16x32_bf16 v[0:3], a[104:107], v[160:163], v[0:3]
ds_read_b128 a[192:195], v232 offset:16384
v_mfma_f32_16x16x32_bf16 v[32:35], a[168:171], v[160:163], v[32:35]
buffer_load_dwordx4 a[216:219], v208, s[36:39], s19 offen offset:1024 sc1
v_mfma_f32_16x16x32_bf16 v[36:39], a[168:171], v[164:167], v[36:39]
ds_read_b128 a[16:19], v232 offset:18432
v_mfma_f32_16x16x32_bf16 v[4:7], a[104:107], v[164:167], v[4:7]
v_mfma_f32_16x16x32_bf16 v[8:11], a[104:107], a[56:59], v[8:11]
ds_read_b128 a[20:23], v232 offset:20480
v_mfma_f32_16x16x32_bf16 v[40:43], a[168:171], a[56:59], v[40:43]
buffer_load_dwordx4 v[196:199], v208, s[36:39], s18 offen offset:2048 sc1
v_mfma_f32_16x16x32_bf16 v[44:47], a[168:171], a[220:223], v[44:47]
ds_read_b128 a[100:103], v232 offset:22528
v_mfma_f32_16x16x32_bf16 v[12:15], a[104:107], a[220:223], v[12:15]
s_waitcnt lgkmcnt(4)
v_mfma_f32_16x16x32_bf16 v[16:19], a[104:107], v[204:207], v[16:19]
ds_read_b128 v[184:187], v225 offset:16384
v_mfma_f32_16x16x32_bf16 v[48:51], a[168:171], v[204:207], v[48:51]
buffer_load_dwordx4 a[212:215], v208, s[36:39], s19 offen offset:2048 sc1
v_mfma_f32_16x16x32_bf16 v[52:55], a[168:171], a[148:151], v[52:55]
ds_read_b128 a[36:39], v225 offset:18432
v_mfma_f32_16x16x32_bf16 v[20:23], a[104:107], a[148:151], v[20:23]
v_mfma_f32_16x16x32_bf16 v[24:27], a[104:107], a[8:11], v[24:27]
ds_read_b128 a[0:3], v225 offset:20480
v_mfma_f32_16x16x32_bf16 v[56:59], a[168:171], a[8:11], v[56:59]
v_mfma_f32_16x16x32_bf16 v[60:63], a[168:171], a[40:43], v[60:63]
buffer_load_dwordx4 a[224:227], v208, s[36:39], s18 offen offset:3072 sc1
v_mfma_f32_16x16x32_bf16 v[28:31], a[104:107], a[40:43], v[28:31]
ds_read_b128 a[116:119], v225 offset:22528
s_waitcnt lgkmcnt(4)
v_mfma_f32_16x16x32_bf16 v[0:3], a[200:203], a[192:195], v[0:3]
ds_read_b128 a[124:127], v232 offset:24576
v_mfma_f32_16x16x32_bf16 v[32:35], v[104:107], a[192:195], v[32:35]
v_mfma_f32_16x16x32_bf16 v[36:39], v[104:107], a[16:19], v[36:39]
buffer_load_dwordx4 a[108:111], v208, s[36:39], s19 offen offset:3072 sc1
v_mfma_f32_16x16x32_bf16 v[4:7], a[200:203], a[16:19], v[4:7]
ds_read_b128 a[228:231], v232 offset:26624
v_mfma_f32_16x16x32_bf16 v[8:11], a[200:203], a[20:23], v[8:11]
ds_read_b128 a[180:183], v232 offset:28672
v_mfma_f32_16x16x32_bf16 v[40:43], v[104:107], a[20:23], v[40:43]
ds_read_b128 a[184:187], v232 offset:30720
s_add_u32 s18, s10, 20480
v_mfma_f32_16x16x32_bf16 v[44:47], v[104:107], a[100:103], v[44:47]
buffer_load_dwordx4 a[32:35], v208, s[36:39], s18 offen offset:0 sc1
v_mfma_f32_16x16x32_bf16 v[12:15], a[200:203], a[100:103], v[12:15]
s_waitcnt lgkmcnt(4)
v_mfma_f32_16x16x32_bf16 v[16:19], a[200:203], v[184:187], v[16:19]
ds_read_b128 a[248:251], v225 offset:24576
v_mfma_f32_16x16x32_bf16 v[48:51], v[104:107], v[184:187], v[48:51]
v_mfma_f32_16x16x32_bf16 v[52:55], v[104:107], a[36:39], v[52:55]
ds_read_b128 v[124:127], v225 offset:26624
s_add_u32 s19, s11, 20480
v_mfma_f32_16x16x32_bf16 v[20:23], a[200:203], a[36:39], v[20:23]
buffer_load_dwordx4 a[236:239], v208, s[36:39], s19 offen offset:0 sc1
v_mfma_f32_16x16x32_bf16 v[24:27], a[200:203], a[0:3], v[24:27]
ds_read_b128 a[72:75], v225 offset:28672
v_mfma_f32_16x16x32_bf16 v[56:59], v[104:107], a[0:3], v[56:59]
ds_read_b128 a[128:131], v225 offset:30720
s_add_u32 s87, s85, 0
v_mfma_f32_16x16x32_bf16 v[60:63], v[104:107], a[116:119], v[60:63]
buffer_load_dwordx4 a[84:87], v208, s[36:39], s87 offen offset:0 sc1
v_mfma_f32_16x16x32_bf16 v[28:31], a[200:203], a[116:119], v[28:31]
s_waitcnt lgkmcnt(4)
v_mfma_f32_16x16x32_bf16 v[0:3], v[112:115], a[124:127], v[0:3]
v_add_u32 v232, 0xfffe0000, v232
v_add_u32 v225, 0xfffe0000, v225
v_mfma_f32_16x16x32_bf16 v[32:35], v[116:119], a[124:127], v[32:35]
ds_read_b128 a[152:155], v232 offset:0
v_mfma_f32_16x16x32_bf16 v[36:39], v[116:119], a[228:231], v[36:39]
ds_read_b128 a[172:175], v232 offset:2048
v_mfma_f32_16x16x32_bf16 v[4:7], v[112:115], a[228:231], v[4:7]
v_mfma_f32_16x16x32_bf16 v[8:11], v[112:115], a[180:183], v[8:11]
buffer_load_dwordx4 a[176:179], v208, s[36:39], s87 offen offset:1024 sc1
v_mfma_f32_16x16x32_bf16 v[40:43], v[116:119], a[180:183], v[40:43]
ds_read_b128 a[48:51], v232 offset:4096
v_mfma_f32_16x16x32_bf16 v[44:47], v[116:119], a[184:187], v[44:47]
ds_read_b128 v[160:163], v232 offset:6144
v_mfma_f32_16x16x32_bf16 v[12:15], v[112:115], a[184:187], v[12:15]
s_waitcnt lgkmcnt(4)
v_mfma_f32_16x16x32_bf16 v[16:19], v[112:115], a[248:251], v[16:19]
buffer_load_dwordx4 v[164:167], v208, s[36:39], s87 offen offset:2048 sc1
v_mfma_f32_16x16x32_bf16 v[48:51], v[116:119], a[248:251], v[48:51]
ds_read_b128 a[56:59], v225 offset:0
v_mfma_f32_16x16x32_bf16 v[52:55], v[116:119], v[124:127], v[52:55]
ds_read_b128 a[220:223], v225 offset:2048
v_mfma_f32_16x16x32_bf16 v[20:23], v[112:115], v[124:127], v[20:23]
v_mfma_f32_16x16x32_bf16 v[24:27], v[112:115], a[72:75], v[24:27]
ds_read_b128 v[204:207], v225 offset:4096
v_mfma_f32_16x16x32_bf16 v[56:59], v[116:119], a[72:75], v[56:59]
buffer_load_dwordx4 a[148:151], v208, s[36:39], s87 offen offset:3072 sc1
v_mfma_f32_16x16x32_bf16 v[60:63], v[116:119], a[128:131], v[60:63]
ds_read_b128 a[8:11], v225 offset:6144
v_mfma_f32_16x16x32_bf16 v[28:31], v[112:115], a[128:131], v[28:31]
v_mfma_f32_16x16x32_bf16 v[0:3], v[192:195], v[128:131], v[0:3]
s_add_u32 s87, s85, 4096
v_mfma_f32_16x16x32_bf16 v[32:35], a[136:139], v[128:131], v[32:35]
buffer_load_dwordx4 a[168:171], v208, s[36:39], s87 offen offset:0 sc1
v_mfma_f32_16x16x32_bf16 v[36:39], a[136:139], v[132:135], v[36:39]
v_mfma_f32_16x16x32_bf16 v[4:7], v[192:195], v[132:135], v[4:7]
v_mfma_f32_16x16x32_bf16 v[8:11], v[192:195], v[136:139], v[8:11]
v_mfma_f32_16x16x32_bf16 v[40:43], a[136:139], v[136:139], v[40:43]
buffer_load_dwordx4 a[104:107], v208, s[36:39], s87 offen offset:1024 sc1
v_mfma_f32_16x16x32_bf16 v[44:47], a[136:139], v[140:143], v[44:47]
v_mfma_f32_16x16x32_bf16 v[12:15], v[192:195], v[140:143], v[12:15]
v_mfma_f32_16x16x32_bf16 v[16:19], v[192:195], v[144:147], v[16:19]
v_mfma_f32_16x16x32_bf16 v[48:51], a[136:139], v[144:147], v[48:51]
v_mfma_f32_16x16x32_bf16 v[52:55], a[136:139], v[148:151], v[52:55]
buffer_load_dwordx4 a[40:43], v208, s[36:39], s87 offen offset:2048 sc1
v_mfma_f32_16x16x32_bf16 v[20:23], v[192:195], v[148:151], v[20:23]
v_mfma_f32_16x16x32_bf16 v[24:27], v[192:195], v[152:155], v[24:27]
v_mfma_f32_16x16x32_bf16 v[56:59], a[136:139], v[152:155], v[56:59]
v_mfma_f32_16x16x32_bf16 v[60:63], a[136:139], v[156:159], v[60:63]
buffer_load_dwordx4 a[192:195], v208, s[36:39], s87 offen offset:3072 sc1
v_mfma_f32_16x16x32_bf16 v[28:31], v[192:195], v[156:159], v[28:31]
s_waitcnt lgkmcnt(4)
ds_read_b128 a[16:19], v232 offset:8192
v_mfma_f32_16x16x32_bf16 v[64:67], a[64:67], a[152:155], 0
s_nop 1
s_nop 1
v_mfma_f32_16x16x32_bf16 v[96:99], a[76:79], a[152:155], 0
v_permlane32_swap_b32 v0, v4
v_permlane32_swap_b32 v1, v5
v_permlane32_swap_b32 v2, v6
v_mfma_f32_16x16x32_bf16 v[100:103], a[76:79], a[172:175], 0
ds_read_b128 a[20:23], v232 offset:10240
v_permlane32_swap_b32 v3, v7
v_max_f32 v0, 0, v0
v_mfma_f32_16x16x32_bf16 v[68:71], a[64:67], a[172:175], 0
v_max_f32 v1, 0, v1
v_max_f32 v2, 0, v2
v_max_f32 v3, 0, v3
v_mfma_f32_16x16x32_bf16 v[72:75], a[64:67], a[48:51], 0
s_add_u32 s87, s85, 8192
buffer_load_dwordx4 a[100:103], v208, s[36:39], s87 offen offset:0 sc1
v_mfma_f32_16x16x32_bf16 v[104:107], a[76:79], a[48:51], 0
ds_read_b128 v[184:187], v232 offset:12288
v_mul_f32 v4, v176, v4
v_mul_f32 v5, v176, v5
v_mfma_f32_16x16x32_bf16 v[108:111], a[76:79], v[160:163], 0
ds_read_b128 a[36:39], v232 offset:14336
v_mul_f32 v6, v176, v6
v_mul_f32 v7, v176, v7
v_mfma_f32_16x16x32_bf16 v[76:79], a[64:67], v[160:163], 0
v_mul_f32 v0, v0, v4
v_mul_f32 v1, v1, v5
v_mul_f32 v2, v2, v6
s_waitcnt lgkmcnt(4)
v_mfma_f32_16x16x32_bf16 v[80:83], a[64:67], a[56:59], 0
ds_read_b128 a[0:3], v225 offset:8192
v_mul_f32 v3, v3, v7
v_cvt_pk_bf16_f32 v0, v0, v1
v_mfma_f32_16x16x32_bf16 v[84:87], a[64:67], a[220:223], 0
buffer_load_dwordx4 a[200:203], v208, s[36:39], s87 offen offset:1024 sc1
v_mfma_f32_16x16x32_bf16 v[88:91], a[64:67], v[204:207], 0
ds_read_b128 a[116:119], v225 offset:10240
v_cvt_pk_bf16_f32 v1, v2, v3
v_permlane32_swap_b32 v8, v12
v_mfma_f32_16x16x32_bf16 v[92:95], a[64:67], a[8:11], 0
ds_read_b128 a[124:127], v225 offset:12288
v_permlane32_swap_b32 v9, v13
v_permlane32_swap_b32 v10, v14
s_waitcnt lgkmcnt(3)
v_mfma_f32_16x16x32_bf16 v[64:67], a[132:135], a[16:19], v[64:67]
ds_read_b128 a[228:231], v225 offset:14336
v_permlane32_swap_b32 v11, v15
v_max_f32 v8, 0, v8
v_mfma_f32_16x16x32_bf16 v[96:99], a[88:91], a[16:19], v[96:99]
ds_read_b128 a[180:183], v232 offset:16384
v_max_f32 v9, 0, v9
v_max_f32 v10, 0, v10
v_mfma_f32_16x16x32_bf16 v[100:103], a[88:91], a[20:23], v[100:103]
ds_read_b128 a[184:187], v232 offset:18432
v_max_f32 v11, 0, v11
v_mul_f32 v12, v177, v12
v_mfma_f32_16x16x32_bf16 v[68:71], a[132:135], a[20:23], v[68:71]
buffer_load_dwordx4 a[248:251], v208, s[36:39], s87 offen offset:2048 sc1
v_mfma_f32_16x16x32_bf16 v[72:75], a[132:135], v[184:187], v[72:75]
ds_read_b128 v[124:127], v232 offset:20480
v_mul_f32 v13, v177, v13
v_mul_f32 v14, v177, v14
v_mfma_f32_16x16x32_bf16 v[104:107], a[88:91], v[184:187], v[104:107]
v_mul_f32 v15, v177, v15
v_mul_f32 v8, v8, v12
v_mul_f32 v9, v9, v13
v_mfma_f32_16x16x32_bf16 v[108:111], a[88:91], a[36:39], v[108:111]
ds_read_b128 a[72:75], v232 offset:22528
v_mul_f32 v10, v10, v14
v_mul_f32 v11, v11, v15
v_mfma_f32_16x16x32_bf16 v[76:79], a[132:135], a[36:39], v[76:79]
v_cvt_pk_bf16_f32 v2, v8, v9
v_cvt_pk_bf16_f32 v3, v10, v11
s_nop 0
v_permlane16_swap_b32 v0, v2
s_waitcnt lgkmcnt(4)
v_mfma_f32_16x16x32_bf16 v[80:83], a[132:135], a[0:3], v[80:83]
buffer_load_dwordx4 v[116:119], v208, s[36:39], s87 offen offset:3072 sc1
v_mfma_f32_16x16x32_bf16 v[84:87], a[132:135], a[116:119], v[84:87]
ds_read_b128 v[112:115], v225 offset:16384
v_permlane16_swap_b32 v1, v3
v_mfma_f32_16x16x32_bf16 v[88:91], a[132:135], a[124:127], v[88:91]
ds_read_b128 a[128:131], v225 offset:18432
v_mfma_f32_16x16x32_bf16 v[92:95], a[132:135], a[228:231], v[92:95]
ds_read_b128 a[136:139], v225 offset:20480
s_waitcnt lgkmcnt(3)
v_mfma_f32_16x16x32_bf16 v[64:67], a[112:115], a[180:183], v[64:67]
ds_read_b128 v[192:195], v225 offset:22528
v_mfma_f32_16x16x32_bf16 v[96:99], a[24:27], a[180:183], v[96:99]
ds_read_b128 a[152:155], v232 offset:24576
s_add_u32 s87, s85, 12288
v_mfma_f32_16x16x32_bf16 v[100:103], a[24:27], a[184:187], v[100:103]
buffer_load_dwordx4 a[172:175], v208, s[36:39], s87 offen offset:0 sc1
v_mfma_f32_16x16x32_bf16 v[68:71], a[112:115], a[184:187], v[68:71]
ds_read_b128 a[48:51], v232 offset:26624
ds_read_b128 a[76:79], v232 offset:28672
v_mfma_f32_16x16x32_bf16 v[72:75], a[112:115], v[124:127], v[72:75]
v_mfma_f32_16x16x32_bf16 v[104:107], a[24:27], v[124:127], v[104:107]
buffer_store_dwordx4 v[0:3], v233, s[36:39], s22 offen offset:0 sc1
v_mfma_f32_16x16x32_bf16 v[108:111], a[24:27], a[72:75], v[108:111]
ds_read_b128 v[160:163], v232 offset:30720
v_permlane32_swap_b32 v16, v20
v_permlane32_swap_b32 v17, v21
v_mfma_f32_16x16x32_bf16 v[76:79], a[112:115], a[72:75], v[76:79]
buffer_load_dwordx4 a[56:59], v208, s[36:39], s87 offen offset:1024 sc1
s_waitcnt lgkmcnt(4)
v_mfma_f32_16x16x32_bf16 v[80:83], a[112:115], v[112:115], v[80:83]
ds_read_b128 a[220:223], v225 offset:24576
v_permlane32_swap_b32 v18, v22
v_permlane32_swap_b32 v19, v23
v_mfma_f32_16x16x32_bf16 v[84:87], a[112:115], a[128:131], v[84:87]
ds_read_b128 v[204:207], v225 offset:26624
v_max_f32 v16, 0, v16
v_max_f32 v17, 0, v17
v_mfma_f32_16x16x32_bf16 v[88:91], a[112:115], a[136:139], v[88:91]
ds_read_b128 a[64:67], v225 offset:28672
v_max_f32 v18, 0, v18
v_max_f32 v19, 0, v19
v_mfma_f32_16x16x32_bf16 v[92:95], a[112:115], v[192:195], v[92:95]
ds_read_b128 a[8:11], v225 offset:30720
v_mul_f32 v20, v178, v20
v_mul_f32 v21, v178, v21
s_waitcnt lgkmcnt(4)
v_mfma_f32_16x16x32_bf16 v[64:67], a[4:7], a[152:155], v[64:67]
ds_read_b128 a[16:19], v232 offset:32768
v_mul_f32 v22, v178, v22
v_mul_f32 v23, v178, v23
v_mfma_f32_16x16x32_bf16 v[96:99], v[188:191], a[152:155], v[96:99]
buffer_load_dwordx4 a[20:23], v208, s[36:39], s87 offen offset:2048 sc1
v_mfma_f32_16x16x32_bf16 v[100:103], v[188:191], a[48:51], v[100:103]
ds_read_b128 v[184:187], v232 offset:34816
v_mul_f32 v16, v16, v20
v_mul_f32 v17, v17, v21
v_mfma_f32_16x16x32_bf16 v[68:71], a[4:7], a[48:51], v[68:71]
v_mul_f32 v18, v18, v22
v_mul_f32 v19, v19, v23
v_cvt_pk_bf16_f32 v16, v16, v17
v_mfma_f32_16x16x32_bf16 v[72:75], a[4:7], a[76:79], v[72:75]
ds_read_b128 a[88:91], v232 offset:36864
v_cvt_pk_bf16_f32 v17, v18, v19
v_permlane32_swap_b32 v24, v28
v_mfma_f32_16x16x32_bf16 v[104:107], v[188:191], a[76:79], v[104:107]
v_permlane32_swap_b32 v25, v29
v_permlane32_swap_b32 v26, v30
v_permlane32_swap_b32 v27, v31
v_mfma_f32_16x16x32_bf16 v[108:111], v[188:191], v[160:163], v[108:111]
buffer_load_dwordx4 a[36:39], v208, s[36:39], s87 offen offset:3072 sc1
v_mfma_f32_16x16x32_bf16 v[76:79], a[4:7], v[160:163], v[76:79]
ds_read_b128 a[0:3], v232 offset:38912
v_max_f32 v24, 0, v24
v_max_f32 v25, 0, v25
s_waitcnt lgkmcnt(4)
v_mfma_f32_16x16x32_bf16 v[80:83], a[4:7], a[220:223], v[80:83]
ds_read_b128 a[116:119], v225 offset:32768
v_max_f32 v26, 0, v26
v_max_f32 v27, 0, v27
v_mfma_f32_16x16x32_bf16 v[84:87], a[4:7], v[204:207], v[84:87]
ds_read_b128 a[124:127], v225 offset:34816
v_mul_f32 v28, v179, v28
v_mul_f32 v29, v179, v29
v_mfma_f32_16x16x32_bf16 v[88:91], a[4:7], a[64:67], v[88:91]
ds_read_b128 a[132:135], v225 offset:36864
v_mul_f32 v30, v179, v30
v_mul_f32 v31, v179, v31
v_mfma_f32_16x16x32_bf16 v[92:95], a[4:7], a[8:11], v[92:95]
ds_read_b128 a[228:231], v225 offset:38912
v_mul_f32 v24, v24, v28
v_mul_f32 v25, v25, v29
s_waitcnt vmcnt(50) lgkmcnt(4)
v_mfma_f32_16x16x32_bf16 v[64:67], a[120:123], a[16:19], v[64:67]
s_add_u32 s87, s85, 16384
buffer_load_dwordx4 a[180:183], v208, s[36:39], s87 offen offset:0 sc1
v_mfma_f32_16x16x32_bf16 v[96:99], a[252:255], a[16:19], v[96:99]
ds_read_b128 a[184:187], v232 offset:40960
v_mul_f32 v26, v26, v30
v_mul_f32 v27, v27, v31
v_mfma_f32_16x16x32_bf16 v[100:103], a[252:255], v[184:187], v[100:103]
ds_read_b128 v[124:127], v232 offset:43008
v_cvt_pk_bf16_f32 v18, v24, v25
v_mfma_f32_16x16x32_bf16 v[68:71], a[120:123], v[184:187], v[68:71]
v_cvt_pk_bf16_f32 v19, v26, v27
v_permlane16_swap_b32 v16, v18
v_mfma_f32_16x16x32_bf16 v[72:75], a[120:123], a[88:91], v[72:75]
ds_read_b128 a[24:27], v232 offset:45056
v_permlane16_swap_b32 v17, v19
v_mfma_f32_16x16x32_bf16 v[104:107], a[252:255], a[88:91], v[104:107]
buffer_load_dwordx4 a[72:75], v208, s[36:39], s87 offen offset:1024 sc1
v_mfma_f32_16x16x32_bf16 v[108:111], a[252:255], a[0:3], v[108:111]
ds_read_b128 v[112:115], v232 offset:47104
v_mfma_f32_16x16x32_bf16 v[76:79], a[120:123], a[0:3], v[76:79]
buffer_store_dwordx4 v[16:19], v233, s[36:39], s23 offen offset:0 sc1
s_waitcnt lgkmcnt(4)
v_mfma_f32_16x16x32_bf16 v[80:83], a[120:123], a[116:119], v[80:83]
ds_read_b128 a[128:131], v225 offset:40960
v_permlane32_swap_b32 v32, v36
v_permlane32_swap_b32 v33, v37
v_mfma_f32_16x16x32_bf16 v[84:87], a[120:123], a[124:127], v[84:87]
ds_read_b128 a[136:139], v225 offset:43008
v_permlane32_swap_b32 v34, v38
v_permlane32_swap_b32 v35, v39
v_mfma_f32_16x16x32_bf16 v[88:91], a[120:123], a[132:135], v[88:91]
ds_read_b128 a[112:115], v225 offset:45056
v_max_f32 v32, 0, v32
v_max_f32 v33, 0, v33
v_mfma_f32_16x16x32_bf16 v[92:95], a[120:123], a[228:231], v[92:95]
buffer_load_dwordx4 v[192:195], v208, s[36:39], s87 offen offset:2048 sc1
s_waitcnt vmcnt(52) lgkmcnt(3)
v_mfma_f32_16x16x32_bf16 v[64:67], a[12:15], a[184:187], v[64:67]
ds_read_b128 a[152:155], v225 offset:47104
v_max_f32 v34, 0, v34
v_max_f32 v35, 0, v35
v_mfma_f32_16x16x32_bf16 v[96:99], a[244:247], a[184:187], v[96:99]
ds_read_b128 a[48:51], v232 offset:49152
v_mul_f32 v36, v176, v36
v_mul_f32 v37, v176, v37
v_mfma_f32_16x16x32_bf16 v[100:103], a[244:247], v[124:127], v[100:103]
ds_read_b128 a[76:79], v232 offset:51200
v_mul_f32 v38, v176, v38
v_mul_f32 v39, v176, v39
v_mfma_f32_16x16x32_bf16 v[68:71], a[12:15], v[124:127], v[68:71]
v_mul_f32 v32, v32, v36
v_mul_f32 v33, v33, v37
v_mul_f32 v34, v34, v38
v_mfma_f32_16x16x32_bf16 v[72:75], a[12:15], a[24:27], v[72:75]
buffer_load_dwordx4 v[188:191], v208, s[36:39], s87 offen offset:3072 sc1
v_mfma_f32_16x16x32_bf16 v[104:107], a[244:247], a[24:27], v[104:107]
ds_read_b128 v[160:163], v232 offset:53248
v_mul_f32 v35, v35, v39
v_cvt_pk_bf16_f32 v32, v32, v33
v_mfma_f32_16x16x32_bf16 v[108:111], a[244:247], v[112:115], v[108:111]
ds_read_b128 a[220:223], v232 offset:55296
v_cvt_pk_bf16_f32 v33, v34, v35
v_permlane32_swap_b32 v40, v44
v_mfma_f32_16x16x32_bf16 v[76:79], a[12:15], v[112:115], v[76:79]
v_permlane32_swap_b32 v41, v45
v_permlane32_swap_b32 v42, v46
v_permlane32_swap_b32 v43, v47
s_waitcnt lgkmcnt(4)
v_mfma_f32_16x16x32_bf16 v[80:83], a[12:15], a[128:131], v[80:83]
ds_read_b128 v[204:207], v225 offset:49152
v_max_f32 v40, 0, v40
v_max_f32 v41, 0, v41
v_mfma_f32_16x16x32_bf16 v[84:87], a[12:15], a[136:139], v[84:87]
ds_read_b128 a[64:67], v225 offset:51200
v_max_f32 v42, 0, v42
v_max_f32 v43, 0, v43
v_mfma_f32_16x16x32_bf16 v[88:91], a[12:15], a[112:115], v[88:91]
s_add_u32 s87, s85, 20480
buffer_load_dwordx4 a[4:7], v208, s[36:39], s87 offen offset:0 sc1
v_mfma_f32_16x16x32_bf16 v[92:95], a[12:15], a[152:155], v[92:95]
ds_read_b128 a[8:11], v225 offset:53248
v_mul_f32 v44, v177, v44
v_mul_f32 v45, v177, v45
s_waitcnt vmcnt(52) lgkmcnt(3)
v_mfma_f32_16x16x32_bf16 v[64:67], a[232:235], a[48:51], v[64:67]
ds_read_b128 a[16:19], v225 offset:55296
v_mul_f32 v46, v177, v46
v_mul_f32 v47, v177, v47
v_mfma_f32_16x16x32_bf16 v[96:99], a[96:99], a[48:51], v[96:99]
ds_read_b128 v[184:187], v232 offset:57344
v_mul_f32 v40, v40, v44
v_mul_f32 v41, v41, v45
v_mfma_f32_16x16x32_bf16 v[100:103], a[96:99], a[76:79], v[100:103]
ds_read_b128 a[88:91], v232 offset:59392
v_mul_f32 v42, v42, v46
v_mul_f32 v43, v43, v47
v_mfma_f32_16x16x32_bf16 v[68:71], a[232:235], a[76:79], v[68:71]
buffer_load_dwordx4 a[252:255], v208, s[36:39], s87 offen offset:1024 sc1
v_mfma_f32_16x16x32_bf16 v[72:75], a[232:235], v[160:163], v[72:75]
ds_read_b128 a[0:3], v232 offset:61440
v_cvt_pk_bf16_f32 v34, v40, v41
v_mfma_f32_16x16x32_bf16 v[104:107], a[96:99], v[160:163], v[104:107]
v_cvt_pk_bf16_f32 v35, v42, v43
v_permlane16_swap_b32 v32, v34
v_mfma_f32_16x16x32_bf16 v[108:111], a[96:99], a[220:223], v[108:111]
ds_read_b128 a[116:119], v232 offset:63488
v_permlane16_swap_b32 v33, v35
v_mfma_f32_16x16x32_bf16 v[76:79], a[232:235], a[220:223], v[76:79]
buffer_store_dwordx4 v[32:35], v233, s[36:39], s22 offen offset:256 sc1
s_waitcnt lgkmcnt(4)
v_mfma_f32_16x16x32_bf16 v[80:83], a[232:235], v[204:207], v[80:83]
ds_read_b128 a[124:127], v225 offset:57344
v_permlane32_swap_b32 v48, v52
v_permlane32_swap_b32 v49, v53
v_mfma_f32_16x16x32_bf16 v[84:87], a[232:235], a[64:67], v[84:87]
buffer_load_dwordx4 a[132:135], v208, s[36:39], s87 offen offset:2048 sc1
v_mfma_f32_16x16x32_bf16 v[88:91], a[232:235], a[8:11], v[88:91]
ds_read_b128 a[120:123], v225 offset:59392
v_permlane32_swap_b32 v50, v54
v_permlane32_swap_b32 v51, v55
v_mfma_f32_16x16x32_bf16 v[92:95], a[232:235], a[16:19], v[92:95]
ds_read_b128 a[228:231], v225 offset:61440
v_max_f32 v48, 0, v48
v_max_f32 v49, 0, v49
s_waitcnt vmcnt(53) lgkmcnt(3)
v_mfma_f32_16x16x32_bf16 v[64:67], a[140:143], v[184:187], v[64:67]
ds_read_b128 a[184:187], v225 offset:63488
v_max_f32 v50, 0, v50
v_max_f32 v51, 0, v51
v_mfma_f32_16x16x32_bf16 v[96:99], a[80:83], v[184:187], v[96:99]
v_add_u32 v232, 0x10000, v232
v_add_u32 v225, 0x10000, v225
v_mul_f32 v52, v178, v52
v_mul_f32 v53, v178, v53
v_mfma_f32_16x16x32_bf16 v[100:103], a[80:83], a[88:91], v[100:103]
buffer_load_dwordx4 v[124:127], v208, s[36:39], s87 offen offset:3072 sc1
v_mfma_f32_16x16x32_bf16 v[68:71], a[140:143], a[88:91], v[68:71]
ds_read_b128 a[24:27], v232 offset:0
v_mul_f32 v54, v178, v54
v_mul_f32 v55, v178, v55
v_mfma_f32_16x16x32_bf16 v[72:75], a[140:143], a[0:3], v[72:75]
ds_read_b128 a[244:247], v232 offset:2048
v_mul_f32 v48, v48, v52
v_mul_f32 v49, v49, v53
v_mfma_f32_16x16x32_bf16 v[104:107], a[80:83], a[0:3], v[104:107]
ds_read_b128 v[112:115], v232 offset:4096
v_mul_f32 v50, v50, v54
v_mul_f32 v51, v51, v55
v_mfma_f32_16x16x32_bf16 v[108:111], a[80:83], a[116:119], v[108:111]
ds_read_b128 a[128:131], v232 offset:6144
v_cvt_pk_bf16_f32 v48, v48, v49
v_cvt_pk_bf16_f32 v49, v50, v51
v_mfma_f32_16x16x32_bf16 v[76:79], a[140:143], a[116:119], v[76:79]
v_permlane32_swap_b32 v56, v60
v_permlane32_swap_b32 v57, v61
v_permlane32_swap_b32 v58, v62
s_waitcnt lgkmcnt(4)
v_mfma_f32_16x16x32_bf16 v[80:83], a[140:143], a[124:127], v[80:83]
s_add_u32 s87, s85, 24576
buffer_load_dwordx4 a[136:139], v208, s[36:39], s87 offen offset:0 sc1
v_mfma_f32_16x16x32_bf16 v[84:87], a[140:143], a[120:123], v[84:87]
ds_read_b128 a[112:115], v225 offset:0
v_permlane32_swap_b32 v59, v63
v_max_f32 v56, 0, v56
v_mfma_f32_16x16x32_bf16 v[88:91], a[140:143], a[228:231], v[88:91]
ds_read_b128 a[12:15], v225 offset:2048
v_max_f32 v57, 0, v57
v_max_f32 v58, 0, v58
v_mfma_f32_16x16x32_bf16 v[92:95], a[140:143], a[184:187], v[92:95]
ds_read_b128 a[152:155], v225 offset:4096
v_max_f32 v59, 0, v59
v_mul_f32 v60, v179, v60
s_waitcnt vmcnt(53) lgkmcnt(3)
v_mfma_f32_16x16x32_bf16 v[64:67], a[144:147], a[24:27], v[64:67]
ds_read_b128 a[48:51], v225 offset:6144
v_mul_f32 v61, v179, v61
v_mul_f32 v62, v179, v62
v_mfma_f32_16x16x32_bf16 v[96:99], a[52:55], a[24:27], v[96:99]
buffer_load_dwordx4 a[76:79], v208, s[36:39], s87 offen offset:1024 sc1
v_mfma_f32_16x16x32_bf16 v[100:103], a[52:55], a[244:247], v[100:103]
ds_read_b128 v[160:163], v232 offset:8192
v_mul_f32 v63, v179, v63
v_mul_f32 v56, v56, v60
v_mfma_f32_16x16x32_bf16 v[68:71], a[144:147], a[244:247], v[68:71]
ds_read_b128 a[96:99], v232 offset:10240
v_mul_f32 v57, v57, v61
v_mul_f32 v58, v58, v62
v_mfma_f32_16x16x32_bf16 v[72:75], a[144:147], v[112:115], v[72:75]
ds_read_b128 a[220:223], v232 offset:12288
v_mul_f32 v59, v59, v63
v_cvt_pk_bf16_f32 v50, v56, v57
v_mfma_f32_16x16x32_bf16 v[104:107], a[52:55], v[112:115], v[104:107]
v_cvt_pk_bf16_f32 v51, v58, v59
v_permlane16_swap_b32 v48, v50
v_mfma_f32_16x16x32_bf16 v[108:111], a[52:55], a[128:131], v[108:111]
ds_read_b128 v[204:207], v232 offset:14336
v_permlane16_swap_b32 v49, v51
v_mfma_f32_16x16x32_bf16 v[76:79], a[144:147], a[128:131], v[76:79]
buffer_load_dwordx4 a[64:67], v208, s[36:39], s87 offen offset:2048 sc1
s_waitcnt lgkmcnt(4)
v_mfma_f32_16x16x32_bf16 v[80:83], a[144:147], a[112:115], v[80:83]
ds_read_b128 a[8:11], v225 offset:8192
v_mfma_f32_16x16x32_bf16 v[84:87], a[144:147], a[12:15], v[84:87]
ds_read_b128 a[232:235], v225 offset:10240
v_mfma_f32_16x16x32_bf16 v[88:91], a[144:147], a[152:155], v[88:91]
ds_read_b128 a[16:19], v225 offset:12288
v_mfma_f32_16x16x32_bf16 v[92:95], a[144:147], a[48:51], v[92:95]
ds_read_b128 v[184:187], v225 offset:14336
s_waitcnt vmcnt(52) lgkmcnt(4)
v_mfma_f32_16x16x32_bf16 v[64:67], v[168:171], v[160:163], v[64:67]
buffer_load_dwordx4 a[88:91], v208, s[36:39], s87 offen offset:3072 sc1
v_mfma_f32_16x16x32_bf16 v[96:99], a[188:191], v[160:163], v[96:99]
ds_read_b128 a[0:3], v232 offset:16384
v_mfma_f32_16x16x32_bf16 v[100:103], a[188:191], a[96:99], v[100:103]
ds_read_b128 a[80:83], v232 offset:18432
v_mfma_f32_16x16x32_bf16 v[68:71], v[168:171], a[96:99], v[68:71]
buffer_store_dwordx4 v[48:51], v233, s[36:39], s23 offen offset:256 sc1
v_mfma_f32_16x16x32_bf16 v[72:75], v[168:171], a[220:223], v[72:75]
ds_read_b128 a[116:119], v232 offset:20480
v_mfma_f32_16x16x32_bf16 v[104:107], a[188:191], a[220:223], v[104:107]
s_add_u32 s87, s85, 28672
v_mfma_f32_16x16x32_bf16 v[108:111], a[188:191], v[204:207], v[108:111]
buffer_load_dwordx4 a[124:127], v208, s[36:39], s87 offen offset:0 sc1
v_mfma_f32_16x16x32_bf16 v[76:79], v[168:171], v[204:207], v[76:79]
ds_read_b128 a[120:123], v232 offset:22528
s_waitcnt lgkmcnt(4)
v_mfma_f32_16x16x32_bf16 v[80:83], v[168:171], a[8:11], v[80:83]
ds_read_b128 a[228:231], v225 offset:16384
v_mfma_f32_16x16x32_bf16 v[84:87], v[168:171], a[232:235], v[84:87]
ds_read_b128 a[140:143], v225 offset:18432
ds_read_b128 a[184:187], v225 offset:20480
v_mfma_f32_16x16x32_bf16 v[88:91], v[168:171], a[16:19], v[88:91]
buffer_load_dwordx4 a[24:27], v208, s[36:39], s87 offen offset:1024 sc1
v_mfma_f32_16x16x32_bf16 v[92:95], v[168:171], v[184:187], v[92:95]
s_waitcnt vmcnt(54) lgkmcnt(3)
v_mfma_f32_16x16x32_bf16 v[64:67], a[196:199], a[0:3], v[64:67]
ds_read_b128 a[244:247], v225 offset:22528
v_mfma_f32_16x16x32_bf16 v[96:99], a[44:47], a[0:3], v[96:99]
ds_read_b128 v[112:115], v232 offset:24576
v_mfma_f32_16x16x32_bf16 v[100:103], a[44:47], a[80:83], v[100:103]
ds_read_b128 a[52:55], v232 offset:26624
v_mfma_f32_16x16x32_bf16 v[68:71], a[196:199], a[80:83], v[68:71]
v_mfma_f32_16x16x32_bf16 v[72:75], a[196:199], a[116:119], v[72:75]
ds_read_b128 a[128:131], v232 offset:28672
v_mfma_f32_16x16x32_bf16 v[104:107], a[44:47], a[116:119], v[104:107]
buffer_load_dwordx4 a[112:115], v208, s[36:39], s87 offen offset:2048 sc1
v_mfma_f32_16x16x32_bf16 v[108:111], a[44:47], a[120:123], v[108:111]
ds_read_b128 a[12:15], v232 offset:30720
v_mfma_f32_16x16x32_bf16 v[76:79], a[196:199], a[120:123], v[76:79]
s_waitcnt lgkmcnt(4)
v_mfma_f32_16x16x32_bf16 v[80:83], a[196:199], a[228:231], v[80:83]
ds_read_b128 a[152:155], v225 offset:24576
v_mfma_f32_16x16x32_bf16 v[84:87], a[196:199], a[140:143], v[84:87]
ds_read_b128 a[144:147], v225 offset:26624
v_mfma_f32_16x16x32_bf16 v[88:91], a[196:199], a[184:187], v[88:91]
buffer_load_dwordx4 a[48:51], v208, s[36:39], s87 offen offset:3072 sc1
v_mfma_f32_16x16x32_bf16 v[92:95], a[196:199], a[244:247], v[92:95]
ds_read_b128 v[160:163], v225 offset:28672
s_waitcnt vmcnt(54) lgkmcnt(3)
v_mfma_f32_16x16x32_bf16 v[64:67], a[60:63], v[112:115], v[64:67]
ds_read_b128 a[96:99], v225 offset:30720
v_mfma_f32_16x16x32_bf16 v[96:99], v[120:123], v[112:115], v[96:99]
ds_read_b128 a[220:223], v232 offset:32768
v_mfma_f32_16x16x32_bf16 v[100:103], v[120:123], a[52:55], v[100:103]
ds_read_b128 a[188:191], v232 offset:34816
v_mfma_f32_16x16x32_bf16 v[68:71], a[60:63], a[52:55], v[68:71]
s_add_u32 s87, s85, 32768
v_mfma_f32_16x16x32_bf16 v[72:75], a[60:63], a[128:131], v[72:75]
buffer_load_dwordx4 v[204:207], v208, s[36:39], s87 offen offset:0 sc1
v_mfma_f32_16x16x32_bf16 v[104:107], v[120:123], a[128:131], v[104:107]
ds_read_b128 a[8:11], v232 offset:36864
v_mfma_f32_16x16x32_bf16 v[108:111], v[120:123], a[12:15], v[108:111]
ds_read_b128 a[232:235], v232 offset:38912
v_mfma_f32_16x16x32_bf16 v[76:79], a[60:63], a[12:15], v[76:79]
s_waitcnt lgkmcnt(4)
v_mfma_f32_16x16x32_bf16 v[80:83], a[60:63], a[152:155], v[80:83]
ds_read_b128 a[16:19], v225 offset:32768
v_mfma_f32_16x16x32_bf16 v[84:87], a[60:63], a[144:147], v[84:87]
buffer_load_dwordx4 v[168:171], v208, s[36:39], s87 offen offset:1024 sc1
v_mfma_f32_16x16x32_bf16 v[88:91], a[60:63], v[160:163], v[88:91]
ds_read_b128 v[184:187], v225 offset:34816
ds_read_b128 a[0:3], v225 offset:36864
v_mfma_f32_16x16x32_bf16 v[92:95], a[60:63], a[96:99], v[92:95]
s_waitcnt vmcnt(54) lgkmcnt(3)
ds_read_b128 a[80:83], v225 offset:38912
v_mfma_f32_16x16x32_bf16 v[64:67], a[160:163], a[220:223], v[64:67]
s_lshl_b32 s75, s99, 6
s_add_u32 s75, s75, s77
v_mfma_f32_16x16x32_bf16 v[96:99], a[208:211], a[220:223], v[96:99]
ds_read_b128 a[116:119], v232 offset:40960
s_lshl_b32 s75, s75, 3
v_mfma_f32_16x16x32_bf16 v[100:103], a[208:211], a[188:191], v[100:103]
s_add_u32 s75, s75, 16941056
ds_read_b128 a[44:47], v232 offset:43008
v_mfma_f32_16x16x32_bf16 v[68:71], a[160:163], a[188:191], v[68:71]
buffer_load_dwordx4 a[120:123], v208, s[36:39], s87 offen offset:2048 sc1
v_mfma_f32_16x16x32_bf16 v[72:75], a[160:163], a[8:11], v[72:75]
ds_read_b128 a[228:231], v232 offset:45056
v_mfma_f32_16x16x32_bf16 v[104:107], a[208:211], a[8:11], v[104:107]
s_waitcnt vmcnt(7)
s_mov_b64 exec, 15
buffer_atomic_swap_x2 v[210:211], v231, s[36:39], s75 offen sc1
s_mov_b64 exec, -1
v_mfma_f32_16x16x32_bf16 v[108:111], a[208:211], a[232:235], v[108:111]
ds_read_b128 a[140:143], v232 offset:47104
v_mfma_f32_16x16x32_bf16 v[76:79], a[160:163], a[232:235], v[76:79]
s_waitcnt lgkmcnt(4)
v_mfma_f32_16x16x32_bf16 v[80:83], a[160:163], a[16:19], v[80:83]
buffer_load_dwordx4 a[184:187], v208, s[36:39], s87 offen offset:3072 sc1
v_mfma_f32_16x16x32_bf16 v[84:87], a[160:163], v[184:187], v[84:87]
ds_read_b128 a[196:199], v225 offset:40960
v_mfma_f32_16x16x32_bf16 v[88:91], a[160:163], a[0:3], v[88:91]
ds_read_b128 a[244:247], v225 offset:43008
v_mfma_f32_16x16x32_bf16 v[92:95], a[160:163], a[80:83], v[92:95]
ds_read_b128 v[112:115], v225 offset:45056
s_waitcnt lgkmcnt(3)
v_mfma_f32_16x16x32_bf16 v[64:67], a[240:243], a[116:119], v[64:67]
ds_read_b128 v[0:3], v225 offset:47104
v_mfma_f32_16x16x32_bf16 v[96:99], a[156:159], a[116:119], v[96:99]
ds_read_b128 v[4:7], v232 offset:49152
s_add_u32 s87, s85, 36864
v_mfma_f32_16x16x32_bf16 v[100:103], a[156:159], a[44:47], v[100:103]
buffer_load_dwordx4 a[52:55], v208, s[36:39], s87 offen offset:0 sc1
v_mfma_f32_16x16x32_bf16 v[68:71], a[240:243], a[44:47], v[68:71]
ds_read_b128 v[8:11], v232 offset:51200
v_mfma_f32_16x16x32_bf16 v[72:75], a[240:243], a[228:231], v[72:75]
ds_read_b128 v[12:15], v232 offset:53248
v_mfma_f32_16x16x32_bf16 v[104:107], a[156:159], a[228:231], v[104:107]
ds_read_b128 v[16:19], v232 offset:55296
v_mfma_f32_16x16x32_bf16 v[108:111], a[156:159], a[140:143], v[108:111]
buffer_load_dwordx4 a[128:131], v208, s[36:39], s87 offen offset:1024 sc1
v_mfma_f32_16x16x32_bf16 v[76:79], a[240:243], a[140:143], v[76:79]
s_waitcnt lgkmcnt(4)
v_mfma_f32_16x16x32_bf16 v[80:83], a[240:243], a[196:199], v[80:83]
ds_read_b128 v[20:23], v225 offset:49152
v_mfma_f32_16x16x32_bf16 v[84:87], a[240:243], a[244:247], v[84:87]
ds_read_b128 v[24:27], v225 offset:51200
v_mfma_f32_16x16x32_bf16 v[88:91], a[240:243], v[112:115], v[88:91]
ds_read_b128 v[28:31], v225 offset:53248
v_mfma_f32_16x16x32_bf16 v[92:95], a[240:243], v[0:3], v[92:95]
ds_read_b128 v[32:35], v225 offset:55296
s_waitcnt lgkmcnt(4)
v_mfma_f32_16x16x32_bf16 v[64:67], a[92:95], v[4:7], v[64:67]
ds_read_b128 v[36:39], v232 offset:57344
v_mfma_f32_16x16x32_bf16 v[96:99], a[68:71], v[4:7], v[96:99]
buffer_load_dwordx4 v[120:123], v208, s[36:39], s87 offen offset:2048 sc1
v_mfma_f32_16x16x32_bf16 v[100:103], a[68:71], v[8:11], v[100:103]
ds_read_b128 v[40:43], v232 offset:59392
v_mfma_f32_16x16x32_bf16 v[68:71], a[92:95], v[8:11], v[68:71]
v_mfma_f32_16x16x32_bf16 v[72:75], a[92:95], v[12:15], v[72:75]
ds_read_b128 v[44:47], v232 offset:61440
v_mfma_f32_16x16x32_bf16 v[104:107], a[68:71], v[12:15], v[104:107]
v_mfma_f32_16x16x32_bf16 v[108:111], a[68:71], v[16:19], v[108:111]
buffer_load_dwordx4 a[12:15], v208, s[36:39], s87 offen offset:3072 sc1
v_mfma_f32_16x16x32_bf16 v[76:79], a[92:95], v[16:19], v[76:79]
ds_read_b128 v[48:51], v232 offset:63488
s_waitcnt lgkmcnt(4)
v_mfma_f32_16x16x32_bf16 v[80:83], a[92:95], v[20:23], v[80:83]
ds_read_b128 v[52:55], v225 offset:57344
v_mfma_f32_16x16x32_bf16 v[84:87], a[92:95], v[24:27], v[84:87]
ds_read_b128 v[56:59], v225 offset:59392
v_mfma_f32_16x16x32_bf16 v[88:91], a[92:95], v[28:31], v[88:91]
ds_read_b128 v[60:63], v225 offset:61440
ds_read_b128 v[0:3], v225 offset:63488
v_mfma_f32_16x16x32_bf16 v[92:95], a[92:95], v[32:35], v[92:95]
s_waitcnt lgkmcnt(4)
s_add_u32 s87, s78, 0
v_mfma_f32_16x16x32_bf16 v[64:67], v[200:203], v[36:39], v[64:67]
buffer_load_dwordx4 a[152:155], v208, s[36:39], s87 offen offset:0 sc1
v_mfma_f32_16x16x32_bf16 v[96:99], a[164:167], v[36:39], v[96:99]
v_add_u32 v232, 0x10000, v232
v_add_u32 v225, 0x10000, v225
v_mfma_f32_16x16x32_bf16 v[100:103], a[164:167], v[40:43], v[100:103]
ds_read_b128 v[4:7], v232 offset:0
v_mfma_f32_16x16x32_bf16 v[68:71], v[200:203], v[40:43], v[68:71]
ds_read_b128 v[8:11], v232 offset:2048
v_mfma_f32_16x16x32_bf16 v[72:75], v[200:203], v[44:47], v[72:75]
ds_read_b128 v[12:15], v232 offset:4096
v_mfma_f32_16x16x32_bf16 v[104:107], a[164:167], v[44:47], v[104:107]
buffer_load_dwordx4 a[144:147], v208, s[36:39], s87 offen offset:1024 sc1
v_mfma_f32_16x16x32_bf16 v[108:111], a[164:167], v[48:51], v[108:111]
ds_read_b128 v[16:19], v232 offset:6144
v_mfma_f32_16x16x32_bf16 v[76:79], v[200:203], v[48:51], v[76:79]
s_waitcnt lgkmcnt(4)
v_mfma_f32_16x16x32_bf16 v[80:83], v[200:203], v[52:55], v[80:83]
ds_read_b128 v[20:23], v225 offset:0
v_mfma_f32_16x16x32_bf16 v[84:87], v[200:203], v[56:59], v[84:87]
ds_read_b128 v[24:27], v225 offset:2048
ds_read_b128 v[28:31], v225 offset:4096
v_mfma_f32_16x16x32_bf16 v[88:91], v[200:203], v[60:63], v[88:91]
buffer_load_dwordx4 v[160:163], v208, s[36:39], s87 offen offset:2048 sc1
v_mfma_f32_16x16x32_bf16 v[92:95], v[200:203], v[0:3], v[92:95]
s_waitcnt lgkmcnt(3)
ds_read_b128 v[32:35], v225 offset:6144
v_mfma_f32_16x16x32_bf16 v[64:67], v[172:175], v[4:7], v[64:67]
s_lshr_b32 s76, s82, 1
s_lshl_b32 s76, s76, 2
v_mfma_f32_16x16x32_bf16 v[96:99], a[28:31], v[4:7], v[96:99]
ds_read_b128 v[36:39], v232 offset:8192
s_add_u32 s76, s76, s64
s_lshl_b32 s76, s76, 7
v_mfma_f32_16x16x32_bf16 v[100:103], a[28:31], v[8:11], v[100:103]
ds_read_b128 v[40:43], v232 offset:10240
s_add_u32 s76, s76, 16941056
v_mfma_f32_16x16x32_bf16 v[68:71], v[172:175], v[8:11], v[68:71]
s_add_u32 s76, s76, s14
v_lshlrev_b32 v230, 3, v209
v_mfma_f32_16x16x32_bf16 v[72:75], v[172:175], v[12:15], v[72:75]
buffer_load_dwordx4 a[60:63], v208, s[36:39], s87 offen offset:3072 sc1
v_mfma_f32_16x16x32_bf16 v[104:107], a[28:31], v[12:15], v[104:107]
ds_read_b128 v[44:47], v232 offset:12288
v_mfma_f32_16x16x32_bf16 v[108:111], a[28:31], v[16:19], v[108:111]
ds_read_b128 v[48:51], v232 offset:14336
v_mov_b64 v[216:217], 0
v_mfma_f32_16x16x32_bf16 v[76:79], v[172:175], v[16:19], v[76:79]
s_mov_b64 exec, 65535
buffer_atomic_add_x2 v[216:217], v230, s[36:39], s76 offen sc0 sc1
s_mov_b64 exec, -1
s_waitcnt lgkmcnt(4)
v_mfma_f32_16x16x32_bf16 v[80:83], v[172:175], v[20:23], v[80:83]
ds_read_b128 v[52:55], v225 offset:8192
v_mfma_f32_16x16x32_bf16 v[84:87], v[172:175], v[24:27], v[84:87]
ds_read_b128 v[56:59], v225 offset:10240
s_add_u32 s87, s78, 4096
v_mfma_f32_16x16x32_bf16 v[88:91], v[172:175], v[28:31], v[88:91]
buffer_load_dwordx4 a[96:99], v208, s[36:39], s87 offen offset:0 sc1
v_mfma_f32_16x16x32_bf16 v[92:95], v[172:175], v[32:35], v[92:95]
ds_read_b128 v[60:63], v225 offset:12288
s_waitcnt lgkmcnt(3)
v_mfma_f32_16x16x32_bf16 v[64:67], a[204:207], v[36:39], v[64:67]
ds_read_b128 v[0:3], v225 offset:14336
v_mfma_f32_16x16x32_bf16 v[96:99], a[216:219], v[36:39], v[96:99]
ds_read_b128 v[4:7], v232 offset:16384
v_mfma_f32_16x16x32_bf16 v[100:103], a[216:219], v[40:43], v[100:103]
ds_read_b128 v[8:11], v232 offset:18432
v_mfma_f32_16x16x32_bf16 v[68:71], a[204:207], v[40:43], v[68:71]
buffer_load_dwordx4 a[220:223], v208, s[36:39], s87 offen offset:1024 sc1
v_mfma_f32_16x16x32_bf16 v[72:75], a[204:207], v[44:47], v[72:75]
ds_read_b128 v[12:15], v232 offset:20480
v_mfma_f32_16x16x32_bf16 v[104:107], a[216:219], v[44:47], v[104:107]
v_mfma_f32_16x16x32_bf16 v[108:111], a[216:219], v[48:51], v[108:111]
ds_read_b128 v[16:19], v232 offset:22528
v_mfma_f32_16x16x32_bf16 v[76:79], a[204:207], v[48:51], v[76:79]
s_waitcnt lgkmcnt(4)
v_mfma_f32_16x16x32_bf16 v[80:83], a[204:207], v[52:55], v[80:83]
ds_read_b128 v[20:23], v225 offset:16384
v_mfma_f32_16x16x32_bf16 v[84:87], a[204:207], v[56:59], v[84:87]
buffer_load_dwordx4 a[188:191], v208, s[36:39], s87 offen offset:2048 sc1
v_mfma_f32_16x16x32_bf16 v[88:91], a[204:207], v[60:63], v[88:91]
ds_read_b128 v[24:27], v225 offset:18432
v_mfma_f32_16x16x32_bf16 v[92:95], a[204:207], v[0:3], v[92:95]
ds_read_b128 v[28:31], v225 offset:20480
s_waitcnt lgkmcnt(3)
v_mfma_f32_16x16x32_bf16 v[64:67], v[196:199], v[4:7], v[64:67]
ds_read_b128 v[32:35], v225 offset:22528
v_mfma_f32_16x16x32_bf16 v[96:99], a[212:215], v[4:7], v[96:99]
ds_read_b128 v[36:39], v232 offset:24576
v_mfma_f32_16x16x32_bf16 v[100:103], a[212:215], v[8:11], v[100:103]
buffer_load_dwordx4 a[8:11], v208, s[36:39], s87 offen offset:3072 sc1
v_mfma_f32_16x16x32_bf16 v[68:71], v[196:199], v[8:11], v[68:71]
ds_read_b128 v[40:43], v232 offset:26624
v_mfma_f32_16x16x32_bf16 v[72:75], v[196:199], v[12:15], v[72:75]
ds_read_b128 v[44:47], v232 offset:28672
v_mfma_f32_16x16x32_bf16 v[104:107], a[212:215], v[12:15], v[104:107]
v_mfma_f32_16x16x32_bf16 v[108:111], a[212:215], v[16:19], v[108:111]
ds_read_b128 v[48:51], v232 offset:30720
v_mfma_f32_16x16x32_bf16 v[76:79], v[196:199], v[16:19], v[76:79]
s_waitcnt lgkmcnt(4)
s_add_u32 s87, s78, 8192
v_mfma_f32_16x16x32_bf16 v[80:83], v[196:199], v[20:23], v[80:83]
buffer_load_dwordx4 a[208:211], v208, s[36:39], s87 offen offset:0 sc1
v_mfma_f32_16x16x32_bf16 v[84:87], v[196:199], v[24:27], v[84:87]
ds_read_b128 v[52:55], v225 offset:24576
v_mfma_f32_16x16x32_bf16 v[88:91], v[196:199], v[28:31], v[88:91]
ds_read_b128 v[56:59], v225 offset:26624
v_mfma_f32_16x16x32_bf16 v[92:95], v[196:199], v[32:35], v[92:95]
ds_read_b128 v[60:63], v225 offset:28672
s_waitcnt lgkmcnt(3)
v_mfma_f32_16x16x32_bf16 v[64:67], a[224:227], v[36:39], v[64:67]
ds_read_b128 v[0:3], v225 offset:30720
v_mfma_f32_16x16x32_bf16 v[96:99], a[108:111], v[36:39], v[96:99]
buffer_load_dwordx4 a[232:235], v208, s[36:39], s87 offen offset:1024 sc1
v_mfma_f32_16x16x32_bf16 v[100:103], a[108:111], v[40:43], v[100:103]
v_mfma_f32_16x16x32_bf16 v[68:71], a[224:227], v[40:43], v[68:71]
v_mfma_f32_16x16x32_bf16 v[72:75], a[224:227], v[44:47], v[72:75]
v_mfma_f32_16x16x32_bf16 v[104:107], a[108:111], v[44:47], v[104:107]
v_mfma_f32_16x16x32_bf16 v[108:111], a[108:111], v[48:51], v[108:111]
buffer_load_dwordx4 a[16:19], v208, s[36:39], s87 offen offset:2048 sc1
v_mfma_f32_16x16x32_bf16 v[76:79], a[224:227], v[48:51], v[76:79]
s_waitcnt lgkmcnt(0)
v_mfma_f32_16x16x32_bf16 v[80:83], a[224:227], v[52:55], v[80:83]
v_mfma_f32_16x16x32_bf16 v[84:87], a[224:227], v[56:59], v[84:87]
v_mfma_f32_16x16x32_bf16 v[88:91], a[224:227], v[60:63], v[88:91]
v_mfma_f32_16x16x32_bf16 v[92:95], a[224:227], v[0:3], v[92:95]
v_mfma_f32_16x16x32_bf16 v[64:67], a[32:35], v[128:131], v[64:67]
buffer_load_dwordx4 v[184:187], v208, s[36:39], s87 offen offset:3072 sc1
v_mfma_f32_16x16x32_bf16 v[96:99], a[236:239], v[128:131], v[96:99]
v_mfma_f32_16x16x32_bf16 v[100:103], a[236:239], v[132:135], v[100:103]
v_mfma_f32_16x16x32_bf16 v[68:71], a[32:35], v[132:135], v[68:71]
v_mfma_f32_16x16x32_bf16 v[72:75], a[32:35], v[136:139], v[72:75]
v_mfma_f32_16x16x32_bf16 v[104:107], a[236:239], v[136:139], v[104:107]
s_add_u32 s87, s78, 12288
v_mfma_f32_16x16x32_bf16 v[108:111], a[236:239], v[140:143], v[108:111]
buffer_load_dwordx4 a[0:3], v208, s[36:39], s87 offen offset:0 sc1
v_mfma_f32_16x16x32_bf16 v[76:79], a[32:35], v[140:143], v[76:79]
v_mfma_f32_16x16x32_bf16 v[80:83], a[32:35], v[144:147], v[80:83]
v_mfma_f32_16x16x32_bf16 v[84:87], a[32:35], v[148:151], v[84:87]
v_mfma_f32_16x16x32_bf16 v[88:91], a[32:35], v[152:155], v[88:91]
v_mfma_f32_16x16x32_bf16 v[92:95], a[32:35], v[156:159], v[92:95]
buffer_load_dwordx4 a[160:163], v208, s[36:39], s87 offen offset:1024 sc1
s_waitcnt vmcnt(10)
v_cmp_ne_u64 vcc, s[80:81], v[216:217]
s_nop 0
s_and_b64 vcc, vcc, 65535
s_cbranch_scc0 .Lk_p3a_ok
s_mov_b32 s89, 0
.Lk_p3a:
v_mov_b64 v[216:217], 0
s_mov_b64 exec, 65535
buffer_atomic_add_x2 v[216:217], v230, s[36:39], s76 offen sc0 sc1
s_mov_b64 exec, -1
s_waitcnt vmcnt(0)
v_cmp_ne_u64 vcc, s[80:81], v[216:217]
s_nop 0
s_and_b64 vcc, vcc, 65535
s_cbranch_scc0 .Lk_p3a_ok
s_add_u32 s89, s89, 1
s_cmp_lt_u32 s89, 0x20000
s_cbranch_scc1 .Lk_p3a
.Lk_p3a_ok:
s_mul_i32 s96, s82, 221184
s_add_u32 s96, s96, 35471360
s_lshl_b32 s75, s64, 15
s_add_u32 s94, s96, s75
s_add_u32 s88, s94, 0
buffer_load_dwordx4 a[80:83], v218, s[36:39], s88 offen sc1
buffer_load_dwordx4 a[116:119], v219, s[36:39], s88 offen sc1
buffer_load_dwordx4 a[44:47], v220, s[36:39], s88 offen sc1
buffer_load_dwordx4 a[228:231], v221, s[36:39], s88 offen sc1
s_add_u32 s88, s94, 4096
buffer_load_dwordx4 a[156:159], v218, s[36:39], s88 offen sc1
buffer_load_dwordx4 a[140:143], v219, s[36:39], s88 offen sc1
buffer_load_dwordx4 a[196:199], v220, s[36:39], s88 offen sc1
buffer_load_dwordx4 a[244:247], v221, s[36:39], s88 offen sc1
v_permlane32_swap_b32 v64, v68
v_permlane32_swap_b32 v65, v69
v_permlane32_swap_b32 v66, v70
v_permlane32_swap_b32 v67, v71
v_max_f32 v64, 0, v64
v_max_f32 v65, 0, v65
v_max_f32 v66, 0, v66
v_max_f32 v67, 0, v67
v_pk_mul_f32 v[68:69], v[68:69], v[176:177] op_sel_hi:[1,0]
v_pk_mul_f32 v[70:71], v[70:71], v[176:177] op_sel_hi:[1,0]
v_pk_mul_f32 v[64:65], v[64:65], v[68:69]
v_pk_mul_f32 v[66:67], v[66:67], v[70:71]
v_cvt_pk_bf16_f32 v64, v64, v65
v_cvt_pk_bf16_f32 v65, v66, v67
v_permlane32_swap_b32 v72, v76
v_permlane32_swap_b32 v73, v77
v_permlane32_swap_b32 v74, v78
v_permlane32_swap_b32 v75, v79
v_max_f32 v72, 0, v72
v_max_f32 v73, 0, v73
v_max_f32 v74, 0, v74
v_max_f32 v75, 0, v75
v_pk_mul_f32 v[76:77], v[76:77], v[176:177] op_sel:[0,1] op_sel_hi:[1,1]
v_pk_mul_f32 v[78:79], v[78:79], v[176:177] op_sel:[0,1] op_sel_hi:[1,1]
v_pk_mul_f32 v[72:73], v[72:73], v[76:77]
v_pk_mul_f32 v[74:75], v[74:75], v[78:79]
v_cvt_pk_bf16_f32 v66, v72, v73
v_cvt_pk_bf16_f32 v67, v74, v75
s_nop 0
v_permlane16_swap_b32 v64, v66
v_permlane16_swap_b32 v65, v67
buffer_store_dwordx4 v[64:67], v233, s[36:39], s24 offen sc1
v_permlane32_swap_b32 v80, v84
v_permlane32_swap_b32 v81, v85
v_permlane32_swap_b32 v82, v86
v_permlane32_swap_b32 v83, v87
v_max_f32 v80, 0, v80
v_max_f32 v81, 0, v81
v_max_f32 v82, 0, v82
v_max_f32 v83, 0, v83
v_pk_mul_f32 v[84:85], v[84:85], v[178:179] op_sel_hi:[1,0]
v_pk_mul_f32 v[86:87], v[86:87], v[178:179] op_sel_hi:[1,0]
v_pk_mul_f32 v[80:81], v[80:81], v[84:85]
v_pk_mul_f32 v[82:83], v[82:83], v[86:87]
v_cvt_pk_bf16_f32 v80, v80, v81
v_cvt_pk_bf16_f32 v81, v82, v83
v_permlane32_swap_b32 v88, v92
v_permlane32_swap_b32 v89, v93
v_permlane32_swap_b32 v90, v94
v_permlane32_swap_b32 v91, v95
v_max_f32 v88, 0, v88
v_max_f32 v89, 0, v89
v_max_f32 v90, 0, v90
v_max_f32 v91, 0, v91
v_pk_mul_f32 v[92:93], v[92:93], v[178:179] op_sel:[0,1] op_sel_hi:[1,1]
v_pk_mul_f32 v[94:95], v[94:95], v[178:179] op_sel:[0,1] op_sel_hi:[1,1]
v_pk_mul_f32 v[88:89], v[88:89], v[92:93]
v_pk_mul_f32 v[90:91], v[90:91], v[94:95]
v_cvt_pk_bf16_f32 v82, v88, v89
v_cvt_pk_bf16_f32 v83, v90, v91
s_nop 0
v_permlane16_swap_b32 v80, v82
v_permlane16_swap_b32 v81, v83
buffer_store_dwordx4 v[80:83], v233, s[36:39], s25 offen sc1
v_permlane32_swap_b32 v96, v100
v_permlane32_swap_b32 v97, v101
v_permlane32_swap_b32 v98, v102
v_permlane32_swap_b32 v99, v103
v_max_f32 v96, 0, v96
v_max_f32 v97, 0, v97
v_max_f32 v98, 0, v98
v_max_f32 v99, 0, v99
v_pk_mul_f32 v[100:101], v[100:101], v[176:177] op_sel_hi:[1,0]
v_pk_mul_f32 v[102:103], v[102:103], v[176:177] op_sel_hi:[1,0]
v_pk_mul_f32 v[96:97], v[96:97], v[100:101]
v_pk_mul_f32 v[98:99], v[98:99], v[102:103]
v_cvt_pk_bf16_f32 v96, v96, v97
v_cvt_pk_bf16_f32 v97, v98, v99
v_permlane32_swap_b32 v104, v108
v_permlane32_swap_b32 v105, v109
v_permlane32_swap_b32 v106, v110
v_permlane32_swap_b32 v107, v111
v_max_f32 v104, 0, v104
v_max_f32 v105, 0, v105
v_max_f32 v106, 0, v106
v_max_f32 v107, 0, v107
v_pk_mul_f32 v[108:109], v[108:109], v[176:177] op_sel:[0,1] op_sel_hi:[1,1]
v_pk_mul_f32 v[110:111], v[110:111], v[176:177] op_sel:[0,1] op_sel_hi:[1,1]
v_pk_mul_f32 v[104:105], v[104:105], v[108:109]
v_pk_mul_f32 v[106:107], v[106:107], v[110:111]
v_cvt_pk_bf16_f32 v98, v104, v105
v_cvt_pk_bf16_f32 v99, v106, v107
s_nop 0
v_permlane16_swap_b32 v96, v98
v_permlane16_swap_b32 v97, v99
buffer_store_dwordx4 v[96:99], v233, s[36:39], s26 offen sc1
s_add_u32 s88, s94, 8192
buffer_load_dwordx4 v[112:115], v218, s[36:39], s88 offen sc1
buffer_load_dwordx4 a[240:243], v219, s[36:39], s88 offen sc1
buffer_load_dwordx4 a[68:71], v220, s[36:39], s88 offen sc1
buffer_load_dwordx4 a[92:95], v221, s[36:39], s88 offen sc1
s_add_u32 s88, s94, 12288
buffer_load_dwordx4 a[164:167], v218, s[36:39], s88 offen sc1
s_waitcnt vmcnt(15)
v_mfma_f32_16x16x32_bf16 v[0:3], a[84:87], a[80:83], 0
v_mfma_f32_16x16x32_bf16 v[4:7], a[176:179], a[80:83], 0
v_mfma_f32_16x16x32_bf16 v[8:11], v[164:167], a[80:83], 0
v_mfma_f32_16x16x32_bf16 v[12:15], a[148:151], a[80:83], 0
buffer_load_dwordx4 v[200:203], v219, s[36:39], s88 offen sc1
v_mfma_f32_16x16x32_bf16 v[16:19], a[168:171], a[80:83], 0
s_waitcnt vmcnt(15)
v_mfma_f32_16x16x32_bf16 v[36:39], a[168:171], a[116:119], 0
v_mfma_f32_16x16x32_bf16 v[32:35], a[148:151], a[116:119], 0
v_mfma_f32_16x16x32_bf16 v[28:31], v[164:167], a[116:119], 0
buffer_load_dwordx4 a[28:31], v220, s[36:39], s88 offen sc1
v_mfma_f32_16x16x32_bf16 v[24:27], a[176:179], a[116:119], 0
v_mfma_f32_16x16x32_bf16 v[20:23], a[84:87], a[116:119], 0
s_waitcnt vmcnt(15)
v_mfma_f32_16x16x32_bf16 v[40:43], a[84:87], a[44:47], 0
v_mfma_f32_16x16x32_bf16 v[44:47], a[176:179], a[44:47], 0
buffer_load_dwordx4 v[172:175], v221, s[36:39], s88 offen sc1
v_mfma_f32_16x16x32_bf16 v[48:51], v[164:167], a[44:47], 0
v_mfma_f32_16x16x32_bf16 v[52:55], a[148:151], a[44:47], 0
v_mfma_f32_16x16x32_bf16 v[56:59], a[168:171], a[44:47], 0
s_waitcnt vmcnt(15)
s_add_u32 s88, s94, 16384
v_mfma_f32_16x16x32_bf16 v[76:79], a[168:171], a[228:231], 0
buffer_load_dwordx4 a[216:219], v218, s[36:39], s88 offen sc1
v_mfma_f32_16x16x32_bf16 v[72:75], a[148:151], a[228:231], 0
v_mfma_f32_16x16x32_bf16 v[68:71], v[164:167], a[228:231], 0
v_mfma_f32_16x16x32_bf16 v[64:67], a[176:179], a[228:231], 0
buffer_load_dwordx4 a[204:207], v219, s[36:39], s88 offen sc1
v_mfma_f32_16x16x32_bf16 v[60:63], a[84:87], a[228:231], 0
s_waitcnt vmcnt(16)
s_lshl_b32 s75, s99, 6
v_mfma_f32_16x16x32_bf16 v[0:3], a[104:107], a[156:159], v[0:3]
s_add_u32 s75, s75, s77
s_lshl_b32 s75, s75, 3
v_mfma_f32_16x16x32_bf16 v[4:7], a[40:43], a[156:159], v[4:7]
s_add_u32 s75, s75, 16973824
v_mfma_f32_16x16x32_bf16 v[8:11], a[192:195], a[156:159], v[8:11]
s_waitcnt vmcnt(10)
s_mov_b64 exec, 15
buffer_atomic_swap_x2 v[210:211], v231, s[36:39], s75 offen sc1
s_mov_b64 exec, -1
v_mfma_f32_16x16x32_bf16 v[12:15], a[100:103], a[156:159], v[12:15]
buffer_load_dwordx4 a[212:215], v220, s[36:39], s88 offen sc1
v_mfma_f32_16x16x32_bf16 v[16:19], a[200:203], a[156:159], v[16:19]
s_lshl_b32 s76, s64, 4
v_add_u32 v230, s76, v209
v_min_u32 v230, 63, v230
v_mfma_f32_16x16x32_bf16 v[36:39], a[200:203], a[140:143], v[36:39]
v_lshlrev_b32 v230, 3, v230
s_lshr_b32 s76, s82, 1
s_lshl_b32 s76, s76, 9
v_mfma_f32_16x16x32_bf16 v[32:35], a[100:103], a[140:143], v[32:35]
s_add_u32 s76, s76, 16973824
s_add_u32 s76, s76, s14
s_lshl_b32 s74, s74, 12
v_mfma_f32_16x16x32_bf16 v[28:31], a[192:195], a[140:143], v[28:31]
buffer_load_dwordx4 v[196:199], v221, s[36:39], s88 offen sc1
v_mfma_f32_16x16x32_bf16 v[24:27], a[40:43], a[140:143], v[24:27]
s_add_u32 s96, s96, s74
v_mfma_f32_16x16x32_bf16 v[20:23], a[104:107], a[140:143], v[20:23]
s_cmp_ge_u32 s64, 2
s_cselect_b32 s49, 47923200, s96
v_mfma_f32_16x16x32_bf16 v[40:43], a[104:107], a[196:199], v[40:43]
s_add_u32 s88, s94, 20480
v_mfma_f32_16x16x32_bf16 v[44:47], a[40:43], a[196:199], v[44:47]
buffer_load_dwordx4 a[108:111], v218, s[36:39], s88 offen sc1
v_mfma_f32_16x16x32_bf16 v[48:51], a[192:195], a[196:199], v[48:51]
v_mfma_f32_16x16x32_bf16 v[52:55], a[100:103], a[196:199], v[52:55]
v_mfma_f32_16x16x32_bf16 v[56:59], a[200:203], a[196:199], v[56:59]
v_mfma_f32_16x16x32_bf16 v[76:79], a[200:203], a[244:247], v[76:79]
buffer_load_dwordx4 a[224:227], v219, s[36:39], s88 offen sc1
v_mfma_f32_16x16x32_bf16 v[72:75], a[100:103], a[244:247], v[72:75]
v_mfma_f32_16x16x32_bf16 v[68:71], a[192:195], a[244:247], v[68:71]
v_mfma_f32_16x16x32_bf16 v[64:67], a[40:43], a[244:247], v[64:67]
buffer_load_dwordx4 a[236:239], v220, s[36:39], s88 offen sc1
v_mfma_f32_16x16x32_bf16 v[60:63], a[104:107], a[244:247], v[60:63]
s_waitcnt vmcnt(15)
v_mfma_f32_16x16x32_bf16 v[0:3], a[248:251], v[112:115], v[0:3]
v_mfma_f32_16x16x32_bf16 v[4:7], v[116:119], v[112:115], v[4:7]
v_mfma_f32_16x16x32_bf16 v[8:11], a[172:175], v[112:115], v[8:11]
v_mfma_f32_16x16x32_bf16 v[12:15], a[56:59], v[112:115], v[12:15]
buffer_load_dwordx4 a[32:35], v221, s[36:39], s88 offen sc1
v_mfma_f32_16x16x32_bf16 v[16:19], a[20:23], v[112:115], v[16:19]
s_waitcnt vmcnt(15)
v_mfma_f32_16x16x32_bf16 v[36:39], a[20:23], a[240:243], v[36:39]
v_mfma_f32_16x16x32_bf16 v[32:35], a[56:59], a[240:243], v[32:35]
s_add_u32 s88, s94, 24576
v_mfma_f32_16x16x32_bf16 v[28:31], a[172:175], a[240:243], v[28:31]
buffer_load_dwordx4 v[148:151], v218, s[36:39], s88 offen sc1
v_mfma_f32_16x16x32_bf16 v[24:27], v[116:119], a[240:243], v[24:27]
v_mfma_f32_16x16x32_bf16 v[20:23], a[248:251], a[240:243], v[20:23]
s_waitcnt vmcnt(15)
v_mfma_f32_16x16x32_bf16 v[40:43], a[248:251], a[68:71], v[40:43]
v_mfma_f32_16x16x32_bf16 v[44:47], v[116:119], a[68:71], v[44:47]
buffer_load_dwordx4 v[152:155], v219, s[36:39], s88 offen sc1
v_mfma_f32_16x16x32_bf16 v[48:51], a[172:175], a[68:71], v[48:51]
v_mfma_f32_16x16x32_bf16 v[52:55], a[56:59], a[68:71], v[52:55]
v_mfma_f32_16x16x32_bf16 v[56:59], a[20:23], a[68:71], v[56:59]
s_waitcnt vmcnt(15)
v_mfma_f32_16x16x32_bf16 v[76:79], a[20:23], a[92:95], v[76:79]
buffer_load_dwordx4 v[156:159], v220, s[36:39], s88 offen sc1
v_mfma_f32_16x16x32_bf16 v[72:75], a[56:59], a[92:95], v[72:75]
v_mfma_f32_16x16x32_bf16 v[68:71], a[172:175], a[92:95], v[68:71]
v_mfma_f32_16x16x32_bf16 v[64:67], v[116:119], a[92:95], v[64:67]
buffer_load_dwordx4 v[84:87], v221, s[36:39], s88 offen sc1
v_mfma_f32_16x16x32_bf16 v[60:63], a[248:251], a[92:95], v[60:63]
s_waitcnt vmcnt(16)
v_mfma_f32_16x16x32_bf16 v[0:3], a[36:39], a[164:167], v[0:3]
v_mfma_f32_16x16x32_bf16 v[4:7], a[180:183], a[164:167], v[4:7]
v_mfma_f32_16x16x32_bf16 v[8:11], a[72:75], a[164:167], v[8:11]
s_add_u32 s88, s94, 28672
v_mfma_f32_16x16x32_bf16 v[12:15], v[192:195], a[164:167], v[12:15]
buffer_load_dwordx4 v[88:91], v218, s[36:39], s88 offen sc1
v_mfma_f32_16x16x32_bf16 v[16:19], v[188:191], a[164:167], v[16:19]
s_waitcnt vmcnt(16)
v_mfma_f32_16x16x32_bf16 v[36:39], v[188:191], v[200:203], v[36:39]
v_mfma_f32_16x16x32_bf16 v[32:35], v[192:195], v[200:203], v[32:35]
v_mfma_f32_16x16x32_bf16 v[28:31], a[72:75], v[200:203], v[28:31]
buffer_load_dwordx4 v[92:95], v219, s[36:39], s88 offen sc1
v_mfma_f32_16x16x32_bf16 v[24:27], a[180:183], v[200:203], v[24:27]
v_mfma_f32_16x16x32_bf16 v[20:23], a[36:39], v[200:203], v[20:23]
s_waitcnt vmcnt(16)
v_mfma_f32_16x16x32_bf16 v[40:43], a[36:39], a[28:31], v[40:43]
v_mfma_f32_16x16x32_bf16 v[44:47], a[180:183], a[28:31], v[44:47]
buffer_load_dwordx4 v[100:103], v220, s[36:39], s88 offen sc1
v_mfma_f32_16x16x32_bf16 v[48:51], a[72:75], a[28:31], v[48:51]
v_mfma_f32_16x16x32_bf16 v[52:55], v[192:195], a[28:31], v[52:55]
v_mfma_f32_16x16x32_bf16 v[56:59], v[188:191], a[28:31], v[56:59]
s_waitcnt vmcnt(16)
v_mfma_f32_16x16x32_bf16 v[76:79], v[188:191], v[172:175], v[76:79]
buffer_load_dwordx4 v[104:107], v221, s[36:39], s88 offen sc1
v_mfma_f32_16x16x32_bf16 v[72:75], v[192:195], v[172:175], v[72:75]
v_mfma_f32_16x16x32_bf16 v[68:71], a[72:75], v[172:175], v[68:71]
buffer_load_dwordx4 v[108:111], v208, s[36:39], s87 offen offset:2048 sc1
v_mfma_f32_16x16x32_bf16 v[64:67], a[180:183], v[172:175], v[64:67]
v_mov_b64 v[216:217], 0
v_mfma_f32_16x16x32_bf16 v[60:63], a[36:39], v[172:175], v[60:63]
s_mov_b64 exec, 1048575
buffer_atomic_add_x2 v[216:217], v230, s[36:39], s76 offen sc0 sc1
s_mov_b64 exec, -1
s_barrier
s_waitcnt vmcnt(18)
v_mfma_f32_16x16x32_bf16 v[0:3], a[4:7], a[216:219], v[0:3]
ds_write_b128 v228, v[180:183] offset:23552
ds_read_b128 v[128:131], v226 offset:640
v_mfma_f32_16x16x32_bf16 v[4:7], a[252:255], a[216:219], v[4:7]
ds_read_b128 v[132:135], v226 offset:656
ds_read_b128 v[136:139], v226 offset:768
ds_read_b128 v[140:143], v226 offset:784
v_mfma_f32_16x16x32_bf16 v[8:11], a[132:135], a[216:219], v[8:11]
ds_read_b128 v[144:147], v227 offset:896
s_mov_b64 s[60:61], s[30:31]
s_and_b32 s61, s61, 0xffff
v_mfma_f32_16x16x32_bf16 v[12:15], v[124:127], a[216:219], v[12:15]
buffer_load_dwordx4 a[80:83], v208, s[36:39], s87 offen offset:3072 sc1
v_mfma_f32_16x16x32_bf16 v[16:19], a[136:139], a[216:219], v[16:19]
s_mov_b32 s62, 2621440
s_mov_b32 s63, 0x20000
s_waitcnt vmcnt(18)
v_mfma_f32_16x16x32_bf16 v[36:39], a[136:139], a[204:207], v[36:39]
v_mfma_f32_16x16x32_bf16 v[32:35], v[124:127], a[204:207], v[32:35]
s_add_u32 s87, s78, 16384
v_mfma_f32_16x16x32_bf16 v[28:31], a[132:135], a[204:207], v[28:31]
buffer_load_dwordx4 a[116:119], v208, s[36:39], s87 offen offset:0 sc1
v_mfma_f32_16x16x32_bf16 v[24:27], a[252:255], a[204:207], v[24:27]
v_mfma_f32_16x16x32_bf16 v[20:23], a[4:7], a[204:207], v[20:23]
s_waitcnt vmcnt(17)
v_mfma_f32_16x16x32_bf16 v[40:43], a[4:7], a[212:215], v[40:43]
v_mfma_f32_16x16x32_bf16 v[44:47], a[252:255], a[212:215], v[44:47]
buffer_load_dwordx4 a[44:47], v208, s[36:39], s87 offen offset:1024 sc1
v_mfma_f32_16x16x32_bf16 v[48:51], a[132:135], a[212:215], v[48:51]
v_mfma_f32_16x16x32_bf16 v[52:55], v[124:127], a[212:215], v[52:55]
v_mfma_f32_16x16x32_bf16 v[56:59], a[136:139], a[212:215], v[56:59]
s_waitcnt vmcnt(17)
v_mfma_f32_16x16x32_bf16 v[76:79], a[136:139], v[196:199], v[76:79]
buffer_load_dwordx4 a[168:171], v208, s[36:39], s87 offen offset:2048 sc1
v_mfma_f32_16x16x32_bf16 v[72:75], v[124:127], v[196:199], v[72:75]
v_mfma_f32_16x16x32_bf16 v[68:71], a[132:135], v[196:199], v[68:71]
v_mfma_f32_16x16x32_bf16 v[64:67], a[252:255], v[196:199], v[64:67]
buffer_load_dwordx4 a[148:151], v208, s[36:39], s87 offen offset:3072 sc1
v_mfma_f32_16x16x32_bf16 v[60:63], a[4:7], v[196:199], v[60:63]
s_waitcnt vmcnt(18)
v_mfma_f32_16x16x32_bf16 v[0:3], a[76:79], a[108:111], v[0:3]
v_mfma_f32_16x16x32_bf16 v[4:7], a[64:67], a[108:111], v[4:7]
v_mfma_f32_16x16x32_bf16 v[8:11], a[88:91], a[108:111], v[8:11]
s_add_u32 s87, s78, 20480
v_mfma_f32_16x16x32_bf16 v[12:15], a[124:127], a[108:111], v[12:15]
buffer_load_dwordx4 v[164:167], v208, s[36:39], s87 offen offset:0 sc1
v_mfma_f32_16x16x32_bf16 v[16:19], a[24:27], a[108:111], v[16:19]
s_waitcnt vmcnt(18)
v_mfma_f32_16x16x32_bf16 v[36:39], a[24:27], a[224:227], v[36:39]
v_mfma_f32_16x16x32_bf16 v[32:35], a[124:127], a[224:227], v[32:35]
v_mfma_f32_16x16x32_bf16 v[28:31], a[88:91], a[224:227], v[28:31]
buffer_load_dwordx4 a[176:179], v208, s[36:39], s87 offen offset:1024 sc1
v_mfma_f32_16x16x32_bf16 v[24:27], a[64:67], a[224:227], v[24:27]
v_mfma_f32_16x16x32_bf16 v[20:23], a[76:79], a[224:227], v[20:23]
s_waitcnt vmcnt(18)
v_mfma_f32_16x16x32_bf16 v[40:43], a[76:79], a[236:239], v[40:43]
v_mfma_f32_16x16x32_bf16 v[44:47], a[64:67], a[236:239], v[44:47]
buffer_load_dwordx4 a[228:231], v208, s[36:39], s87 offen offset:2048 sc1
v_mfma_f32_16x16x32_bf16 v[48:51], a[88:91], a[236:239], v[48:51]
v_mfma_f32_16x16x32_bf16 v[52:55], a[124:127], a[236:239], v[52:55]
v_mfma_f32_16x16x32_bf16 v[56:59], a[24:27], a[236:239], v[56:59]
s_waitcnt vmcnt(18)
v_mfma_f32_16x16x32_bf16 v[76:79], a[24:27], a[32:35], v[76:79]
buffer_load_dwordx4 a[84:87], v208, s[36:39], s87 offen offset:3072 sc1
v_mfma_f32_16x16x32_bf16 v[72:75], a[124:127], a[32:35], v[72:75]
v_mfma_f32_16x16x32_bf16 v[68:71], a[88:91], a[32:35], v[68:71]
v_mfma_f32_16x16x32_bf16 v[64:67], a[64:67], a[32:35], v[64:67]
v_mfma_f32_16x16x32_bf16 v[60:63], a[76:79], a[32:35], v[60:63]
s_add_u32 s87, s78, 24576
buffer_load_dwordx4 a[156:159], v208, s[36:39], s87 offen offset:0 sc1
s_waitcnt vmcnt(10)
v_cmp_ne_u64 vcc, s[80:81], v[216:217]
s_nop 0
s_and_b64 vcc, vcc, 1048575
s_cbranch_scc0 .Lk_p3b_ok
s_mov_b32 s89, 0
.Lk_p3b:
v_mov_b64 v[216:217], 0
s_mov_b64 exec, 1048575
buffer_atomic_add_x2 v[216:217], v230, s[36:39], s76 offen sc0 sc1
s_mov_b64 exec, -1
s_waitcnt vmcnt(0)
v_cmp_ne_u64 vcc, s[80:81], v[216:217]
s_nop 0
s_and_b64 vcc, vcc, 1048575
s_cbranch_scc0 .Lk_p3b_ok
s_add_u32 s89, s89, 1
s_cmp_lt_u32 s89, 0x20000
s_cbranch_scc1 .Lk_p3b
.Lk_p3b_ok:
s_add_u32 s88, s96, 0
buffer_load_dwordx4 a[140:143], v221, s[36:39], s88 offen sc1
s_add_u32 s88, s96, 4096
buffer_load_dwordx4 a[196:199], v221, s[36:39], s88 offen sc1
s_add_u32 s88, s96, 8192
buffer_load_dwordx4 a[200:203], v221, s[36:39], s88 offen sc1
s_add_u32 s88, s96, 12288
buffer_load_dwordx4 a[100:103], v221, s[36:39], s88 offen sc1
s_add_u32 s88, s96, 16384
buffer_load_dwordx4 a[192:195], v221, s[36:39], s88 offen sc1
s_add_u32 s88, s49, 20480
buffer_load_dwordx4 a[40:43], v221, s[36:39], s88 offen sc1
s_add_u32 s87, s79, 0
buffer_load_dwordx4 a[244:247], v208, s[36:39], s87 offen offset:0 sc1
buffer_load_dwordx4 a[104:107], v208, s[36:39], s87 offen offset:1024 sc1
v_mfma_f32_16x16x32_bf16 v[0:3], a[112:115], v[148:151], v[0:3]
v_mfma_f32_16x16x32_bf16 v[4:7], a[48:51], v[148:151], v[4:7]
v_mfma_f32_16x16x32_bf16 v[8:11], v[204:207], v[148:151], v[8:11]
v_mfma_f32_16x16x32_bf16 v[12:15], v[168:171], v[148:151], v[12:15]
v_mfma_f32_16x16x32_bf16 v[16:19], a[120:123], v[148:151], v[16:19]
v_mfma_f32_16x16x32_bf16 v[36:39], a[120:123], v[152:155], v[36:39]
buffer_load_dwordx4 v[112:115], v208, s[36:39], s87 offen offset:2048 sc1
v_mfma_f32_16x16x32_bf16 v[32:35], v[168:171], v[152:155], v[32:35]
buffer_load_dwordx4 a[240:243], v208, s[36:39], s87 offen offset:3072 sc1
v_mfma_f32_16x16x32_bf16 v[28:31], v[204:207], v[152:155], v[28:31]
s_add_u32 s87, s79, 4096
v_mfma_f32_16x16x32_bf16 v[24:27], a[48:51], v[152:155], v[24:27]
buffer_load_dwordx4 a[68:71], v208, s[36:39], s87 offen offset:0 sc1
v_mfma_f32_16x16x32_bf16 v[20:23], a[112:115], v[152:155], v[20:23]
s_add_u32 s88, s96, 0
buffer_load_dwordx4 a[20:23], v220, s[36:39], s88 offen sc1
v_mfma_f32_16x16x32_bf16 v[40:43], a[112:115], v[156:159], v[40:43]
v_mfma_f32_16x16x32_bf16 v[44:47], a[48:51], v[156:159], v[44:47]
v_mfma_f32_16x16x32_bf16 v[48:51], v[204:207], v[156:159], v[48:51]
v_mfma_f32_16x16x32_bf16 v[52:55], v[168:171], v[156:159], v[52:55]
s_add_u32 s88, s96, 4096
v_mfma_f32_16x16x32_bf16 v[56:59], a[120:123], v[156:159], v[56:59]
buffer_load_dwordx4 a[56:59], v220, s[36:39], s88 offen sc1
v_mfma_f32_16x16x32_bf16 v[76:79], a[120:123], v[84:87], v[76:79]
v_mfma_f32_16x16x32_bf16 v[72:75], v[168:171], v[84:87], v[72:75]
v_mfma_f32_16x16x32_bf16 v[68:71], v[204:207], v[84:87], v[68:71]
v_mfma_f32_16x16x32_bf16 v[64:67], a[48:51], v[84:87], v[64:67]
s_add_u32 s88, s96, 8192
v_mfma_f32_16x16x32_bf16 v[60:63], a[112:115], v[84:87], v[60:63]
buffer_load_dwordx4 a[172:175], v220, s[36:39], s88 offen sc1
v_mfma_f32_16x16x32_bf16 v[0:3], a[184:187], v[88:91], v[0:3]
v_mfma_f32_16x16x32_bf16 v[4:7], a[52:55], v[88:91], v[4:7]
v_mfma_f32_16x16x32_bf16 v[8:11], a[128:131], v[88:91], v[8:11]
v_mfma_f32_16x16x32_bf16 v[12:15], v[120:123], v[88:91], v[12:15]
s_add_u32 s88, s96, 12288
v_mfma_f32_16x16x32_bf16 v[16:19], a[12:15], v[88:91], v[16:19]
buffer_load_dwordx4 v[116:119], v220, s[36:39], s88 offen sc1
v_mfma_f32_16x16x32_bf16 v[36:39], a[12:15], v[92:95], v[36:39]
v_mfma_f32_16x16x32_bf16 v[32:35], v[120:123], v[92:95], v[32:35]
v_mfma_f32_16x16x32_bf16 v[28:31], a[128:131], v[92:95], v[28:31]
v_mfma_f32_16x16x32_bf16 v[24:27], a[52:55], v[92:95], v[24:27]
s_add_u32 s88, s96, 16384
v_mfma_f32_16x16x32_bf16 v[20:23], a[184:187], v[92:95], v[20:23]
buffer_load_dwordx4 a[92:95], v220, s[36:39], s88 offen sc1
v_mfma_f32_16x16x32_bf16 v[40:43], a[184:187], v[100:103], v[40:43]
v_mfma_f32_16x16x32_bf16 v[44:47], a[52:55], v[100:103], v[44:47]
v_mfma_f32_16x16x32_bf16 v[48:51], a[128:131], v[100:103], v[48:51]
v_mfma_f32_16x16x32_bf16 v[52:55], v[120:123], v[100:103], v[52:55]
s_add_u32 s88, s49, 20480
v_mfma_f32_16x16x32_bf16 v[56:59], a[12:15], v[100:103], v[56:59]
buffer_load_dwordx4 a[248:251], v220, s[36:39], s88 offen sc1
v_mfma_f32_16x16x32_bf16 v[76:79], a[12:15], v[104:107], v[76:79]
v_mfma_f32_16x16x32_bf16 v[72:75], v[120:123], v[104:107], v[72:75]
v_mfma_f32_16x16x32_bf16 v[68:71], a[128:131], v[104:107], v[68:71]
s_add_u32 s88, s96, 0
v_mfma_f32_16x16x32_bf16 v[64:67], a[52:55], v[104:107], v[64:67]
buffer_load_dwordx4 a[164:167], v219, s[36:39], s88 offen sc1
v_mfma_f32_16x16x32_bf16 v[60:63], a[184:187], v[104:107], v[60:63]
s_waitcnt vmcnt(17)
v_mfma_f32_16x16x32_bf16 v[60:63], a[152:155], a[140:143], v[60:63]
v_mfma_f32_16x16x32_bf16 v[64:67], a[144:147], a[140:143], v[64:67]
v_mfma_f32_16x16x32_bf16 v[68:71], v[160:163], a[140:143], v[68:71]
s_add_u32 s88, s96, 4096
v_mfma_f32_16x16x32_bf16 v[72:75], a[60:63], a[140:143], v[72:75]
buffer_load_dwordx4 v[200:203], v219, s[36:39], s88 offen sc1
v_mfma_f32_16x16x32_bf16 v[76:79], a[96:99], a[140:143], v[76:79]
s_waitcnt vmcnt(17)
v_mfma_f32_16x16x32_bf16 v[60:63], a[220:223], a[196:199], v[60:63]
v_mfma_f32_16x16x32_bf16 v[64:67], a[188:191], a[196:199], v[64:67]
v_mfma_f32_16x16x32_bf16 v[68:71], a[8:11], a[196:199], v[68:71]
s_add_u32 s88, s96, 8192
v_mfma_f32_16x16x32_bf16 v[72:75], a[208:211], a[196:199], v[72:75]
buffer_load_dwordx4 a[28:31], v219, s[36:39], s88 offen sc1
v_mfma_f32_16x16x32_bf16 v[76:79], a[232:235], a[196:199], v[76:79]
s_waitcnt vmcnt(17)
v_mfma_f32_16x16x32_bf16 v[60:63], a[16:19], a[200:203], v[60:63]
v_mfma_f32_16x16x32_bf16 v[64:67], v[184:187], a[200:203], v[64:67]
v_mfma_f32_16x16x32_bf16 v[68:71], a[0:3], a[200:203], v[68:71]
s_add_u32 s88, s96, 12288
v_mfma_f32_16x16x32_bf16 v[72:75], a[160:163], a[200:203], v[72:75]
buffer_load_dwordx4 v[188:191], v219, s[36:39], s88 offen sc1
v_mfma_f32_16x16x32_bf16 v[76:79], v[108:111], a[200:203], v[76:79]
s_waitcnt vmcnt(17)
v_mfma_f32_16x16x32_bf16 v[60:63], a[80:83], a[100:103], v[60:63]
v_mfma_f32_16x16x32_bf16 v[64:67], a[116:119], a[100:103], v[64:67]
v_mfma_f32_16x16x32_bf16 v[68:71], a[44:47], a[100:103], v[68:71]
s_add_u32 s88, s96, 16384
v_mfma_f32_16x16x32_bf16 v[72:75], a[168:171], a[100:103], v[72:75]
buffer_load_dwordx4 v[192:195], v219, s[36:39], s88 offen sc1
v_mfma_f32_16x16x32_bf16 v[76:79], a[148:151], a[100:103], v[76:79]
s_waitcnt vmcnt(17)
v_mfma_f32_16x16x32_bf16 v[60:63], v[164:167], a[192:195], v[60:63]
v_mfma_f32_16x16x32_bf16 v[64:67], a[176:179], a[192:195], v[64:67]
v_mfma_f32_16x16x32_bf16 v[68:71], a[228:231], a[192:195], v[68:71]
s_add_u32 s88, s49, 20480
v_mfma_f32_16x16x32_bf16 v[72:75], a[84:87], a[192:195], v[72:75]
buffer_load_dwordx4 a[72:75], v219, s[36:39], s88 offen sc1
v_mfma_f32_16x16x32_bf16 v[76:79], a[156:159], a[192:195], v[76:79]
s_waitcnt vmcnt(12)
v_mfma_f32_16x16x32_bf16 v[60:63], a[244:247], a[40:43], v[60:63]
v_mfma_f32_16x16x32_bf16 v[64:67], a[104:107], a[40:43], v[64:67]
v_mfma_f32_16x16x32_bf16 v[68:71], v[112:115], a[40:43], v[68:71]
s_add_u32 s88, s96, 0
v_mfma_f32_16x16x32_bf16 v[72:75], a[240:243], a[40:43], v[72:75]
buffer_load_dwordx4 a[180:183], v218, s[36:39], s88 offen sc1
v_mfma_f32_16x16x32_bf16 v[76:79], a[68:71], a[40:43], v[76:79]
s_waitcnt vmcnt(12)
v_mfma_f32_16x16x32_bf16 v[40:43], a[152:155], a[20:23], v[40:43]
v_mfma_f32_16x16x32_bf16 v[44:47], a[144:147], a[20:23], v[44:47]
v_mfma_f32_16x16x32_bf16 v[48:51], v[160:163], a[20:23], v[48:51]
s_add_u32 s88, s96, 4096
v_mfma_f32_16x16x32_bf16 v[52:55], a[60:63], a[20:23], v[52:55]
buffer_load_dwordx4 v[172:175], v218, s[36:39], s88 offen sc1
v_mfma_f32_16x16x32_bf16 v[56:59], a[96:99], a[20:23], v[56:59]
s_waitcnt vmcnt(12)
v_mfma_f32_16x16x32_bf16 v[40:43], a[220:223], a[56:59], v[40:43]
ds_write_b128 v228, v[60:63] offset:10240
v_mfma_f32_16x16x32_bf16 v[44:47], a[188:191], a[56:59], v[44:47]
ds_write_b128 v228, v[64:67] offset:11264
v_mfma_f32_16x16x32_bf16 v[48:51], a[8:11], a[56:59], v[48:51]
s_add_u32 s88, s96, 8192
v_mfma_f32_16x16x32_bf16 v[52:55], a[208:211], a[56:59], v[52:55]
buffer_load_dwordx4 a[36:39], v218, s[36:39], s88 offen sc1
v_mfma_f32_16x16x32_bf16 v[56:59], a[232:235], a[56:59], v[56:59]
s_waitcnt vmcnt(12)
v_mfma_f32_16x16x32_bf16 v[40:43], a[16:19], a[172:175], v[40:43]
v_mfma_f32_16x16x32_bf16 v[44:47], v[184:187], a[172:175], v[44:47]
ds_write_b128 v228, v[68:71] offset:12288
v_mfma_f32_16x16x32_bf16 v[48:51], a[0:3], a[172:175], v[48:51]
s_add_u32 s88, s96, 12288
v_mfma_f32_16x16x32_bf16 v[52:55], a[160:163], a[172:175], v[52:55]
buffer_load_dwordx4 a[216:219], v218, s[36:39], s88 offen sc1
v_mfma_f32_16x16x32_bf16 v[56:59], v[108:111], a[172:175], v[56:59]
s_waitcnt vmcnt(12)
v_mfma_f32_16x16x32_bf16 v[40:43], a[80:83], v[116:119], v[40:43]
v_mfma_f32_16x16x32_bf16 v[44:47], a[116:119], v[116:119], v[44:47]
ds_write_b128 v228, v[72:75] offset:13312
v_mfma_f32_16x16x32_bf16 v[48:51], a[44:47], v[116:119], v[48:51]
s_add_u32 s88, s96, 16384
v_mfma_f32_16x16x32_bf16 v[52:55], a[168:171], v[116:119], v[52:55]
buffer_load_dwordx4 a[204:207], v218, s[36:39], s88 offen sc1
v_mfma_f32_16x16x32_bf16 v[56:59], a[148:151], v[116:119], v[56:59]
s_waitcnt vmcnt(12)
v_mfma_f32_16x16x32_bf16 v[40:43], v[164:167], a[92:95], v[40:43]
ds_write_b128 v228, v[76:79] offset:14336
v_mfma_f32_16x16x32_bf16 v[44:47], a[176:179], a[92:95], v[44:47]
v_mfma_f32_16x16x32_bf16 v[48:51], a[228:231], a[92:95], v[48:51]
s_add_u32 s88, s49, 20480
v_mfma_f32_16x16x32_bf16 v[52:55], a[84:87], a[92:95], v[52:55]
buffer_load_dwordx4 a[212:215], v218, s[36:39], s88 offen sc1
v_mfma_f32_16x16x32_bf16 v[56:59], a[156:159], a[92:95], v[56:59]
s_waitcnt vmcnt(12)
v_mfma_f32_16x16x32_bf16 v[40:43], a[244:247], a[248:251], v[40:43]
v_mfma_f32_16x16x32_bf16 v[44:47], a[104:107], a[248:251], v[44:47]
v_mfma_f32_16x16x32_bf16 v[48:51], v[112:115], a[248:251], v[48:51]
v_mfma_f32_16x16x32_bf16 v[52:55], a[240:243], a[248:251], v[52:55]
v_mfma_f32_16x16x32_bf16 v[56:59], a[68:71], a[248:251], v[56:59]
s_waitcnt vmcnt(11)
v_mfma_f32_16x16x32_bf16 v[20:23], a[152:155], a[164:167], v[20:23]
v_mfma_f32_16x16x32_bf16 v[24:27], a[144:147], a[164:167], v[24:27]
v_mfma_f32_16x16x32_bf16 v[28:31], v[160:163], a[164:167], v[28:31]
v_mfma_f32_16x16x32_bf16 v[32:35], a[60:63], a[164:167], v[32:35]
ds_write_b128 v228, v[40:43] offset:5120
v_mfma_f32_16x16x32_bf16 v[36:39], a[96:99], a[164:167], v[36:39]
s_waitcnt vmcnt(10)
v_mfma_f32_16x16x32_bf16 v[20:23], a[220:223], v[200:203], v[20:23]
v_mfma_f32_16x16x32_bf16 v[24:27], a[188:191], v[200:203], v[24:27]
v_mfma_f32_16x16x32_bf16 v[28:31], a[8:11], v[200:203], v[28:31]
ds_write_b128 v228, v[44:47] offset:6144
v_mfma_f32_16x16x32_bf16 v[32:35], a[208:211], v[200:203], v[32:35]
v_mfma_f32_16x16x32_bf16 v[36:39], a[232:235], v[200:203], v[36:39]
s_waitcnt vmcnt(9)
v_mfma_f32_16x16x32_bf16 v[20:23], a[16:19], a[28:31], v[20:23]
ds_write_b128 v228, v[48:51] offset:7168
v_mfma_f32_16x16x32_bf16 v[24:27], v[184:187], a[28:31], v[24:27]
v_mfma_f32_16x16x32_bf16 v[28:31], a[0:3], a[28:31], v[28:31]
v_mfma_f32_16x16x32_bf16 v[32:35], a[160:163], a[28:31], v[32:35]
ds_write_b128 v228, v[52:55] offset:8192
v_mfma_f32_16x16x32_bf16 v[36:39], v[108:111], a[28:31], v[36:39]
s_waitcnt vmcnt(8)
v_mfma_f32_16x16x32_bf16 v[20:23], a[80:83], v[188:191], v[20:23]
v_mfma_f32_16x16x32_bf16 v[24:27], a[116:119], v[188:191], v[24:27]
ds_write_b128 v228, v[56:59] offset:9216
v_mfma_f32_16x16x32_bf16 v[28:31], a[44:47], v[188:191], v[28:31]
v_mfma_f32_16x16x32_bf16 v[32:35], a[168:171], v[188:191], v[32:35]
v_mfma_f32_16x16x32_bf16 v[36:39], a[148:151], v[188:191], v[36:39]
s_waitcnt vmcnt(7)
v_mfma_f32_16x16x32_bf16 v[20:23], v[164:167], v[192:195], v[20:23]
v_mfma_f32_16x16x32_bf16 v[24:27], a[176:179], v[192:195], v[24:27]
v_mfma_f32_16x16x32_bf16 v[28:31], a[228:231], v[192:195], v[28:31]
v_mfma_f32_16x16x32_bf16 v[32:35], a[84:87], v[192:195], v[32:35]
v_mfma_f32_16x16x32_bf16 v[36:39], a[156:159], v[192:195], v[36:39]
s_waitcnt vmcnt(6)
v_mfma_f32_16x16x32_bf16 v[20:23], a[244:247], a[72:75], v[20:23]
v_mfma_f32_16x16x32_bf16 v[24:27], a[104:107], a[72:75], v[24:27]
v_mfma_f32_16x16x32_bf16 v[28:31], v[112:115], a[72:75], v[28:31]
v_mfma_f32_16x16x32_bf16 v[32:35], a[240:243], a[72:75], v[32:35]
v_mfma_f32_16x16x32_bf16 v[36:39], a[68:71], a[72:75], v[36:39]
s_waitcnt vmcnt(5)
v_mfma_f32_16x16x32_bf16 v[0:3], a[152:155], a[180:183], v[0:3]
v_mfma_f32_16x16x32_bf16 v[4:7], a[144:147], a[180:183], v[4:7]
v_mfma_f32_16x16x32_bf16 v[8:11], v[160:163], a[180:183], v[8:11]
v_mfma_f32_16x16x32_bf16 v[12:15], a[60:63], a[180:183], v[12:15]
ds_write_b128 v228, v[20:23] offset:0
v_mfma_f32_16x16x32_bf16 v[16:19], a[96:99], a[180:183], v[16:19]
s_waitcnt vmcnt(4)
v_mfma_f32_16x16x32_bf16 v[0:3], a[220:223], v[172:175], v[0:3]
v_mfma_f32_16x16x32_bf16 v[4:7], a[188:191], v[172:175], v[4:7]
v_mfma_f32_16x16x32_bf16 v[8:11], a[8:11], v[172:175], v[8:11]
ds_write_b128 v228, v[24:27] offset:1024
v_mfma_f32_16x16x32_bf16 v[12:15], a[208:211], v[172:175], v[12:15]
v_mfma_f32_16x16x32_bf16 v[16:19], a[232:235], v[172:175], v[16:19]
s_waitcnt vmcnt(3)
v_mfma_f32_16x16x32_bf16 v[0:3], a[16:19], a[36:39], v[0:3]
ds_write_b128 v228, v[28:31] offset:2048
v_mfma_f32_16x16x32_bf16 v[4:7], v[184:187], a[36:39], v[4:7]
v_mfma_f32_16x16x32_bf16 v[8:11], a[0:3], a[36:39], v[8:11]
v_mfma_f32_16x16x32_bf16 v[12:15], a[160:163], a[36:39], v[12:15]
ds_write_b128 v228, v[32:35] offset:3072
v_mfma_f32_16x16x32_bf16 v[16:19], v[108:111], a[36:39], v[16:19]
s_waitcnt vmcnt(2)
v_mfma_f32_16x16x32_bf16 v[0:3], a[80:83], a[216:219], v[0:3]
v_mfma_f32_16x16x32_bf16 v[4:7], a[116:119], a[216:219], v[4:7]
ds_write_b128 v228, v[36:39] offset:4096
v_mfma_f32_16x16x32_bf16 v[8:11], a[44:47], a[216:219], v[8:11]
v_mfma_f32_16x16x32_bf16 v[12:15], a[168:171], a[216:219], v[12:15]
v_mfma_f32_16x16x32_bf16 v[16:19], a[148:151], a[216:219], v[16:19]
s_waitcnt vmcnt(1)
v_mfma_f32_16x16x32_bf16 v[0:3], v[164:167], a[204:207], v[0:3]
v_mfma_f32_16x16x32_bf16 v[4:7], a[176:179], a[204:207], v[4:7]
v_mfma_f32_16x16x32_bf16 v[8:11], a[228:231], a[204:207], v[8:11]
v_mfma_f32_16x16x32_bf16 v[12:15], a[84:87], a[204:207], v[12:15]
v_mfma_f32_16x16x32_bf16 v[16:19], a[156:159], a[204:207], v[16:19]
s_waitcnt vmcnt(0)
v_mfma_f32_16x16x32_bf16 v[0:3], a[244:247], a[212:215], v[0:3]
v_mfma_f32_16x16x32_bf16 v[4:7], a[104:107], a[212:215], v[4:7]
v_mfma_f32_16x16x32_bf16 v[8:11], v[112:115], a[212:215], v[8:11]
v_mfma_f32_16x16x32_bf16 v[12:15], a[240:243], a[212:215], v[12:15]
v_mfma_f32_16x16x32_bf16 v[16:19], a[68:71], a[212:215], v[16:19]
s_waitcnt lgkmcnt(0)
s_barrier
ds_read_b128 v[20:23], v229 offset:0
ds_read_b128 v[40:43], v234 offset:5120
ds_read_b128 v[60:63], v235 offset:10240
ds_read_b128 v[24:27], v229 offset:1024
ds_read_b128 v[44:47], v234 offset:6144
ds_read_b128 v[64:67], v235 offset:11264
ds_read_b128 v[28:31], v229 offset:2048
ds_read_b128 v[48:51], v234 offset:7168
ds_read_b128 v[68:71], v235 offset:12288
ds_read_b128 v[32:35], v229 offset:3072
ds_read_b128 v[52:55], v234 offset:8192
ds_read_b128 v[72:75], v235 offset:13312
ds_read_b128 v[36:39], v229 offset:4096
ds_read_b128 v[56:59], v234 offset:9216
ds_read_b128 v[76:79], v235 offset:14336
s_waitcnt lgkmcnt(14)
v_pk_add_f32 v[0:1], v[0:1], v[20:21]
s_waitcnt lgkmcnt(12)
v_pk_add_f32 v[40:41], v[40:41], v[60:61]
v_pk_add_f32 v[0:1], v[0:1], v[40:41]
v_pk_add_f32 v[2:3], v[2:3], v[22:23]
v_pk_add_f32 v[42:43], v[42:43], v[62:63]
v_pk_add_f32 v[2:3], v[2:3], v[42:43]
s_waitcnt lgkmcnt(11)
v_pk_add_f32 v[4:5], v[4:5], v[24:25]
s_waitcnt lgkmcnt(9)
v_pk_add_f32 v[44:45], v[44:45], v[64:65]
v_pk_add_f32 v[4:5], v[4:5], v[44:45]
v_pk_add_f32 v[6:7], v[6:7], v[26:27]
v_pk_add_f32 v[46:47], v[46:47], v[66:67]
v_pk_add_f32 v[6:7], v[6:7], v[46:47]
s_waitcnt lgkmcnt(8)
v_pk_add_f32 v[8:9], v[8:9], v[28:29]
s_waitcnt lgkmcnt(6)
v_pk_add_f32 v[48:49], v[48:49], v[68:69]
v_pk_add_f32 v[8:9], v[8:9], v[48:49]
v_pk_add_f32 v[10:11], v[10:11], v[30:31]
v_pk_add_f32 v[50:51], v[50:51], v[70:71]
v_pk_add_f32 v[10:11], v[10:11], v[50:51]
s_waitcnt lgkmcnt(5)
v_pk_add_f32 v[12:13], v[12:13], v[32:33]
s_waitcnt lgkmcnt(3)
v_pk_add_f32 v[52:53], v[52:53], v[72:73]
v_pk_add_f32 v[12:13], v[12:13], v[52:53]
v_pk_add_f32 v[14:15], v[14:15], v[34:35]
v_pk_add_f32 v[54:55], v[54:55], v[74:75]
v_pk_add_f32 v[14:15], v[14:15], v[54:55]
s_waitcnt lgkmcnt(2)
v_pk_add_f32 v[16:17], v[16:17], v[36:37]
s_waitcnt lgkmcnt(0)
v_pk_add_f32 v[56:57], v[56:57], v[76:77]
v_pk_add_f32 v[16:17], v[16:17], v[56:57]
v_pk_add_f32 v[18:19], v[18:19], v[38:39]
v_pk_add_f32 v[58:59], v[58:59], v[78:79]
v_pk_add_f32 v[18:19], v[18:19], v[58:59]
v_pk_fma_f32 v[236:237], v[128:129], v[0:1], v[236:237]
v_pk_fma_f32 v[238:239], v[130:131], v[2:3], v[238:239]
v_pk_fma_f32 v[240:241], v[132:133], v[4:5], v[240:241]
v_pk_fma_f32 v[242:243], v[134:135], v[6:7], v[242:243]
v_pk_fma_f32 v[244:245], v[136:137], v[8:9], v[244:245]
v_pk_fma_f32 v[246:247], v[138:139], v[10:11], v[246:247]
v_pk_fma_f32 v[248:249], v[140:141], v[12:13], v[248:249]
v_pk_fma_f32 v[250:251], v[142:143], v[14:15], v[250:251]
v_pk_fma_f32 v[252:253], v[144:145], v[16:17], v[252:253]
v_pk_fma_f32 v[254:255], v[146:147], v[18:19], v[254:255]
v_cvt_pk_bf16_f32 v0, v236, v237
v_cvt_pk_bf16_f32 v1, v238, v239
v_cvt_pk_bf16_f32 v2, v240, v241
v_cvt_pk_bf16_f32 v3, v242, v243
v_cvt_pk_bf16_f32 v4, v244, v245
v_cvt_pk_bf16_f32 v5, v246, v247
v_cvt_pk_bf16_f32 v6, v248, v249
v_cvt_pk_bf16_f32 v7, v250, v251
v_cvt_pk_bf16_f32 v8, v252, v253
v_cvt_pk_bf16_f32 v9, v254, v255
buffer_store_dwordx4 v[0:3], v222, s[60:63], s86 offen
buffer_store_dwordx4 v[4:7], v222, s[60:63], s86 offen offset:64
buffer_store_dwordx2 v[8:9], v223, s[60:63], s86 offen offset:128
s_branch .Lk_end
.Lk_fb:
s_waitcnt vmcnt(0) lgkmcnt(0)
s_nop 15
s_mov_b64 s[48:49], s[10:11]
s_and_b32 s49, s49, 0xffff
s_mov_b32 s50, 16777216
s_mov_b32 s51, 0x20000
s_mov_b64 s[52:53], s[12:13]
s_and_b32 s53, s53, 0xffff
s_mov_b32 s54, 16777216
s_mov_b32 s55, 0x20000
v_and_b32 v216, 15, v209
v_lshrrev_b32 v217, 4, v209
v_lshlrev_b32 v219, 8, v216
v_lshl_add_u32 v219, v217, 4, v219
v_lshlrev_b32 v220, 1, v216
v_lshl_add_u32 v220, v217, 10, v220
v_mov_b32 v222, 0xff800000
v_mov_b32 v223, 0xff800000
v_mov_b32 v224, 0xff800000
v_mov_b32 v225, 0xff800000
s_lshl_b32 s72, s67, 19
s_add_u32 s72, s72, 393216
s_mov_b32 s74, 0
.Lk_fb1:
s_add_u32 s75, s72, 4096
buffer_load_dwordx4 a[64:67], v219, s[48:51], s72 offen offset:0
buffer_load_dwordx4 a[68:71], v219, s[48:51], s75 offen offset:0
buffer_load_dwordx4 a[72:75], v219, s[48:51], s72 offen offset:64
buffer_load_dwordx4 a[76:79], v219, s[48:51], s75 offen offset:64
buffer_load_dwordx4 a[80:83], v219, s[48:51], s72 offen offset:128
buffer_load_dwordx4 a[84:87], v219, s[48:51], s75 offen offset:128
buffer_load_dwordx4 a[88:91], v219, s[48:51], s72 offen offset:192
buffer_load_dwordx4 a[92:95], v219, s[48:51], s75 offen offset:192
s_waitcnt vmcnt(7)
v_mfma_f32_16x16x32_bf16 v[128:131], a[64:67], a[0:3], 0
v_mfma_f32_16x16x32_bf16 v[136:139], a[64:67], a[16:19], 0
s_waitcnt vmcnt(6)
v_mfma_f32_16x16x32_bf16 v[140:143], a[68:71], a[16:19], 0
v_mfma_f32_16x16x32_bf16 v[132:135], a[68:71], a[0:3], 0
s_waitcnt vmcnt(5)
v_mfma_f32_16x16x32_bf16 v[128:131], a[72:75], a[4:7], v[128:131]
v_mfma_f32_16x16x32_bf16 v[136:139], a[72:75], a[20:23], v[136:139]
s_waitcnt vmcnt(4)
v_mfma_f32_16x16x32_bf16 v[140:143], a[76:79], a[20:23], v[140:143]
v_mfma_f32_16x16x32_bf16 v[132:135], a[76:79], a[4:7], v[132:135]
s_waitcnt vmcnt(3)
v_mfma_f32_16x16x32_bf16 v[128:131], a[80:83], a[8:11], v[128:131]
v_mfma_f32_16x16x32_bf16 v[136:139], a[80:83], a[24:27], v[136:139]
s_waitcnt vmcnt(2)
v_mfma_f32_16x16x32_bf16 v[140:143], a[84:87], a[24:27], v[140:143]
v_mfma_f32_16x16x32_bf16 v[132:135], a[84:87], a[8:11], v[132:135]
s_waitcnt vmcnt(1)
v_mfma_f32_16x16x32_bf16 v[128:131], a[88:91], a[12:15], v[128:131]
v_mfma_f32_16x16x32_bf16 v[136:139], a[88:91], a[28:31], v[136:139]
s_waitcnt vmcnt(0)
v_mfma_f32_16x16x32_bf16 v[140:143], a[92:95], a[28:31], v[140:143]
v_mfma_f32_16x16x32_bf16 v[132:135], a[92:95], a[12:15], v[132:135]
v_mfma_f32_16x16x32_bf16 v[144:147], a[64:67], a[32:35], 0
v_mfma_f32_16x16x32_bf16 v[152:155], a[64:67], a[48:51], 0
v_mfma_f32_16x16x32_bf16 v[156:159], a[68:71], a[48:51], 0
v_mfma_f32_16x16x32_bf16 v[148:151], a[68:71], a[32:35], 0
v_mfma_f32_16x16x32_bf16 v[144:147], a[72:75], a[36:39], v[144:147]
v_mfma_f32_16x16x32_bf16 v[152:155], a[72:75], a[52:55], v[152:155]
v_mfma_f32_16x16x32_bf16 v[156:159], a[76:79], a[52:55], v[156:159]
v_mfma_f32_16x16x32_bf16 v[148:151], a[76:79], a[36:39], v[148:151]
v_mfma_f32_16x16x32_bf16 v[144:147], a[80:83], a[40:43], v[144:147]
v_mfma_f32_16x16x32_bf16 v[152:155], a[80:83], a[56:59], v[152:155]
v_mfma_f32_16x16x32_bf16 v[156:159], a[84:87], a[56:59], v[156:159]
v_mfma_f32_16x16x32_bf16 v[148:151], a[84:87], a[40:43], v[148:151]
v_mfma_f32_16x16x32_bf16 v[144:147], a[88:91], a[44:47], v[144:147]
v_mfma_f32_16x16x32_bf16 v[152:155], a[88:91], a[60:63], v[152:155]
v_mfma_f32_16x16x32_bf16 v[156:159], a[92:95], a[60:63], v[156:159]
v_mfma_f32_16x16x32_bf16 v[148:151], a[92:95], a[44:47], v[148:151]
v_max3_f32 v222, v222, v128, v129
v_max3_f32 v222, v222, v130, v131
v_max3_f32 v222, v222, v132, v133
v_max3_f32 v222, v222, v134, v135
v_max3_f32 v223, v223, v136, v137
v_max3_f32 v223, v223, v138, v139
v_max3_f32 v223, v223, v140, v141
v_max3_f32 v223, v223, v142, v143
v_max3_f32 v224, v224, v144, v145
v_max3_f32 v224, v224, v146, v147
v_max3_f32 v224, v224, v148, v149
v_max3_f32 v224, v224, v150, v151
v_max3_f32 v225, v225, v152, v153
v_max3_f32 v225, v225, v154, v155
v_max3_f32 v225, v225, v156, v157
v_max3_f32 v225, v225, v158, v159
s_add_u32 s72, s72, 8192
s_add_u32 s74, s74, 1
s_cmp_lt_u32 s74, 16
s_cbranch_scc1 .Lk_fb1
v_mov_b32 v216, v222
s_nop 1
v_permlane16_swap_b32 v222, v216
v_max_f32 v222, v222, v216
v_mov_b32 v216, v222
s_nop 1
v_permlane32_swap_b32 v222, v216
v_max_f32 v222, v222, v216
v_mov_b32 v216, v223
s_nop 1
v_permlane16_swap_b32 v223, v216
v_max_f32 v223, v223, v216
v_mov_b32 v216, v223
s_nop 1
v_permlane32_swap_b32 v223, v216
v_max_f32 v223, v223, v216
v_mov_b32 v216, v224
s_nop 1
v_permlane16_swap_b32 v224, v216
v_max_f32 v224, v224, v216
v_mov_b32 v216, v224
s_nop 1
v_permlane32_swap_b32 v224, v216
v_max_f32 v224, v224, v216
v_mov_b32 v216, v225
s_nop 1
v_permlane16_swap_b32 v225, v216
v_max_f32 v225, v225, v216
v_mov_b32 v216, v225
s_nop 1
v_permlane32_swap_b32 v225, v216
v_max_f32 v225, v225, v216
v_mov_b64 v[0:1], 0
v_mov_b64 v[2:3], 0
v_mov_b64 v[4:5], 0
v_mov_b64 v[6:7], 0
v_mov_b64 v[8:9], 0
v_mov_b64 v[10:11], 0
v_mov_b64 v[12:13], 0
v_mov_b64 v[14:15], 0
v_mov_b64 v[16:17], 0
v_mov_b64 v[18:19], 0
v_mov_b64 v[20:21], 0
v_mov_b64 v[22:23], 0
v_mov_b64 v[24:25], 0
v_mov_b64 v[26:27], 0
v_mov_b64 v[28:29], 0
v_mov_b64 v[30:31], 0
v_mov_b64 v[32:33], 0
v_mov_b64 v[34:35], 0
v_mov_b64 v[36:37], 0
v_mov_b64 v[38:39], 0
v_mov_b64 v[40:41], 0
v_mov_b64 v[42:43], 0
v_mov_b64 v[44:45], 0
v_mov_b64 v[46:47], 0
v_mov_b64 v[48:49], 0
v_mov_b64 v[50:51], 0
v_mov_b64 v[52:53], 0
v_mov_b64 v[54:55], 0
v_mov_b64 v[56:57], 0
v_mov_b64 v[58:59], 0
v_mov_b64 v[60:61], 0
v_mov_b64 v[62:63], 0
v_mov_b64 v[64:65], 0
v_mov_b64 v[66:67], 0
v_mov_b64 v[68:69], 0
v_mov_b64 v[70:71], 0
v_mov_b64 v[72:73], 0
v_mov_b64 v[74:75], 0
v_mov_b64 v[76:77], 0
v_mov_b64 v[78:79], 0
v_mov_b64 v[80:81], 0
v_mov_b64 v[82:83], 0
v_mov_b64 v[84:85], 0
v_mov_b64 v[86:87], 0
v_mov_b64 v[88:89], 0
v_mov_b64 v[90:91], 0
v_mov_b64 v[92:93], 0
v_mov_b64 v[94:95], 0
v_mov_b64 v[96:97], 0
v_mov_b64 v[98:99], 0
v_mov_b64 v[100:101], 0
v_mov_b64 v[102:103], 0
v_mov_b64 v[104:105], 0
v_mov_b64 v[106:107], 0
v_mov_b64 v[108:109], 0
v_mov_b64 v[110:111], 0
v_mov_b64 v[112:113], 0
v_mov_b64 v[114:115], 0
v_mov_b64 v[116:117], 0
v_mov_b64 v[118:119], 0
v_mov_b64 v[120:121], 0
v_mov_b64 v[122:123], 0
v_mov_b64 v[124:125], 0
v_mov_b64 v[126:127], 0
v_mov_b64 v[192:193], 0
v_mov_b64 v[194:195], 0
v_mov_b64 v[196:197], 0
v_mov_b64 v[198:199], 0
v_mov_b64 v[200:201], 0
v_mov_b64 v[202:203], 0
v_mov_b64 v[204:205], 0
v_mov_b64 v[206:207], 0
s_lshl_b32 s72, s67, 19
s_add_u32 s72, s72, 393216
s_mov_b32 s74, 0
.Lk_fb2:
s_add_u32 s75, s72, 4096
buffer_load_dwordx4 a[64:67], v219, s[48:51], s72 offen offset:0
buffer_load_dwordx4 a[68:71], v219, s[48:51], s75 offen offset:0
buffer_load_dwordx4 a[72:75], v219, s[48:51], s72 offen offset:64
buffer_load_dwordx4 a[76:79], v219, s[48:51], s75 offen offset:64
buffer_load_dwordx4 a[80:83], v219, s[48:51], s72 offen offset:128
buffer_load_dwordx4 a[84:87], v219, s[48:51], s75 offen offset:128
buffer_load_dwordx4 a[88:91], v219, s[48:51], s72 offen offset:192
buffer_load_dwordx4 a[92:95], v219, s[48:51], s75 offen offset:192
s_waitcnt vmcnt(7)
v_mfma_f32_16x16x32_bf16 v[128:131], a[64:67], a[0:3], 0
v_mfma_f32_16x16x32_bf16 v[136:139], a[64:67], a[16:19], 0
s_waitcnt vmcnt(6)
v_mfma_f32_16x16x32_bf16 v[140:143], a[68:71], a[16:19], 0
v_mfma_f32_16x16x32_bf16 v[132:135], a[68:71], a[0:3], 0
s_waitcnt vmcnt(5)
v_mfma_f32_16x16x32_bf16 v[128:131], a[72:75], a[4:7], v[128:131]
v_mfma_f32_16x16x32_bf16 v[136:139], a[72:75], a[20:23], v[136:139]
s_waitcnt vmcnt(4)
v_mfma_f32_16x16x32_bf16 v[140:143], a[76:79], a[20:23], v[140:143]
v_mfma_f32_16x16x32_bf16 v[132:135], a[76:79], a[4:7], v[132:135]
s_waitcnt vmcnt(3)
v_mfma_f32_16x16x32_bf16 v[128:131], a[80:83], a[8:11], v[128:131]
v_mfma_f32_16x16x32_bf16 v[136:139], a[80:83], a[24:27], v[136:139]
s_waitcnt vmcnt(2)
v_mfma_f32_16x16x32_bf16 v[140:143], a[84:87], a[24:27], v[140:143]
v_mfma_f32_16x16x32_bf16 v[132:135], a[84:87], a[8:11], v[132:135]
s_waitcnt vmcnt(1)
v_mfma_f32_16x16x32_bf16 v[128:131], a[88:91], a[12:15], v[128:131]
v_mfma_f32_16x16x32_bf16 v[136:139], a[88:91], a[28:31], v[136:139]
s_waitcnt vmcnt(0)
v_mfma_f32_16x16x32_bf16 v[140:143], a[92:95], a[28:31], v[140:143]
v_mfma_f32_16x16x32_bf16 v[132:135], a[92:95], a[12:15], v[132:135]
v_mfma_f32_16x16x32_bf16 v[144:147], a[64:67], a[32:35], 0
v_mfma_f32_16x16x32_bf16 v[152:155], a[64:67], a[48:51], 0
v_mfma_f32_16x16x32_bf16 v[156:159], a[68:71], a[48:51], 0
v_mfma_f32_16x16x32_bf16 v[148:151], a[68:71], a[32:35], 0
v_mfma_f32_16x16x32_bf16 v[144:147], a[72:75], a[36:39], v[144:147]
v_mfma_f32_16x16x32_bf16 v[152:155], a[72:75], a[52:55], v[152:155]
v_mfma_f32_16x16x32_bf16 v[156:159], a[76:79], a[52:55], v[156:159]
v_mfma_f32_16x16x32_bf16 v[148:151], a[76:79], a[36:39], v[148:151]
v_mfma_f32_16x16x32_bf16 v[144:147], a[80:83], a[40:43], v[144:147]
v_mfma_f32_16x16x32_bf16 v[152:155], a[80:83], a[56:59], v[152:155]
v_mfma_f32_16x16x32_bf16 v[156:159], a[84:87], a[56:59], v[156:159]
v_mfma_f32_16x16x32_bf16 v[148:151], a[84:87], a[40:43], v[148:151]
v_mfma_f32_16x16x32_bf16 v[144:147], a[88:91], a[44:47], v[144:147]
v_mfma_f32_16x16x32_bf16 v[152:155], a[88:91], a[60:63], v[152:155]
v_mfma_f32_16x16x32_bf16 v[156:159], a[92:95], a[60:63], v[156:159]
v_mfma_f32_16x16x32_bf16 v[148:151], a[92:95], a[44:47], v[148:151]
v_sub_f32 v128, v128, v222
v_exp_f32 v128, v128
v_sub_f32 v129, v129, v222
v_exp_f32 v129, v129
v_sub_f32 v130, v130, v222
v_exp_f32 v130, v130
v_sub_f32 v131, v131, v222
v_exp_f32 v131, v131
v_sub_f32 v132, v132, v222
v_exp_f32 v132, v132
v_sub_f32 v133, v133, v222
v_exp_f32 v133, v133
v_sub_f32 v134, v134, v222
v_exp_f32 v134, v134
v_sub_f32 v135, v135, v222
v_exp_f32 v135, v135
v_sub_f32 v136, v136, v223
v_exp_f32 v136, v136
v_sub_f32 v137, v137, v223
v_exp_f32 v137, v137
v_sub_f32 v138, v138, v223
v_exp_f32 v138, v138
v_sub_f32 v139, v139, v223
v_exp_f32 v139, v139
v_sub_f32 v140, v140, v223
v_exp_f32 v140, v140
v_sub_f32 v141, v141, v223
v_exp_f32 v141, v141
v_sub_f32 v142, v142, v223
v_exp_f32 v142, v142
v_sub_f32 v143, v143, v223
v_exp_f32 v143, v143
v_sub_f32 v144, v144, v224
v_exp_f32 v144, v144
v_sub_f32 v145, v145, v224
v_exp_f32 v145, v145
v_sub_f32 v146, v146, v224
v_exp_f32 v146, v146
v_sub_f32 v147, v147, v224
v_exp_f32 v147, v147
v_sub_f32 v148, v148, v224
v_exp_f32 v148, v148
v_sub_f32 v149, v149, v224
v_exp_f32 v149, v149
v_sub_f32 v150, v150, v224
v_exp_f32 v150, v150
v_sub_f32 v151, v151, v224
v_exp_f32 v151, v151
v_sub_f32 v152, v152, v225
v_exp_f32 v152, v152
v_sub_f32 v153, v153, v225
v_exp_f32 v153, v153
v_sub_f32 v154, v154, v225
v_exp_f32 v154, v154
v_sub_f32 v155, v155, v225
v_exp_f32 v155, v155
v_sub_f32 v156, v156, v225
v_exp_f32 v156, v156
v_sub_f32 v157, v157, v225
v_exp_f32 v157, v157
v_sub_f32 v158, v158, v225
v_exp_f32 v158, v158
v_sub_f32 v159, v159, v225
v_exp_f32 v159, v159
v_cvt_pk_bf16_f32 v160, v128, v129
v_cvt_pk_bf16_f32 v161, v130, v131
v_cvt_pk_bf16_f32 v162, v132, v133
v_cvt_pk_bf16_f32 v163, v134, v135
v_cvt_pk_bf16_f32 v164, v136, v137
v_cvt_pk_bf16_f32 v165, v138, v139
v_cvt_pk_bf16_f32 v166, v140, v141
v_cvt_pk_bf16_f32 v167, v142, v143
v_cvt_pk_bf16_f32 v168, v144, v145
v_cvt_pk_bf16_f32 v169, v146, v147
v_cvt_pk_bf16_f32 v170, v148, v149
v_cvt_pk_bf16_f32 v171, v150, v151
v_cvt_pk_bf16_f32 v172, v152, v153
v_cvt_pk_bf16_f32 v173, v154, v155
v_cvt_pk_bf16_f32 v174, v156, v157
v_cvt_pk_bf16_f32 v175, v158, v159
buffer_load_ushort v128, v220, s[52:55], s72 offen offset:0
buffer_load_ushort v129, v220, s[52:55], s72 offen offset:256
buffer_load_ushort v130, v220, s[52:55], s72 offen offset:512
buffer_load_ushort v131, v220, s[52:55], s72 offen offset:768
buffer_load_ushort v132, v220, s[52:55], s75 offen offset:0
buffer_load_ushort v133, v220, s[52:55], s75 offen offset:256
buffer_load_ushort v134, v220, s[52:55], s75 offen offset:512
buffer_load_ushort v135, v220, s[52:55], s75 offen offset:768
buffer_load_ushort v136, v220, s[52:55], s72 offen offset:32
buffer_load_ushort v137, v220, s[52:55], s72 offen offset:288
buffer_load_ushort v138, v220, s[52:55], s72 offen offset:544
buffer_load_ushort v139, v220, s[52:55], s72 offen offset:800
buffer_load_ushort v140, v220, s[52:55], s75 offen offset:32
buffer_load_ushort v141, v220, s[52:55], s75 offen offset:288
buffer_load_ushort v142, v220, s[52:55], s75 offen offset:544
buffer_load_ushort v143, v220, s[52:55], s75 offen offset:800
buffer_load_ushort v144, v220, s[52:55], s72 offen offset:64
buffer_load_ushort v145, v220, s[52:55], s72 offen offset:320
buffer_load_ushort v146, v220, s[52:55], s72 offen offset:576
buffer_load_ushort v147, v220, s[52:55], s72 offen offset:832
buffer_load_ushort v148, v220, s[52:55], s75 offen offset:64
buffer_load_ushort v149, v220, s[52:55], s75 offen offset:320
buffer_load_ushort v150, v220, s[52:55], s75 offen offset:576
buffer_load_ushort v151, v220, s[52:55], s75 offen offset:832
buffer_load_ushort v152, v220, s[52:55], s72 offen offset:96
buffer_load_ushort v153, v220, s[52:55], s72 offen offset:352
buffer_load_ushort v154, v220, s[52:55], s72 offen offset:608
buffer_load_ushort v155, v220, s[52:55], s72 offen offset:864
buffer_load_ushort v156, v220, s[52:55], s75 offen offset:96
buffer_load_ushort v157, v220, s[52:55], s75 offen offset:352
buffer_load_ushort v158, v220, s[52:55], s75 offen offset:608
buffer_load_ushort v159, v220, s[52:55], s75 offen offset:864
s_waitcnt vmcnt(30)
v_lshl_or_b32 v216, v129, 16, v128
v_accvgpr_write_b32 a96, v216
s_waitcnt vmcnt(28)
v_lshl_or_b32 v216, v131, 16, v130
v_accvgpr_write_b32 a97, v216
s_waitcnt vmcnt(26)
v_lshl_or_b32 v216, v133, 16, v132
v_accvgpr_write_b32 a98, v216
s_waitcnt vmcnt(24)
v_lshl_or_b32 v216, v135, 16, v134
v_accvgpr_write_b32 a99, v216
s_waitcnt vmcnt(22)
v_lshl_or_b32 v216, v137, 16, v136
v_accvgpr_write_b32 a100, v216
s_waitcnt vmcnt(20)
v_lshl_or_b32 v216, v139, 16, v138
v_accvgpr_write_b32 a101, v216
s_waitcnt vmcnt(18)
v_lshl_or_b32 v216, v141, 16, v140
v_accvgpr_write_b32 a102, v216
s_waitcnt vmcnt(16)
v_lshl_or_b32 v216, v143, 16, v142
v_accvgpr_write_b32 a103, v216
s_waitcnt vmcnt(14)
v_lshl_or_b32 v216, v145, 16, v144
v_accvgpr_write_b32 a104, v216
s_waitcnt vmcnt(12)
v_lshl_or_b32 v216, v147, 16, v146
v_accvgpr_write_b32 a105, v216
s_waitcnt vmcnt(10)
v_lshl_or_b32 v216, v149, 16, v148
v_accvgpr_write_b32 a106, v216
s_waitcnt vmcnt(8)
v_lshl_or_b32 v216, v151, 16, v150
v_accvgpr_write_b32 a107, v216
s_waitcnt vmcnt(6)
v_lshl_or_b32 v216, v153, 16, v152
v_accvgpr_write_b32 a108, v216
s_waitcnt vmcnt(4)
v_lshl_or_b32 v216, v155, 16, v154
v_accvgpr_write_b32 a109, v216
s_waitcnt vmcnt(2)
v_lshl_or_b32 v216, v157, 16, v156
v_accvgpr_write_b32 a110, v216
s_waitcnt vmcnt(0)
v_lshl_or_b32 v216, v159, 16, v158
v_accvgpr_write_b32 a111, v216
buffer_load_ushort v128, v220, s[52:55], s72 offen offset:128
buffer_load_ushort v129, v220, s[52:55], s72 offen offset:384
buffer_load_ushort v130, v220, s[52:55], s72 offen offset:640
buffer_load_ushort v131, v220, s[52:55], s72 offen offset:896
buffer_load_ushort v132, v220, s[52:55], s75 offen offset:128
buffer_load_ushort v133, v220, s[52:55], s75 offen offset:384
buffer_load_ushort v134, v220, s[52:55], s75 offen offset:640
buffer_load_ushort v135, v220, s[52:55], s75 offen offset:896
buffer_load_ushort v136, v220, s[52:55], s72 offen offset:160
buffer_load_ushort v137, v220, s[52:55], s72 offen offset:416
buffer_load_ushort v138, v220, s[52:55], s72 offen offset:672
buffer_load_ushort v139, v220, s[52:55], s72 offen offset:928
buffer_load_ushort v140, v220, s[52:55], s75 offen offset:160
buffer_load_ushort v141, v220, s[52:55], s75 offen offset:416
buffer_load_ushort v142, v220, s[52:55], s75 offen offset:672
buffer_load_ushort v143, v220, s[52:55], s75 offen offset:928
buffer_load_ushort v144, v220, s[52:55], s72 offen offset:192
buffer_load_ushort v145, v220, s[52:55], s72 offen offset:448
buffer_load_ushort v146, v220, s[52:55], s72 offen offset:704
buffer_load_ushort v147, v220, s[52:55], s72 offen offset:960
buffer_load_ushort v148, v220, s[52:55], s75 offen offset:192
buffer_load_ushort v149, v220, s[52:55], s75 offen offset:448
buffer_load_ushort v150, v220, s[52:55], s75 offen offset:704
buffer_load_ushort v151, v220, s[52:55], s75 offen offset:960
buffer_load_ushort v152, v220, s[52:55], s72 offen offset:224
buffer_load_ushort v153, v220, s[52:55], s72 offen offset:480
buffer_load_ushort v154, v220, s[52:55], s72 offen offset:736
buffer_load_ushort v155, v220, s[52:55], s72 offen offset:992
buffer_load_ushort v156, v220, s[52:55], s75 offen offset:224
buffer_load_ushort v157, v220, s[52:55], s75 offen offset:480
buffer_load_ushort v158, v220, s[52:55], s75 offen offset:736
buffer_load_ushort v159, v220, s[52:55], s75 offen offset:992
s_waitcnt vmcnt(30)
v_lshl_or_b32 v216, v129, 16, v128
v_accvgpr_write_b32 a112, v216
s_waitcnt vmcnt(28)
v_lshl_or_b32 v216, v131, 16, v130
v_accvgpr_write_b32 a113, v216
s_waitcnt vmcnt(26)
v_lshl_or_b32 v216, v133, 16, v132
v_accvgpr_write_b32 a114, v216
s_waitcnt vmcnt(24)
v_lshl_or_b32 v216, v135, 16, v134
v_accvgpr_write_b32 a115, v216
s_waitcnt vmcnt(22)
v_lshl_or_b32 v216, v137, 16, v136
v_accvgpr_write_b32 a116, v216
s_waitcnt vmcnt(20)
v_lshl_or_b32 v216, v139, 16, v138
v_accvgpr_write_b32 a117, v216
s_waitcnt vmcnt(18)
v_lshl_or_b32 v216, v141, 16, v140
v_accvgpr_write_b32 a118, v216
s_waitcnt vmcnt(16)
v_lshl_or_b32 v216, v143, 16, v142
v_accvgpr_write_b32 a119, v216
s_waitcnt vmcnt(14)
v_lshl_or_b32 v216, v145, 16, v144
v_accvgpr_write_b32 a120, v216
s_waitcnt vmcnt(12)
v_lshl_or_b32 v216, v147, 16, v146
v_accvgpr_write_b32 a121, v216
s_waitcnt vmcnt(10)
v_lshl_or_b32 v216, v149, 16, v148
v_accvgpr_write_b32 a122, v216
s_waitcnt vmcnt(8)
v_lshl_or_b32 v216, v151, 16, v150
v_accvgpr_write_b32 a123, v216
s_waitcnt vmcnt(6)
v_lshl_or_b32 v216, v153, 16, v152
v_accvgpr_write_b32 a124, v216
s_waitcnt vmcnt(4)
v_lshl_or_b32 v216, v155, 16, v154
v_accvgpr_write_b32 a125, v216
s_waitcnt vmcnt(2)
v_lshl_or_b32 v216, v157, 16, v156
v_accvgpr_write_b32 a126, v216
s_waitcnt vmcnt(0)
v_lshl_or_b32 v216, v159, 16, v158
v_accvgpr_write_b32 a127, v216
v_mfma_f32_16x16x32_bf16 v[192:195], v[232:235], v[160:163], v[192:195]
v_mfma_f32_16x16x32_bf16 v[196:199], v[232:235], v[164:167], v[196:199]
v_mfma_f32_16x16x32_bf16 v[32:35], a[96:99], v[164:167], v[32:35]
v_mfma_f32_16x16x32_bf16 v[0:3], a[96:99], v[160:163], v[0:3]
v_mfma_f32_16x16x32_bf16 v[4:7], a[100:103], v[160:163], v[4:7]
v_mfma_f32_16x16x32_bf16 v[36:39], a[100:103], v[164:167], v[36:39]
v_mfma_f32_16x16x32_bf16 v[40:43], a[104:107], v[164:167], v[40:43]
v_mfma_f32_16x16x32_bf16 v[8:11], a[104:107], v[160:163], v[8:11]
v_mfma_f32_16x16x32_bf16 v[12:15], a[108:111], v[160:163], v[12:15]
v_mfma_f32_16x16x32_bf16 v[44:47], a[108:111], v[164:167], v[44:47]
v_mfma_f32_16x16x32_bf16 v[48:51], a[112:115], v[164:167], v[48:51]
v_mfma_f32_16x16x32_bf16 v[16:19], a[112:115], v[160:163], v[16:19]
v_mfma_f32_16x16x32_bf16 v[20:23], a[116:119], v[160:163], v[20:23]
v_mfma_f32_16x16x32_bf16 v[52:55], a[116:119], v[164:167], v[52:55]
v_mfma_f32_16x16x32_bf16 v[56:59], a[120:123], v[164:167], v[56:59]
v_mfma_f32_16x16x32_bf16 v[24:27], a[120:123], v[160:163], v[24:27]
v_mfma_f32_16x16x32_bf16 v[28:31], a[124:127], v[160:163], v[28:31]
v_mfma_f32_16x16x32_bf16 v[60:63], a[124:127], v[164:167], v[60:63]
v_mfma_f32_16x16x32_bf16 v[200:203], v[232:235], v[168:171], v[200:203]
v_mfma_f32_16x16x32_bf16 v[204:207], v[232:235], v[172:175], v[204:207]
v_mfma_f32_16x16x32_bf16 v[96:99], a[96:99], v[172:175], v[96:99]
v_mfma_f32_16x16x32_bf16 v[64:67], a[96:99], v[168:171], v[64:67]
v_mfma_f32_16x16x32_bf16 v[68:71], a[100:103], v[168:171], v[68:71]
v_mfma_f32_16x16x32_bf16 v[100:103], a[100:103], v[172:175], v[100:103]
v_mfma_f32_16x16x32_bf16 v[104:107], a[104:107], v[172:175], v[104:107]
v_mfma_f32_16x16x32_bf16 v[72:75], a[104:107], v[168:171], v[72:75]
v_mfma_f32_16x16x32_bf16 v[76:79], a[108:111], v[168:171], v[76:79]
v_mfma_f32_16x16x32_bf16 v[108:111], a[108:111], v[172:175], v[108:111]
v_mfma_f32_16x16x32_bf16 v[112:115], a[112:115], v[172:175], v[112:115]
v_mfma_f32_16x16x32_bf16 v[80:83], a[112:115], v[168:171], v[80:83]
v_mfma_f32_16x16x32_bf16 v[84:87], a[116:119], v[168:171], v[84:87]
v_mfma_f32_16x16x32_bf16 v[116:119], a[116:119], v[172:175], v[116:119]
v_mfma_f32_16x16x32_bf16 v[120:123], a[120:123], v[172:175], v[120:123]
v_mfma_f32_16x16x32_bf16 v[88:91], a[120:123], v[168:171], v[88:91]
v_mfma_f32_16x16x32_bf16 v[92:95], a[124:127], v[168:171], v[92:95]
v_mfma_f32_16x16x32_bf16 v[124:127], a[124:127], v[172:175], v[124:127]
s_add_u32 s72, s72, 8192
s_add_u32 s74, s74, 1
s_cmp_lt_u32 s74, 16
s_cbranch_scc1 .Lk_fb2
v_rcp_f32 v214, v192
v_rcp_f32 v215, v196
s_nop 0
v_pk_mul_f32 v[0:1], v[0:1], v[214:215] op_sel_hi:[1,0]
v_pk_mul_f32 v[2:3], v[2:3], v[214:215] op_sel_hi:[1,0]
v_pk_mul_f32 v[4:5], v[4:5], v[214:215] op_sel_hi:[1,0]
v_pk_mul_f32 v[6:7], v[6:7], v[214:215] op_sel_hi:[1,0]
v_cvt_pk_bf16_f32 v0, v0, v1
v_cvt_pk_bf16_f32 v1, v2, v3
v_cvt_pk_bf16_f32 v2, v4, v5
v_cvt_pk_bf16_f32 v3, v6, v7
s_xor_b32 s96, s73, 0
buffer_store_dwordx4 v[0:3], v208, s[36:39], s96 offen sc1
v_pk_mul_f32 v[8:9], v[8:9], v[214:215] op_sel_hi:[1,0]
v_pk_mul_f32 v[10:11], v[10:11], v[214:215] op_sel_hi:[1,0]
v_pk_mul_f32 v[12:13], v[12:13], v[214:215] op_sel_hi:[1,0]
v_pk_mul_f32 v[14:15], v[14:15], v[214:215] op_sel_hi:[1,0]
v_cvt_pk_bf16_f32 v8, v8, v9
v_cvt_pk_bf16_f32 v9, v10, v11
v_cvt_pk_bf16_f32 v10, v12, v13
v_cvt_pk_bf16_f32 v11, v14, v15
s_xor_b32 s96, s73, 4096
buffer_store_dwordx4 v[8:11], v208, s[36:39], s96 offen sc1
v_pk_mul_f32 v[16:17], v[16:17], v[214:215] op_sel_hi:[1,0]
v_pk_mul_f32 v[18:19], v[18:19], v[214:215] op_sel_hi:[1,0]
v_pk_mul_f32 v[20:21], v[20:21], v[214:215] op_sel_hi:[1,0]
v_pk_mul_f32 v[22:23], v[22:23], v[214:215] op_sel_hi:[1,0]
v_cvt_pk_bf16_f32 v16, v16, v17
v_cvt_pk_bf16_f32 v17, v18, v19
v_cvt_pk_bf16_f32 v18, v20, v21
v_cvt_pk_bf16_f32 v19, v22, v23
s_xor_b32 s96, s73, 8192
buffer_store_dwordx4 v[16:19], v208, s[36:39], s96 offen sc1
v_pk_mul_f32 v[24:25], v[24:25], v[214:215] op_sel_hi:[1,0]
v_pk_mul_f32 v[26:27], v[26:27], v[214:215] op_sel_hi:[1,0]
v_pk_mul_f32 v[28:29], v[28:29], v[214:215] op_sel_hi:[1,0]
v_pk_mul_f32 v[30:31], v[30:31], v[214:215] op_sel_hi:[1,0]
v_cvt_pk_bf16_f32 v24, v24, v25
v_cvt_pk_bf16_f32 v25, v26, v27
v_cvt_pk_bf16_f32 v26, v28, v29
v_cvt_pk_bf16_f32 v27, v30, v31
s_xor_b32 s96, s73, 12288
buffer_store_dwordx4 v[24:27], v208, s[36:39], s96 offen sc1
v_pk_mul_f32 v[32:33], v[32:33], v[214:215] op_sel:[0,1] op_sel_hi:[1,1]
v_pk_mul_f32 v[34:35], v[34:35], v[214:215] op_sel:[0,1] op_sel_hi:[1,1]
v_pk_mul_f32 v[36:37], v[36:37], v[214:215] op_sel:[0,1] op_sel_hi:[1,1]
v_pk_mul_f32 v[38:39], v[38:39], v[214:215] op_sel:[0,1] op_sel_hi:[1,1]
v_cvt_pk_bf16_f32 v32, v32, v33
v_cvt_pk_bf16_f32 v33, v34, v35
v_cvt_pk_bf16_f32 v34, v36, v37
v_cvt_pk_bf16_f32 v35, v38, v39
s_xor_b32 s96, s73, 1024
buffer_store_dwordx4 v[32:35], v208, s[36:39], s96 offen sc1
v_pk_mul_f32 v[40:41], v[40:41], v[214:215] op_sel:[0,1] op_sel_hi:[1,1]
v_pk_mul_f32 v[42:43], v[42:43], v[214:215] op_sel:[0,1] op_sel_hi:[1,1]
v_pk_mul_f32 v[44:45], v[44:45], v[214:215] op_sel:[0,1] op_sel_hi:[1,1]
v_pk_mul_f32 v[46:47], v[46:47], v[214:215] op_sel:[0,1] op_sel_hi:[1,1]
v_cvt_pk_bf16_f32 v40, v40, v41
v_cvt_pk_bf16_f32 v41, v42, v43
v_cvt_pk_bf16_f32 v42, v44, v45
v_cvt_pk_bf16_f32 v43, v46, v47
s_xor_b32 s96, s73, 5120
buffer_store_dwordx4 v[40:43], v208, s[36:39], s96 offen sc1
v_pk_mul_f32 v[48:49], v[48:49], v[214:215] op_sel:[0,1] op_sel_hi:[1,1]
v_pk_mul_f32 v[50:51], v[50:51], v[214:215] op_sel:[0,1] op_sel_hi:[1,1]
v_pk_mul_f32 v[52:53], v[52:53], v[214:215] op_sel:[0,1] op_sel_hi:[1,1]
v_pk_mul_f32 v[54:55], v[54:55], v[214:215] op_sel:[0,1] op_sel_hi:[1,1]
v_cvt_pk_bf16_f32 v48, v48, v49
v_cvt_pk_bf16_f32 v49, v50, v51
v_cvt_pk_bf16_f32 v50, v52, v53
v_cvt_pk_bf16_f32 v51, v54, v55
s_xor_b32 s96, s73, 9216
buffer_store_dwordx4 v[48:51], v208, s[36:39], s96 offen sc1
v_pk_mul_f32 v[56:57], v[56:57], v[214:215] op_sel:[0,1] op_sel_hi:[1,1]
v_pk_mul_f32 v[58:59], v[58:59], v[214:215] op_sel:[0,1] op_sel_hi:[1,1]
v_pk_mul_f32 v[60:61], v[60:61], v[214:215] op_sel:[0,1] op_sel_hi:[1,1]
v_pk_mul_f32 v[62:63], v[62:63], v[214:215] op_sel:[0,1] op_sel_hi:[1,1]
v_cvt_pk_bf16_f32 v56, v56, v57
v_cvt_pk_bf16_f32 v57, v58, v59
v_cvt_pk_bf16_f32 v58, v60, v61
v_cvt_pk_bf16_f32 v59, v62, v63
s_xor_b32 s96, s73, 13312
buffer_store_dwordx4 v[56:59], v208, s[36:39], s96 offen sc1
s_add_u32 s75, s85, 73728
buffer_load_dwordx4 a[64:67], v208, s[36:39], s75 offen offset:1024 sc1
s_add_u32 s75, s85, 73728
buffer_load_dwordx4 a[68:71], v208, s[36:39], s75 offen offset:2048 sc1
s_add_u32 s75, s85, 73728
buffer_load_dwordx4 a[72:75], v208, s[36:39], s75 offen offset:3072 sc1
s_add_u32 s75, s85, 77824
buffer_load_dwordx4 a[76:79], v208, s[36:39], s75 offen offset:0 sc1
s_add_u32 s75, s85, 77824
buffer_load_dwordx4 a[80:83], v208, s[36:39], s75 offen offset:1024 sc1
s_add_u32 s75, s85, 77824
buffer_load_dwordx4 a[84:87], v208, s[36:39], s75 offen offset:2048 sc1
s_add_u32 s75, s85, 77824
buffer_load_dwordx4 a[88:91], v208, s[36:39], s75 offen offset:3072 sc1
s_waitcnt vmcnt(0)
s_nop 15
s_branch .Lk_fbret
.Lk_end:
)ASM" :: [ka]"s"(ka),[wid]"s"(wid),[bid]"s"(bid),[lds]"s"(lb) : "v0","v1","v2","v3","v4","v5","v6","v7","v8","v9","v10","v11","v12","v13","v14","v15","v16","v17","v18","v19","v20","v21","v22","v23","v24","v25","v26","v27","v28","v29","v30","v31","v32","v33","v34","v35","v36","v37","v38","v39","v40","v41","v42","v43","v44","v45","v46","v47","v48","v49","v50","v51","v52","v53","v54","v55","v56","v57","v58","v59","v60","v61","v62","v63","v64","v65","v66","v67","v68","v69","v70","v71","v72","v73","v74","v75","v76","v77","v78","v79","v80","v81","v82","v83","v84","v85","v86","v87","v88","v89","v90","v91","v92","v93","v94","v95","v96","v97","v98","v99","v100","v101","v102","v103","v104","v105","v106","v107","v108","v109","v110","v111","v112","v113","v114","v115","v116","v117","v118","v119","v120","v121","v122","v123","v124","v125","v126","v127","v128","v129","v130","v131","v132","v133","v134","v135","v136","v137","v138","v139","v140","v141","v142","v143","v144","v145","v146","v147","v148","v149","v150","v151","v152","v153","v154","v155","v156","v157","v158","v159","v160","v161","v162","v163","v164","v165","v166","v167","v168","v169","v170","v171","v172","v173","v174","v175","v176","v177","v178","v179","v180","v181","v182","v183","v184","v185","v186","v187","v188","v189","v190","v191","v192","v193","v194","v195","v196","v197","v198","v199","v200","v201","v202","v203","v204","v205","v206","v207","v208","v209","v210","v211","v212","v213","v214","v215","v216","v217","v218","v219","v220","v221","v222","v223","v224","v225","v226","v227","v228","v229","v230","v231","v232","v233","v234","v235","v236","v237","v238","v239","v240","v241","v242","v243","v244","v245","v246","v247","v248","v249","v250","v251","v252","v253","v254","v255","a0","a1","a2","a3","a4","a5","a6","a7","a8","a9","a10","a11","a12","a13","a14","a15","a16","a17","a18","a19","a20","a21","a22","a23","a24","a25","a26","a27","a28","a29","a30","a31","a32","a33","a34","a35","a36","a37","a38","a39","a40","a41","a42","a43","a44","a45","a46","a47","a48","a49","a50","a51","a52","a53","a54","a55","a56","a57","a58","a59","a60","a61","a62","a63","a64","a65","a66","a67","a68","a69","a70","a71","a72","a73","a74","a75","a76","a77","a78","a79","a80","a81","a82","a83","a84","a85","a86","a87","a88","a89","a90","a91","a92","a93","a94","a95","a96","a97","a98","a99","a100","a101","a102","a103","a104","a105","a106","a107","a108","a109","a110","a111","a112","a113","a114","a115","a116","a117","a118","a119","a120","a121","a122","a123","a124","a125","a126","a127","a128","a129","a130","a131","a132","a133","a134","a135","a136","a137","a138","a139","a140","a141","a142","a143","a144","a145","a146","a147","a148","a149","a150","a151","a152","a153","a154","a155","a156","a157","a158","a159","a160","a161","a162","a163","a164","a165","a166","a167","a168","a169","a170","a171","a172","a173","a174","a175","a176","a177","a178","a179","a180","a181","a182","a183","a184","a185","a186","a187","a188","a189","a190","a191","a192","a193","a194","a195","a196","a197","a198","a199","a200","a201","a202","a203","a204","a205","a206","a207","a208","a209","a210","a211","a212","a213","a214","a215","a216","a217","a218","a219","a220","a221","a222","a223","a224","a225","a226","a227","a228","a229","a230","a231","a232","a233","a234","a235","a236","a237","a238","a239","a240","a241","a242","a243","a244","a245","a246","a247","a248","a249","a250","a251","a252","a253","a254","a255","s8","s9","s10","s11","s12","s13","s14","s15","s16","s17","s18","s19","s20","s21","s22","s23","s24","s25","s26","s27","s28","s29","s30","s31","s33","s34","s35","s36","s37","s38","s39","s40","s41","s42","s43","s44","s45","s46","s47","s48","s49","s50","s51","s52","s53","s54","s55","s56","s57","s58","s59","s60","s61","s62","s63","s64","s65","s66","s67","s68","s69","s70","s71","s72","s73","s74","s75","s76","s77","s78","s79","s80","s81","s82","s83","s84","s85","s86","s87","s88","s89","s90","s91","s92","s93","s94","s95","s96","s97","s98","s99","s100","s101","vcc","scc","memory");
}
extern "C" void kernel_launch(void* const* d_in, const int* in_sizes, int n_in, void* d_out, int out_size, void* d_ws, size_t ws_size, hipStream_t st) {
  k_main<<<256,256,0,st>>>((const u16*)d_in[0],(const u16*)d_in[1],(const u16*)d_in[2],(const u16*)d_in[3],(const u16*)d_in[4],(const u16*)d_in[6],(const u16*)d_in[7],
                           (const u16*)d_in[8],(const u16*)d_in[9],(const u16*)d_in[10],(char*)d_ws,(u16*)d_out);
}
```
